```python
import torch

def _softmax(x, axis=-1):
    return torch.softmax(x, dim=axis)

def _gelu(x):
    return torch.nn.functional.gelu(x, approximate='tanh')

def _silu(x):
    return torch.nn.functional.silu(x)

def _sigmoid(x):
    return torch.sigmoid(x)

def _layernorm(x, axis=-1, eps=1e-05):
    m = x.mean(dim=axis, keepdim=True)
    v = x.var(dim=axis, keepdim=True, unbiased=False)
    return (x - m) / torch.sqrt(v + eps)

def _scatter_add(tgt, idx, src, axis=0):
    return tgt.clone().index_add_(axis, idx, src)

def _segment_sum(src, seg, n):
    out = torch.zeros((n,) + src.shape[1:], dtype=src.dtype)
    return out.index_add_(0, seg, src)

def _pad(x, pw):
    flat = tuple((v for pair in reversed(pw) for v in pair))
    return torch.nn.functional.pad(x, flat)
kernel_name = 'adaLN_cond__sliding_block_cached__glu_gated_geglu__prompt'

def setup_inputs():

    _w = lambda a: (lambda t: t.to(torch.bfloat16) if t.is_floating_point() else t)(torch.as_tensor(a))

    def _A_setup():
        B, Sq, D = (1, 2048, 576)
        Hq, Hkv, Sk, Dh = (32, 16, 2048, 128)
        assert Hq % Hkv == 0, (Hq, Hkv)
        return {'x': _w((lambda *s: torch.randn(*s, dtype=torch.bfloat16) * 0.05)(B, Sq, D)), 'k_cache': _w((lambda *s: torch.randn(*s, dtype=torch.bfloat16) * 1.6)(B, Hkv, Sk, Dh)), 'v_cache': _w((lambda *s: torch.randn(*s, dtype=torch.bfloat16) * 0.8)(B, Hkv, Sk, Dh)), 'wq': _w((lambda *s: torch.randn(*s, dtype=torch.bfloat16) * 1.6)(D, Hq * Dh) * D ** (-0.5)), 'wo': _w((lambda *s: torch.randn(*s, dtype=torch.bfloat16) * 0.8)(Hq * Dh, D) * (Hq * Dh) ** (-0.5))}
    _A_in = _A_setup()

    def _B_setup():
        B, S, Dm, Df = (1, 2048, 576, 4608)
        return {'x': _w((lambda *s: torch.randn(*s, dtype=torch.bfloat16) * 0.05)(B, S, Dm)), 'w_gate': _w((lambda *s: torch.randn(*s, dtype=torch.bfloat16) * 0.8)(Dm, Df) * Dm ** (-0.5)), 'w_up': _w((lambda *s: torch.randn(*s, dtype=torch.bfloat16) * 0.8)(Dm, Df) * Dm ** (-0.5)), 'w_down': _w((lambda *s: torch.randn(*s, dtype=torch.bfloat16) * 0.8)(Df, Dm) * Df ** (-0.5))}
    _B_in = _B_setup()
    return {'A_x': _A_in['x'], 'A_k_cache': _A_in['k_cache'], 'A_v_cache': _A_in['v_cache'], 'A_wq': _A_in['wq'], 'A_wo': _A_in['wo'], 'B_x': _B_in['x'], 'B_w_gate': _B_in['w_gate'], 'B_w_up': _B_in['w_up'], 'B_w_down': _B_in['w_down'], 'c': _w((lambda *s: torch.randn(*s, dtype=torch.bfloat16) * 0.1)(1, 576) * 8.0), 'w_mod': _w((lambda *s: torch.randn(*s, dtype=torch.bfloat16) * 0.1)(576, 6 * 576) * 576 ** (-0.5) * 8.0)}

def reference(A_x, A_k_cache, A_v_cache, A_wq, A_wo, B_x, B_w_gate, B_w_up, B_w_down, c, w_mod):

    def _A_ref(x, k_cache, v_cache, wq, wo):
        B, Sq, D = x.shape
        Hq, Dh = (32, 128)
        Hkv, Sk = (k_cache.shape[1], k_cache.shape[2])
        W = min(4096, max(256, Sk // 4))
        rep = Hq // Hkv
        q = (lambda x, *d: x.permute(*d))((x @ wq).reshape(B, Sq, Hq, Dh), 0, 2, 1, 3)
        k = (lambda x, n, axis: torch.repeat_interleave(x, n, dim=axis))(k_cache[:, :, Sk - W:, :], rep, axis=1)
        v = (lambda x, n, axis: torch.repeat_interleave(x, n, dim=axis))(v_cache[:, :, Sk - W:, :], rep, axis=1)
        scores = torch.einsum('bhqd,bhkd->bhqk', q, k) * Dh ** (-0.5)
        p = _softmax(scores, axis=-1)
        o = torch.einsum('bhqk,bhkd->bhqd', p, v)
        return (lambda x, *d: x.permute(*d))(o, 0, 2, 1, 3).reshape(B, Sq, Hq * Dh) @ wo

    def _B_ref(x, w_gate, w_up, w_down):
        g = torch.einsum('bsd,df->bsf', x, w_gate)
        u = torch.einsum('bsd,df->bsf', x, w_up)
        h = _gelu(g) * u
        return torch.einsum('bsf,fd->bsd', h, w_down)
    x = A_x
    mod = (_silu(c) @ w_mod).reshape(c.shape[0], 6, 1, -1)
    sa, ca, ga, sb, cb, gb = (mod[:, 0].clone(), mod[:, 1].clone(), mod[:, 2].clone(), mod[:, 3].clone(), mod[:, 4].clone(), mod[:, 5].clone())
    h = _layernorm(x, axis=-1) * (1 + ca) + sa
    x = x + ga * _A_ref(h, A_k_cache, A_v_cache, A_wq, A_wo)
    h = _layernorm(x, axis=-1) * (1 + cb) + sb
    x = x + gb * _B_ref(h, B_w_gate, B_w_up, B_w_down)
    return x
```

```cpp
#include <hip/hip_runtime.h>
#include <stdint.h>
#include <utility>

#define DI __device__ __forceinline__
#define LAS __attribute__((address_space(3)))
typedef __bf16 bf16x8 __attribute__((ext_vector_type(8)));
typedef __bf16 bf16x4 __attribute__((ext_vector_type(4)));
typedef __bf16 bf16x2 __attribute__((ext_vector_type(2)));
typedef float f32x2 __attribute__((ext_vector_type(2)));
typedef float f32x4 __attribute__((ext_vector_type(4)));
typedef float f32x16 __attribute__((ext_vector_type(16)));
typedef unsigned u32x2 __attribute__((ext_vector_type(2)));
typedef unsigned u32x4 __attribute__((ext_vector_type(4)));

constexpr int M_ = 2048, D_ = 576, HQ = 32, HKV = 16, DH = 128, SK = 2048, WIN = 512, FF = 4608;

constexpr size_t WS_MOD = 0;
constexpr size_t WS_H1 = 16384;
constexpr size_t WS_O = WS_H1 + (size_t)64 * 36 * 1024;
constexpr size_t WS_X1 = WS_O + (size_t)64 * 256 * 1024;
constexpr size_t WS_H2 = WS_X1 + (size_t)M_ * D_ * 4;
constexpr size_t WS_H = WS_H2 + (size_t)64 * 36 * 1024;
constexpr size_t WS_END = WS_H + (size_t)64 * 288 * 1024;

template <int... Is, class F> DI void static_for_impl(std::integer_sequence<int, Is...>, F&& f) { (f(std::integral_constant<int, Is>{}), ...); }
template <int N, class F> DI void static_for(F&& f) { static_for_impl(std::make_integer_sequence<int, N>{}, f); }

DI float bflo(unsigned w) { return __uint_as_float(w << 16); }
DI float bfhi(unsigned w) { return __uint_as_float(w & 0xffff0000u); }
DI unsigned pk2(float lo, float hi) { f32x2 v = {lo, hi}; bf16x2 b = __builtin_convertvector(v, bf16x2); return __builtin_bit_cast(unsigned, b); }
DI unsigned rfl(unsigned x) { return __builtin_amdgcn_readfirstlane(x); }
DI void SB() { __builtin_amdgcn_sched_barrier(0); }

template <int IMM> DI void dma16(unsigned m0v, unsigned voff, const void* sbase) {
  asm volatile("s_mov_b32 m0, %0\n\ts_nop 0\n\tglobal_load_lds_dwordx4 %1, %2 offset:%3" ::"s"(m0v), "v"(voff), "s"(sbase), "i"(IMM) : "memory");
}
template <int IMM> DI void gload16(bf16x8& dst, unsigned voff, const void* sbase) {
  asm volatile("global_load_dwordx4 %0, %1, %2 offset:%3" : "=v"(dst) : "v"(voff), "s"(sbase), "i"(IMM));
}
template <int N> DI void wait_vm() { asm volatile("s_waitcnt vmcnt(%0)" ::"i"(N) : "memory"); }
template <int N> DI void wait_vm_bar() { asm volatile("s_waitcnt vmcnt(%0) lgkmcnt(0)\n\ts_barrier" ::"i"(N) : "memory"); }
DI void pin(bf16x8& a) { asm volatile("" : "+v"(a)); }
DI bf16x4 tr4(LAS char* p) { return __builtin_amdgcn_ds_read_tr16_b64_v4bf16((LAS bf16x4*)p); }
DI bf16x8 cat(bf16x4 a, bf16x4 b) { return __builtin_shufflevector(a, b, 0, 1, 2, 3, 4, 5, 6, 7); }
DI f32x16 mfma32(bf16x8 a, bf16x8 b, f32x16 c) { return __builtin_amdgcn_mfma_f32_32x32x16_bf16(a, b, c, 0, 0, 0); }
DI f32x4 mfma16(bf16x8 a, bf16x8 b, f32x4 c) { return __builtin_amdgcn_mfma_f32_16x16x32_bf16(a, b, c, 0, 0, 0); }

#define ST_DECL
#define ST(i)
#define ST_END(kid)
#define DBG_PARAM
#define DBG_ARG
__global__ __launch_bounds__(512) void k_mod(const unsigned short* __restrict__ c, const unsigned short* __restrict__ wmod, float* __restrict__ mod) {
  __shared__ float red[8][64];
  const int tid = threadIdx.x, cg = tid & 7, rg = tid >> 3, lane = tid & 63, wid = tid >> 6;
  const int col0 = blockIdx.x * 64 + cg * 8;
  u32x4 w[9]; float s[9];
#pragma unroll
  for (int i = 0; i < 9; ++i) { const int row = rg + 64 * i; w[i] = *(const u32x4*)(wmod + (size_t)row * 3456 + col0); const float cv = __uint_as_float((unsigned)c[row] << 16); s[i] = cv / (1.f + __expf(-cv)); }
  float acc[8] = {};
#pragma unroll
  for (int i = 0; i < 9; ++i)
#pragma unroll
    for (int j = 0; j < 4; ++j) { acc[2 * j] += s[i] * bflo(w[i][j]); acc[2 * j + 1] += s[i] * bfhi(w[i][j]); }
#pragma unroll
  for (int j = 0; j < 8; ++j) { acc[j] += __shfl_xor(acc[j], 8); acc[j] += __shfl_xor(acc[j], 16); acc[j] += __shfl_xor(acc[j], 32); }
  if (lane < 8) {
#pragma unroll
    for (int j = 0; j < 8; ++j) red[wid][lane * 8 + j] = acc[j];
  }
  __syncthreads();
  if (tid < 64) { float t = 0.f;
#pragma unroll
    for (int w2 = 0; w2 < 8; ++w2) t += red[w2][tid];
    mod[blockIdx.x * 64 + tid] = t; }
}

template <bool F32IN>
__global__ __launch_bounds__(256) void k_ln(const void* __restrict__ xin, const float* __restrict__ shift, const float* __restrict__ scale, char* __restrict__ out) {
  __shared__ __attribute__((aligned(16))) float sc[576], sh[576];
  const int tid = threadIdx.x, lane = tid & 63, wid = tid >> 6, cc = lane & 7, rr = lane >> 3;
  const int row = (blockIdx.x * 4 + wid) * 8 + rr;
  float v[9][8];
  if (F32IN) {
    const float* x = (const float*)xin + (size_t)row * D_;
#pragma unroll
    for (int i = 0; i < 9; ++i) { const f32x4 a = *(const f32x4*)(x + (cc + 8 * i) * 8), b = *(const f32x4*)(x + (cc + 8 * i) * 8 + 4);
#pragma unroll
      for (int j = 0; j < 4; ++j) { v[i][j] = a[j]; v[i][4 + j] = b[j]; } }
  } else {
    const unsigned short* x = (const unsigned short*)xin + (size_t)row * D_;
#pragma unroll
    for (int i = 0; i < 9; ++i) { const u32x4 a = *(const u32x4*)(x + (cc + 8 * i) * 8);
#pragma unroll
      for (int j = 0; j < 4; ++j) { v[i][2 * j] = bflo(a[j]); v[i][2 * j + 1] = bfhi(a[j]); } }
  }
  for (int i = tid; i < 576; i += 256) { sc[i] = 1.f + scale[i]; sh[i] = shift[i]; }
  float s = 0.f;
#pragma unroll
  for (int i = 0; i < 9; ++i)
#pragma unroll
    for (int j = 0; j < 8; ++j) s += v[i][j];
  s += __shfl_xor(s, 1); s += __shfl_xor(s, 2); s += __shfl_xor(s, 4);
  const float mean = s * (1.f / 576.f);
  float q = 0.f;
#pragma unroll
  for (int i = 0; i < 9; ++i)
#pragma unroll
    for (int j = 0; j < 8; ++j) { const float d = v[i][j] - mean; q += d * d; }
  q += __shfl_xor(q, 1); q += __shfl_xor(q, 2); q += __shfl_xor(q, 4);
  const float rstd = 1.f / sqrtf(q * (1.f / 576.f) + 1e-5f);
  __syncthreads();
  char* ob = out + (size_t)(row >> 5) * 36 * 1024 + (row & 31) * 16;
#pragma unroll
  for (int i = 0; i < 9; ++i) {
    const int c = cc + 8 * i;
    const f32x4 s0 = *(const f32x4*)(sc + c * 8), s1 = *(const f32x4*)(sc + c * 8 + 4), t0 = *(const f32x4*)(sh + c * 8), t1 = *(const f32x4*)(sh + c * 8 + 4);
    float o[8];
#pragma unroll
    for (int j = 0; j < 4; ++j) { o[j] = (v[i][j] - mean) * rstd * s0[j] + t0[j]; o[4 + j] = (v[i][4 + j] - mean) * rstd * s1[j] + t1[j]; }
    u32x4 w = {pk2(o[0], o[1]), pk2(o[2], o[3]), pk2(o[4], o[5]), pk2(o[6], o[7])};
    *(u32x4*)(ob + (c >> 1) * 1024 + (c & 1) * 512) = w;
  }
}

constexpr float QSCALE = 0.08838834764831845f * 1.4426950408889634f;
constexpr float THR = 8.0f;
constexpr int A_LDS = 131072;

struct SMState { float m, l; };

template <bool FIRST> DI void softmax_tile(f32x16& s0, f32x16& s1, f32x16 (&o)[4], float& m, float& l, bf16x8 (&p)[4]) {
  float a = fmaxf(fmaxf(s0[0], s0[1]), s0[2]);
#pragma unroll
  for (int r = 3; r < 15; r += 2) a = fmaxf(fmaxf(a, s0[r]), s0[r + 1]);
  a = fmaxf(a, s0[15]);
  float b = fmaxf(fmaxf(s1[0], s1[1]), s1[2]);
#pragma unroll
  for (int r = 3; r < 15; r += 2) b = fmaxf(fmaxf(b, s1[r]), s1[r + 1]);
  b = fmaxf(b, s1[15]);
  a = fmaxf(a, b);
  { auto rr = __builtin_amdgcn_permlane32_swap(__float_as_uint(a), __float_as_uint(a), false, false); a = fmaxf(__uint_as_float(rr[0]), __uint_as_float(rr[1])); }
  if (FIRST) { m = a; }
  else if (__builtin_expect(__any(a - m > THR), 0)) {
    const float mn = fmaxf(m, a), f = __builtin_amdgcn_exp2f(m - mn);
    m = mn; l *= f;
#pragma unroll
    for (int d = 0; d < 4; ++d)
#pragma unroll
      for (int r = 0; r < 16; ++r) o[d][r] *= f;
  }
  float sum = 0.f;
#pragma unroll
  for (int r = 0; r < 16; ++r) { s0[r] = __builtin_amdgcn_exp2f(s0[r] - m); s1[r] = __builtin_amdgcn_exp2f(s1[r] - m); }
#pragma unroll
  for (int r = 0; r < 16; ++r) sum += s0[r] + s1[r];
  l += sum;
#pragma unroll
  for (int s = 0; s < 2; ++s) {
    u32x4 w0 = {pk2(s0[8 * s], s0[8 * s + 1]), pk2(s0[8 * s + 2], s0[8 * s + 3]), pk2(s0[8 * s + 4], s0[8 * s + 5]), pk2(s0[8 * s + 6], s0[8 * s + 7])};
    u32x4 w1 = {pk2(s1[8 * s], s1[8 * s + 1]), pk2(s1[8 * s + 2], s1[8 * s + 3]), pk2(s1[8 * s + 4], s1[8 * s + 5]), pk2(s1[8 * s + 6], s1[8 * s + 7])};
    p[s] = __builtin_bit_cast(bf16x8, w0); p[2 + s] = __builtin_bit_cast(bf16x8, w1);
  }
}

__global__ __launch_bounds__(512, 2) void k_attn(const char* __restrict__ h1, const char* __restrict__ wq, const char* __restrict__ kc, const char* __restrict__ vc, char* __restrict__ O DBG_PARAM) {
  ST_DECL
  extern __shared__ __attribute__((aligned(16))) char lds_[];
  LAS char* lds = (LAS char*)lds_;
  const int tid = threadIdx.x, lane = tid & 63;
  const int wid = rfl(tid >> 6), grp = wid >> 2, u = wid & 3;
  const int x = blockIdx.x & 7, j = blockIdx.x >> 3, head = 4 * x + (j & 3), rt = j >> 2, kvh = head >> 1;
  const int rb = rt * 8 + wid;
  const int h = lane >> 5, r = lane & 31, blk = (lane >> 4) & 1, q = (lane & 15) >> 2, p = lane & 3;
  const int l4 = lane >> 4, pos = lane & 15;
  unsigned wsrc[2], ksrc2[2];
#pragma unroll
  for (int e = 0; e < 2; ++e) { const int n = 2 * wid + e, row = 4 * n + l4, ch = pos ^ ((l4 << 2) | (n & 3)); wsrc[e] = row * 8192 + ch * 16; ksrc2[e] = row * 256 + ch * 16 - e * 1024; }
  const unsigned kbase = (16 * u + l4) * 256 + (pos ^ (l4 << 2)) * 16;
  const char* wq_h = wq + head * 256;
  const char* kb_ = kc + ((size_t)kvh * SK + (SK - WIN)) * 256;
  const char* vb_ = vc + ((size_t)kvh * SK + (SK - WIN)) * 256;
  const char* h1w = h1 + (size_t)rb * 36 * 1024;
  const unsigned lane16 = lane * 16;
  auto dmaW = [&](int qq) {
    const unsigned m = 65536 + (qq & 3) * 16384 + wid * 2048; const char* s = wq_h + (size_t)qq * 64 * 8192;
    dma16<0>(m, wsrc[0], s); dma16<0>(m + 1024, wsrc[1], s); };
  auto dmaK8 = [&](int t) { const unsigned m = (t & 3) * 16384 + wid * 2048; const char* s = kb_ + t * 16384; dma16<0>(m, ksrc2[0], s); dma16<1024>(m, ksrc2[1], s); };
  auto dmaV8 = [&](int t) { const unsigned m = 65536 + ((t + 1) & 3) * 16384 + wid * 2048; const char* s = vb_ + t * 16384; dma16<0>(m, ksrc2[0], s); dma16<1024>(m, ksrc2[1], s); };
  auto dma4 = [&](unsigned slot, const char* s) { const unsigned m = slot + u * 4096;
    dma16<0>(m, kbase, s); dma16<1024>(m, kbase ^ 16, s); dma16<2048>(m, kbase ^ 32, s); dma16<3072>(m, kbase ^ 48, s); };
  bf16x8 g[9][4];
  auto loadG = [&](auto QQ) { constexpr int qq = QQ; const char* s = h1w + qq * 4096;
    gload16<0>(g[qq][0], lane16, s); gload16<1024>(g[qq][1], lane16, s); gload16<2048>(g[qq][2], lane16, s); gload16<3072>(g[qq][3], lane16, s); };
  using std::integral_constant;
  dmaW(0); loadG(integral_constant<int, 0>{}); dmaW(1); loadG(integral_constant<int, 1>{}); dmaW(2); loadG(integral_constant<int, 2>{});
  dmaK8(0); loadG(integral_constant<int, 3>{});
  const unsigned wb0 = 65536 + 256 * (8 * h + q) + 16 * ((q << 2) | ((2 * blk + (p & 1)) ^ (2 * h))) + 8 * (p >> 1);
  f32x16 acc[4] = {};
  static_for<9>([&](auto QQ) {
    constexpr int qq = QQ;
    constexpr int NW[9] = {18, 18, 18, 16, 16, 16, 14, 12, 10};
    SB(); wait_vm_bar<NW[qq]>(); pin(g[qq][0]); pin(g[qq][1]); pin(g[qq][2]); pin(g[qq][3]); SB();
    if constexpr (qq == 0) ST(1);
    if constexpr (qq == 4) ST(2);
    if constexpr (qq + 3 < 9) dmaW(qq + 3);
    if constexpr (qq == 6) dmaV8(0);
    if constexpr (qq == 7) dmaV8(1);
    if constexpr (qq == 8) dmaV8(2);
    static_for<4>([&](auto KS) {
      constexpr int ks = KS;
      static_for<4>([&](auto DB) {
        constexpr int db = DB;
        constexpr int off = (qq & 3) * 16384 + ks * 4096;
        const bf16x4 lo = tr4(lds + (wb0 ^ (db << 6)) + off), hi = tr4(lds + (wb0 ^ (db << 6) ^ 16) + off + 1024);
        acc[db] = mfma32(cat(lo, hi), g[qq][ks], acc[db]);
      });
    });
    SB();
    if constexpr (qq + 4 < 9) loadG(integral_constant<int, qq + 4>{});
    if constexpr (qq == 5) dmaK8(1);
    if constexpr (qq == 6) dmaK8(2);
    if constexpr (qq == 7) dmaK8(3);
  });
  bf16x8 qf[8];
#pragma unroll
  for (int db = 0; db < 4; ++db)
#pragma unroll
    for (int s = 0; s < 2; ++s) { u32x4 w;
#pragma unroll
      for (int i = 0; i < 4; ++i) w[i] = pk2(acc[db][8 * s + 2 * i] * QSCALE, acc[db][8 * s + 2 * i + 1] * QSCALE);
      qf[2 * db + s] = __builtin_bit_cast(bf16x8, w); }
  const unsigned kb0 = 256 * r + 16 * (h ^ (((r & 3) << 2) | ((r >> 2) & 3)));
  const unsigned vb0 = 65536 + 256 * (4 * h + q) + 16 * ((q << 2) | ((2 * blk + (p >> 1)) ^ h)) + 8 * (p & 1);
  f32x16 o[4] = {}; f32x16 s0, s1; bf16x8 pf[4]; float m = 0.f, l = 0.f;
  auto QK = [&](auto T) { constexpr int t = T; constexpr int so = (t & 3) * 16384;
    s0 = f32x16{}; s1 = f32x16{};
    static_for<8>([&](auto S) { constexpr int s = S;
      const bf16x8 k0 = *(LAS bf16x8*)(lds + (kb0 ^ (s << 5)) + so), k1 = *(LAS bf16x8*)(lds + (kb0 ^ (s << 5)) + so + 8192);
      s0 = mfma32(k0, qf[s], s0); s1 = mfma32(k1, qf[s], s1); }); };
  auto PV = [&](auto T) { constexpr int t = T; constexpr int so = ((t + 1) & 3) * 16384;
    static_for<4>([&](auto KK) { constexpr int kk = KK;
      static_for<4>([&](auto DB) { constexpr int db = DB;
        const bf16x4 lo = tr4(lds + (vb0 ^ (db << 6)) + so + kk * 4096), hi = tr4(lds + (vb0 ^ (db << 6) ^ 32) + so + kk * 4096 + 2048);
        o[db] = mfma32(cat(lo, hi), pf[kk], o[db]); }); }); };
  ST(3);
  SB(); wait_vm_bar<63>(); SB();
  ST(4);
  if (grp == 0) {
    static_for<8>([&](auto T) { constexpr int t = T;
      if constexpr (t > 0) PV(integral_constant<int, t - 1>{});
      QK(T);
      if constexpr (t == 4) ST(8);
      SB(); wait_vm_bar<63>(); SB();
      if constexpr (t == 4) ST(9);
      if constexpr (t == 3) ST(5);
      wait_vm<0>();
      if constexpr (t >= 2 && t <= 5) dma4(65536 + ((t + 2 + 1) & 3) * 16384, vb_ + (t + 2) * 16384);
      softmax_tile<t == 0>(s0, s1, o, m, l, pf);
      if constexpr (t == 3) ST(6);
      SB(); wait_vm_bar<63>(); SB();
      if constexpr (t == 3) ST(7);
      if constexpr (t == 3) {}
    });
    PV(integral_constant<int, 7>{});
  } else {
    wait_vm<0>(); dma4(65536 + ((3 + 1) & 3) * 16384, vb_ + 3 * 16384);
    SB(); wait_vm_bar<63>(); SB();
    static_for<8>([&](auto T) { constexpr int t = T;
      if constexpr (t > 0) PV(integral_constant<int, t - 1>{});
      QK(T);
      SB(); wait_vm_bar<63>(); SB();
      wait_vm<0>();
      if constexpr (t <= 3) dma4(((t + 4) & 3) * 16384, kb_ + (t + 4) * 16384);
      softmax_tile<t == 0>(s0, s1, o, m, l, pf);
      if constexpr (t < 7) { SB(); wait_vm_bar<63>(); SB(); }
    });
    PV(integral_constant<int, 7>{});
  }
  ST(12);
  { auto rr = __builtin_amdgcn_permlane32_swap(__float_as_uint(l), __float_as_uint(l), false, false); l = __uint_as_float(rr[0]) + __uint_as_float(rr[1]); }
  const float inv = 1.f / l;
  char* ob = O + ((size_t)rb * 256 + head * 8) * 1024 + lane16;
#pragma unroll
  for (int db = 0; db < 4; ++db)
#pragma unroll
    for (int s = 0; s < 2; ++s) { u32x4 w;
#pragma unroll
      for (int i = 0; i < 4; ++i) w[i] = pk2(o[db][8 * s + 2 * i] * inv, o[db][8 * s + 2 * i + 1] * inv);
      *(u32x4*)(ob + (2 * db + s) * 1024) = w; }
  ST_END(0);
}

constexpr int SK_LDS = 163840;
template <int NS16, bool FINAL>
__global__ __launch_bounds__(512, 2) void k_skinny(const char* __restrict__ act, const char* __restrict__ W, const float* __restrict__ gate, const void* __restrict__ resid, void* __restrict__ out DBG_PARAM) {
  ST_DECL
  extern __shared__ __attribute__((aligned(16))) char lds_[];
  LAS char* lds = (LAS char*)lds_;
  constexpr int KS = NS16 / 16;
  constexpr int P = 4, NSLOT = 5;
  const int tid = threadIdx.x, lane = tid & 63, wid = rfl(tid >> 6);
  const int b = blockIdx.x, x = b & 7, jj = b >> 3;
  const int idx = x * 30 + jj;  if (idx >= 234) return;
  const int rt = idx / 9, ct = idx - rt * 9;
  const int g = lane >> 4, m16 = lane & 15, q = (lane & 15) >> 2, p = lane & 3;
  unsigned aoff[5];
#pragma unroll
  for (int a = 0; a < 5; ++a) { int row = rt * 80 + a * 16; if (row > M_ - 16) row = M_ - 16; row += m16;
    aoff[a] = (unsigned)(row >> 5) * (NS16 * 1024) + (row & 31) * 16 + (g >> 1) * 1024 + (g & 1) * 512; }
  const char* actw = act + (size_t)wid * KS * 2048;
  const int rr = lane >> 3, pp = lane & 7;
  const unsigned wsrc = rr * 1152 + ((((pp >> 1) ^ ((rr >> 1) & 3)) << 1) + (pp & 1)) * 16;
  const char* Ww = W + (size_t)wid * KS * 32 * 1152 + ct * 128;
  const unsigned ring = wid * (NSLOT * 4096);
  bf16x8 af[KS][5];
  auto issue = [&](auto S) { constexpr int s = S;
    const char* ws = Ww + (size_t)s * 32 * 1152; const unsigned m = ring + (s % NSLOT) * 4096;
    dma16<0>(m, wsrc, ws); dma16<0>(m + 1024, wsrc, ws + 9216); dma16<0>(m + 2048, wsrc, ws + 2 * 9216); dma16<0>(m + 3072, wsrc, ws + 3 * 9216);
    const char* as = actw + s * 2048;
#pragma unroll
    for (int a = 0; a < 5; ++a) gload16<0>(af[s][a], aoff[a], as); };
  static_for<P>([&](auto S) { issue(S); });
  const unsigned xl = 2 * (g & 1) + (q >> 1);
  unsigned tb[4];
#pragma unroll
  for (int c = 0; c < 4; ++c) tb[c] = ring + 128 * (16 * (g >> 1) + 4 * (g & 1) + q) + 32 * (c ^ xl) + 8 * p;
  f32x4 acc[5][4] = {};
  static_for<KS>([&](auto S) { constexpr int s = S;
    constexpr int later = (KS - 1 - s) < (P - 1) ? (KS - 1 - s) : (P - 1);
    SB(); wait_vm<later * 9>();
#pragma unroll
    for (int a = 0; a < 5; ++a) pin(af[s][a]);
    SB();
    if constexpr (s == 0) ST(1);
    if constexpr (s == 4) ST(2);
    if constexpr (s == 8) ST(3);
    if constexpr (s == 12) ST(4);
    bf16x8 wf[4];
#pragma unroll
    for (int c = 0; c < 4; ++c) wf[c] = cat(tr4(lds + tb[c] + (s % NSLOT) * 4096), tr4(lds + tb[c] + (s % NSLOT) * 4096 + 1024));
#pragma unroll
    for (int a = 0; a < 5; ++a)
#pragma unroll
      for (int c = 0; c < 4; ++c) acc[a][c] = mfma16(wf[c], af[s][a], acc[a][c]);
    if constexpr (s + P < KS) { asm volatile("s_waitcnt lgkmcnt(0)" ::: "memory"); issue(std::integral_constant<int, s + P>{}); }
  });
  ST(5);
  SB(); wait_vm_bar<0>(); SB();
  ST(6);
#pragma unroll
  for (int a = 0; a < 5; ++a)
#pragma unroll
    for (int c = 0; c < 4; ++c) *(LAS f32x4*)(lds + wid * 20480 + (a * 4 + c) * 1024 + lane * 16) = acc[a][c];
  __syncthreads();
  ST(7);
  for (int f = wid; f < 20; f += 8) {
    f32x4 t = *(LAS f32x4*)(lds + f * 1024 + lane * 16);
#pragma unroll
    for (int w = 1; w < 8; ++w) t += *(LAS f32x4*)(lds + w * 20480 + f * 1024 + lane * 16);
    const int a = f >> 2, c = f & 3, row = rt * 80 + a * 16 + m16, n = ct * 64 + c * 16 + g * 4;
    if (row < M_) {
      const f32x4 gv = *(const f32x4*)(gate + n);
      if (!FINAL) { const u32x2 xv = *(const u32x2*)((const unsigned short*)resid + (size_t)row * D_ + n);
        f32x4 ov = {bflo(xv[0]) + gv[0] * t[0], bfhi(xv[0]) + gv[1] * t[1], bflo(xv[1]) + gv[2] * t[2], bfhi(xv[1]) + gv[3] * t[3]};
        *(f32x4*)((float*)out + (size_t)row * D_ + n) = ov;
      } else { const f32x4 xv = *(const f32x4*)((const float*)resid + (size_t)row * D_ + n);
        u32x2 w = {pk2(xv[0] + gv[0] * t[0], xv[1] + gv[1] * t[1]), pk2(xv[2] + gv[2] * t[2], xv[3] + gv[3] * t[3])};
        *(u32x2*)((unsigned short*)out + (size_t)row * D_ + n) = w; }
    }
  }
  ST_END(FINAL ? 3 : 1);
}

constexpr int C_STG = 9216 + 128 + 9216, C_UOFF = 9216 + 128, C_NSTG = 4, C_P = 3, C_LDS = C_STG * C_NSTG;
DI float geglu(float g, float u) {
  const float c0 = -2.f * 0.7978845608028654f * 1.4426950408889634f, c1 = c0 * 0.044715f;
  const float e = __builtin_amdgcn_exp2f(g * (c0 + c1 * g * g));
  return g * __builtin_amdgcn_rcpf(1.f + e) * u;
}
__global__ __launch_bounds__(512, 2) void k_ffn1(const char* __restrict__ h2, const char* __restrict__ wg, const char* __restrict__ wu, char* __restrict__ H DBG_PARAM) {
  ST_DECL
  extern __shared__ __attribute__((aligned(16))) char lds_[];
  LAS char* lds = (LAS char*)lds_;
  const int tid = threadIdx.x, lane = tid & 63, wid = rfl(tid >> 6);
  const int x = blockIdx.x & 7, j = blockIdx.x >> 3, ct = 4 * x + (j & 3), rt = j >> 2;
  const int rb = rt * 8 + wid;
  const int h = lane >> 5, blk = (lane >> 4) & 1, q = (lane & 15) >> 2, p = lane & 3;
  unsigned wsrc[3];
#pragma unroll
  for (int e = 0; e < 3; ++e) { const int o = 64 * e + lane, row = o / 18, ch = o - row * 18; wsrc[e] = row * (FF * 2) + ch * 16; }
  const char* Ww = (wid < 4 ? wg : wu) + (size_t)(wid & 3) * 8 * (FF * 2) + ct * 288;
  const unsigned wdst = (wid < 4 ? 0 : C_UOFF) + (wid & 3) * 2304;
  const char* hw = h2 + (size_t)rb * 36 * 1024;
  const unsigned lane16 = lane * 16;
  bf16x8 af[18][2];
  auto issue = [&](auto S) { constexpr int s = S;
    const char* ws = Ww + (size_t)s * 32 * (FF * 2); const unsigned m = (s % C_NSTG) * C_STG + wdst;
    dma16<0>(m, wsrc[0], ws); dma16<0>(m + 1024, wsrc[1], ws); if (lane < 16) dma16<0>(m + 2048, wsrc[2], ws);
    gload16<0>(af[s][0], lane16, hw + s * 2048); gload16<1024>(af[s][1], lane16, hw + s * 2048); };
  static_for<C_P>([&](auto S) { issue(S); });
  const unsigned tb0 = blk * C_UOFF + 288 * (8 * h + q) + 8 * p;
  f32x16 acc[9] = {};
  static_for<18>([&](auto S) { constexpr int s = S;
    constexpr int later = (17 - s) < (C_P - 1) ? (17 - s) : (C_P - 1);
    SB(); wait_vm_bar<later * 5>(); pin(af[s][0]); pin(af[s][1]); SB();
    if constexpr (s == 0) ST(1);
    if constexpr (s == 4) ST(2);
    if constexpr (s == 8) ST(3);
    if constexpr (s == 12) ST(4);
    if constexpr (s == 16) ST(5);
    if constexpr (s + C_P < 18) issue(std::integral_constant<int, s + C_P>{});
    const unsigned tb = tb0 + (s % C_NSTG) * C_STG;
    static_for<2>([&](auto KS) { constexpr int ks = KS;
      static_for<9>([&](auto F) { constexpr int f = F;
        const bf16x4 lo = tr4(lds + tb + 288 * 16 * ks + 32 * f), hi = tr4(lds + tb + 288 * (16 * ks + 4) + 32 * f);
        acc[f] = mfma32(cat(lo, hi), af[s][ks], acc[f]); }); });
  });
  ST(6);
  char* ob = H + ((size_t)rb * 288 + ct * 9) * 1024 + lane16;
#pragma unroll
  for (int f = 0; f < 9; ++f) { u32x4 w;
#pragma unroll
    for (int i = 0; i < 4; ++i) w[i] = pk2(geglu(acc[f][2 * i], acc[f][8 + 2 * i]), geglu(acc[f][2 * i + 1], acc[f][8 + 2 * i + 1]));
    *(u32x4*)(ob + f * 1024) = w; }
  ST_END(2);
}

extern "C" void kernel_launch(void* const* d_in, const int* in_sizes, int n_in, void* d_out, int out_size, void* d_ws, size_t ws_size, hipStream_t stream) {
  const void* x = d_in[0]; const char* kc = (const char*)d_in[1]; const char* vc = (const char*)d_in[2];
  const char* wq = (const char*)d_in[3]; const char* wo = (const char*)d_in[4];
  const char* wg = (const char*)d_in[6]; const char* wu = (const char*)d_in[7]; const char* wd = (const char*)d_in[8];
  const unsigned short* c = (const unsigned short*)d_in[9]; const unsigned short* wmod = (const unsigned short*)d_in[10];
  char* ws = (char*)d_ws; float* mod = (float*)(ws + WS_MOD);
  (void)hipFuncSetAttribute((const void*)k_attn, hipFuncAttributeMaxDynamicSharedMemorySize, A_LDS);
  (void)hipFuncSetAttribute((const void*)k_skinny<256, false>, hipFuncAttributeMaxDynamicSharedMemorySize, SK_LDS);
  (void)hipFuncSetAttribute((const void*)k_skinny<288, true>, hipFuncAttributeMaxDynamicSharedMemorySize, SK_LDS);
  (void)hipFuncSetAttribute((const void*)k_ffn1, hipFuncAttributeMaxDynamicSharedMemorySize, C_LDS);
  k_mod<<<54, 512, 0, stream>>>(c, wmod, mod);
  k_ln<false><<<64, 256, 0, stream>>>(x, mod + 0 * D_, mod + 1 * D_, ws + WS_H1);
  k_attn<<<256, 512, A_LDS, stream>>>(ws + WS_H1, wq, kc, vc, ws + WS_O DBG_ARG);
  k_skinny<256, false><<<240, 512, SK_LDS, stream>>>(ws + WS_O, wo, mod + 2 * D_, x, ws + WS_X1 DBG_ARG);
  k_ln<true><<<64, 256, 0, stream>>>(ws + WS_X1, mod + 3 * D_, mod + 4 * D_, ws + WS_H2);
  k_ffn1<<<256, 512, C_LDS, stream>>>(ws + WS_H2, wg, wu, ws + WS_H DBG_ARG);
  k_skinny<288, true><<<240, 512, SK_LDS, stream>>>(ws + WS_H, wd, mod + 5 * D_, ws + WS_X1, d_out DBG_ARG);
}
```

```cpp
#include <hip/hip_runtime.h>
#include <stdint.h>
#include <utility>

#define DI __device__ __forceinline__
#define LAS __attribute__((address_space(3)))
#define GAS __attribute__((address_space(1)))
typedef __bf16 bf16x8 __attribute__((ext_vector_type(8)));
typedef __bf16 bf16x4 __attribute__((ext_vector_type(4)));
typedef __bf16 bf16x2 __attribute__((ext_vector_type(2)));
typedef float f32x2 __attribute__((ext_vector_type(2)));
typedef float f32x4 __attribute__((ext_vector_type(4)));
typedef float f32x16 __attribute__((ext_vector_type(16)));
typedef unsigned u32x2 __attribute__((ext_vector_type(2)));
typedef unsigned u32x4 __attribute__((ext_vector_type(4)));
using std::integral_constant;

constexpr int M_ = 2048, D_ = 576, SK = 2048, WIN = 512, FF = 4608;

constexpr size_t WS_MOD = 0;
constexpr size_t WS_STAT = 16384;
constexpr size_t WS_H1 = WS_STAT + 149760 + 3840;
constexpr size_t WS_O = WS_H1 + (size_t)64 * 36 * 1024;
constexpr size_t WS_X1 = WS_O + (size_t)64 * 256 * 1024;
constexpr size_t WS_H2 = WS_X1 + (size_t)M_ * D_ * 4;
constexpr size_t WS_H = WS_H2 + (size_t)64 * 36 * 1024;
constexpr size_t WS_WQ = WS_H + (size_t)64 * 288 * 1024;
constexpr size_t WS_BP = WS_WQ + (size_t)32 * 36 * 4 * 1024;
constexpr size_t WS_END = WS_BP + (size_t)18 * 4096 * 4;

template <int... Is, class F> DI void static_for_impl(std::integer_sequence<int, Is...>, F&& f) { (f(std::integral_constant<int, Is>{}), ...); }
template <int N, class F> DI void static_for(F&& f) { static_for_impl(std::make_integer_sequence<int, N>{}, f); }

DI float bflo(unsigned w) { return __uint_as_float(w << 16); }
DI float bfhi(unsigned w) { return __uint_as_float(w & 0xffff0000u); }
DI unsigned pk2(float lo, float hi) { f32x2 v = {lo, hi}; bf16x2 b = __builtin_convertvector(v, bf16x2); return __builtin_bit_cast(unsigned, b); }
DI unsigned rfl(unsigned x) { return __builtin_amdgcn_readfirstlane(x); }
DI void SB() { __builtin_amdgcn_sched_barrier(0); }

template <int IMM> DI void dma16(unsigned m0v, unsigned voff, const void* sbase) {
  asm volatile("s_mov_b32 m0, %0\n\ts_nop 0\n\tglobal_load_lds_dwordx4 %1, %2 offset:%3" ::"s"(m0v), "v"(voff), "s"(sbase), "i"(IMM) : "memory");
}
template <int IMM, class T> DI void gload16(T& dst, unsigned voff, const void* sbase) {
  asm volatile("global_load_dwordx4 %0, %1, %2 offset:%3" : "=v"(dst) : "v"(voff), "s"(sbase), "i"(IMM));
}
template <int N> DI void wait_vm() { asm volatile("s_waitcnt vmcnt(%0)" ::"i"(N) : "memory"); }
template <int N> DI void vm_bar() { asm volatile("s_waitcnt vmcnt(%0)\n\ts_barrier" ::"i"(N) : "memory"); }
template <int N> DI void vm_lgkm_bar() { asm volatile("s_waitcnt vmcnt(%0) lgkmcnt(0)\n\ts_barrier" ::"i"(N) : "memory"); }
DI void bar() { asm volatile("s_barrier" ::: "memory"); }
template <class T> DI void pin(T& a) { asm volatile("" : "+v"(a)); }
DI bf16x4 tr4(LAS char* p) { return __builtin_amdgcn_ds_read_tr16_b64_v4bf16((LAS bf16x4*)p); }
DI bf16x8 cat(bf16x4 a, bf16x4 b) { return __builtin_shufflevector(a, b, 0, 1, 2, 3, 4, 5, 6, 7); }
DI f32x16 mfma32(bf16x8 a, bf16x8 b, f32x16 c) { return __builtin_amdgcn_mfma_f32_32x32x16_bf16(a, b, c, 0, 0, 0); }
DI f32x4 mfma16(bf16x8 a, bf16x8 b, f32x4 c) { return __builtin_amdgcn_mfma_f32_16x16x32_bf16(a, b, c, 0, 0, 0); }
DI float max3f(float a, float b, float c) { float r; asm("v_max3_f32 %0, %1, %2, %3" : "=v"(r) : "v"(a), "v"(b), "v"(c)); return r; }
DI float max2f(float a, float b) { float r; asm("v_max_f32_e32 %0, %1, %2" : "=v"(r) : "v"(a), "v"(b)); return r; }
template <int CTRL> DI float dppf(float v) { return __builtin_bit_cast(float, __builtin_amdgcn_update_dpp(0, __builtin_bit_cast(int, v), CTRL, 0xf, 0xf, true)); }
DI float sum8(float v) { v += dppf<0xB1>(v); v += dppf<0x4E>(v); v += dppf<0x141>(v); return v; }
DI f32x16 splat16(float v) { f32x16 r; for (int i = 0; i < 16; ++i) r[i] = v; return r; }

#define ST_DECL
#define ST(i)
#define ST_END(kid)
#define DBG_PARAM
#define DBG_ARG

constexpr float QSCALE = 0.08838834764831845f * 1.4426950408889634f;
constexpr int PRE_RQ = 288, PRE_MOD = 36, PRE_LN = 64, PRE_MISC = 16;
constexpr int PRE_LDS = 16384 + 1024 + 256;
DI float silu_bf(unsigned short u) { const float cv = __uint_as_float((unsigned)u << 16); return cv * __builtin_amdgcn_rcpf(1.f + __builtin_amdgcn_exp2f(-1.4426950408889634f * cv)); }
DI void mod_partial(const unsigned short* __restrict__ c, const unsigned short* __restrict__ wmod, int col0, int rg, int lane, int wid, float* red) {
  u32x4 w[18]; float s[18];
#pragma unroll
  for (int i = 0; i < 18; ++i) w[i] = *(const u32x4*)(wmod + (size_t)(rg + 32 * i) * 3456 + col0);
#pragma unroll
  for (int i = 0; i < 18; ++i) s[i] = silu_bf(c[rg + 32 * i]);
  float acc[8] = {};
#pragma unroll
  for (int i = 0; i < 18; ++i)
#pragma unroll
    for (int j = 0; j < 4; ++j) { acc[2 * j] += s[i] * bflo(w[i][j]); acc[2 * j + 1] += s[i] * bfhi(w[i][j]); }
#pragma unroll
  for (int j = 0; j < 8; ++j) { acc[j] += __shfl_xor(acc[j], 8); acc[j] += __shfl_xor(acc[j], 16); acc[j] += __shfl_xor(acc[j], 32); }
  if (lane < 8) {
#pragma unroll
    for (int j = 0; j < 8; ++j) red[wid * 64 + lane * 8 + j] = acc[j];
  }
}
__global__ __launch_bounds__(256) void k_pre(const unsigned short* __restrict__ c, const unsigned short* __restrict__ wmod, const unsigned short* __restrict__ xin,
                                             const char* __restrict__ wq, const char* __restrict__ kc, const char* __restrict__ vc, char* __restrict__ ws DBG_PARAM) {
  ST_DECL
  extern __shared__ __attribute__((aligned(16))) char lds_[];
  LAS char* lds = (LAS char*)lds_;
  const int tid = threadIdx.x, lane = tid & 63, wid = rfl(tid >> 6);
  int b = blockIdx.x;
  if (b < PRE_RQ) {
    float* red = (float*)(lds_ + 16384); float* tab = (float*)(lds_ + 16384 + 1024);
    const int s32 = b >> 4, cg0 = (b & 15) * 4 + wid;
    { const int rr = lane >> 3, pp = lane & 7;
      const unsigned v = rr * 8192 + ((((pp >> 1) ^ ((rr >> 1) & 3)) << 1) + (pp & 1)) * 16;
      const char* src = wq + (size_t)s32 * 32 * 8192 + cg0 * 128; const unsigned m = wid * 4096;
      dma16<0>(m, v, src); dma16<0>(m + 1024, v, src + 8 * 8192); dma16<0>(m + 2048, v, src + 16 * 8192); dma16<0>(m + 3072, v, src + 24 * 8192); }
    const int cc = tid & 7;
    ST(1);
    mod_partial(c, wmod, (cc < 4 ? 0 : 576) + 32 * s32 + (cc & 3) * 8, tid >> 3, lane, wid, red);
    ST(2);
    __syncthreads();
    if (tid < 64) { const float t = (red[tid] + red[64 + tid]) + (red[128 + tid] + red[192 + tid]); tab[tid] = (tid < 32 ? t : 1.f + t) * QSCALE; }
    wait_vm<0>();
    __syncthreads();
    ST(3);
    const int g = lane >> 4, q = (lane & 15) >> 2, p = lane & 3, h = lane >> 5, blk = g & 1, pc = ((p & 1) << 1) | (p >> 1);
    f32x4 sh[2][2], sc[2][2];
#pragma unroll
    for (int ks = 0; ks < 2; ++ks) { sh[ks][0] = *(const f32x4*)(tab + 16 * ks + 8 * h); sh[ks][1] = *(const f32x4*)(tab + 16 * ks + 8 * h + 4);
                                     sc[ks][0] = *(const f32x4*)(tab + 32 + 16 * ks + 8 * h); sc[ks][1] = *(const f32x4*)(tab + 32 + 16 * ks + 8 * h + 4); }
    const int i32 = lane & 31, dsw = (i32 & 19) | ((i32 & 4) << 1) | ((i32 & 8) >> 1);
    { const int cg = cg0; LAS char* scr = lds + wid * 4096;
#pragma unroll
      for (int dbl = 0; dbl < 2; ++dbl) { float bias = 0.f;
#pragma unroll
        for (int ks = 0; ks < 2; ++ks) {
          const int r0 = 16 * ks + 8 * h + q, r1 = r0 + 4, cx = 2 * dbl + blk;
          const u32x4 w = __builtin_bit_cast(u32x4, cat(tr4(scr + 128 * r0 + 32 * (cx ^ ((r0 >> 1) & 3)) + 8 * pc), tr4(scr + 128 * r1 + 32 * (cx ^ ((r1 >> 1) & 3)) + 8 * pc)));
          u32x4 o;
#pragma unroll
          for (int jj = 0; jj < 4; ++jj) { const float lo = bflo(w[jj]), hi = bfhi(w[jj]); const int e = (2 * jj) & 3;
            o[jj] = pk2(lo * sc[ks][jj >> 1][e], hi * sc[ks][jj >> 1][e + 1]); bias += lo * sh[ks][jj >> 1][e] + hi * sh[ks][jj >> 1][e + 1]; }
          *(u32x4*)(ws + WS_WQ + (size_t)(((cg >> 1) * 36 + 2 * s32 + ks) * 4 + 2 * (cg & 1) + dbl) * 1024 + lane * 16) = o;
        }
        bias += __shfl_xor(bias, 32);
        if (h == 0) ((float*)(ws + WS_BP))[s32 * 4096 + cg * 64 + 32 * dbl + dsw] = bias;
      } }
    ST_END(4);
    return;
  }
  b -= PRE_RQ;
  if (b < PRE_MOD) {
    float* red = (float*)lds_;
    mod_partial(c, wmod, 1152 + b * 64 + (tid & 7) * 8, tid >> 3, lane, wid, red);
    __syncthreads();
    if (tid < 64) ((float*)(ws + WS_MOD))[1152 + b * 64 + tid] = (red[tid] + red[64 + tid]) + (red[128 + tid] + red[192 + tid]);
    ST_END(5);
    return;
  }
  b -= PRE_MOD;
  if (b < PRE_LN) {
    const int cc = lane & 7, rr = lane >> 3, row = (b * 4 + wid) * 8 + rr;
    const unsigned short* x = xin + (size_t)row * D_;
    float v[9][8];
#pragma unroll
    for (int i = 0; i < 9; ++i) { const u32x4 a = *(const u32x4*)(x + (cc + 8 * i) * 8);
#pragma unroll
      for (int j = 0; j < 4; ++j) { v[i][2 * j] = bflo(a[j]); v[i][2 * j + 1] = bfhi(a[j]); } }
    float s = 0.f;
#pragma unroll
    for (int i = 0; i < 9; ++i)
#pragma unroll
      for (int j = 0; j < 8; ++j) s += v[i][j];
    s = sum8(s);
    const float mean = s * (1.f / 576.f);
    float q = 0.f;
#pragma unroll
    for (int i = 0; i < 9; ++i)
#pragma unroll
      for (int j = 0; j < 8; ++j) { const float d = v[i][j] - mean; q += d * d; }
    q = sum8(q);
    const float rstd = 1.f / sqrtf(q * (1.f / 576.f) + 1e-5f);
    char* ob = ws + WS_H1 + (size_t)(row >> 5) * 36 * 1024 + (row & 31) * 16;
#pragma unroll
    for (int i = 0; i < 9; ++i) { const int ch = cc + 8 * i;
      u32x4 w = {pk2((v[i][0] - mean) * rstd, (v[i][1] - mean) * rstd), pk2((v[i][2] - mean) * rstd, (v[i][3] - mean) * rstd),
                 pk2((v[i][4] - mean) * rstd, (v[i][5] - mean) * rstd), pk2((v[i][6] - mean) * rstd, (v[i][7] - mean) * rstd)};
      *(u32x4*)(ob + (ch >> 1) * 1024 + (ch & 1) * 512) = w; }
    ST_END(6);
    return;
  }
  b -= PRE_LN;
  {
    const int t = b * 256 + tid;
    for (int i = t; i < 9360; i += PRE_MISC * 256) *(u32x4*)(ws + WS_STAT + (size_t)i * 16) = (u32x4){~0u, ~0u, ~0u, ~0u};
    unsigned acc = 0;
    const size_t o = ((size_t)b * SK + (SK - WIN)) * 256;
#pragma unroll
    for (int i = 0; i < 4; ++i) { acc += *(const unsigned*)(kc + o + (size_t)(i * 256 + tid) * 128); acc += *(const unsigned*)(vc + o + (size_t)(i * 256 + tid) * 128); }
    asm volatile("" ::"v"(acc));
    ST_END(7);
  }
}

constexpr float THR = 8.0f;
constexpr int A_TAB = 131072;
constexpr int A_LDS = 131072 + 2048;
#define A_LA 4
#define Q_LA 5
struct AttnArgs { const char* kc; const char* vc; const char* wg; const char* wu; const char* wo; const char* wd; char* ws; };
__global__ __launch_bounds__(512, 2) void k_attn(AttnArgs ar DBG_PARAM) {
  ST_DECL
  extern __shared__ __attribute__((aligned(16))) char lds_[];
  LAS char* lds = (LAS char*)lds_;
  char* const ws = ar.ws;
  const int tid = threadIdx.x, lane = tid & 63;
  const int wid = rfl(tid >> 6);
  const int x = blockIdx.x & 7, j = blockIdx.x >> 3, head = 4 * x + (j & 3), rt = j >> 2, kvh = head >> 1;
  const int rb = rt * 8 + wid;
  const int h = lane >> 5, r = lane & 31, blk = (lane >> 4) & 1, q = (lane & 15) >> 2, p = lane & 3;
  const int l4 = lane >> 4, pos = lane & 15;
  unsigned ksrc2[2];
#pragma unroll
  for (int e = 0; e < 2; ++e) { const int n = 2 * wid + e, row = 4 * n + l4, ch = pos ^ ((l4 << 2) | (n & 3)); ksrc2[e] = row * 256 + ch * 16 - e * 1024; }
  const char* wq_h = ws + WS_WQ + (size_t)head * 36 * 4096 + wid * 2048;
  const char* kb_ = ar.kc + ((size_t)kvh * SK + (SK - WIN)) * 256;
  const char* vb_ = ar.vc + ((size_t)kvh * SK + (SK - WIN)) * 256;
  const char* h1w = ws + WS_H1 + (size_t)rb * 36 * 1024;
  const unsigned lane16 = lane * 16;
  auto dmaW = [&](int qq, int e) { const unsigned m = 65536 + (qq & 3) * 16384 + wid * 2048; if (e == 0) dma16<0>(m, lane16, wq_h + qq * 16384); else dma16<1024>(m, lane16, wq_h + qq * 16384); };
  auto dmaK8 = [&](int t) { const unsigned m = (t & 3) * 16384 + wid * 2048; const char* s = kb_ + t * 16384; dma16<0>(m, ksrc2[0], s); dma16<1024>(m, ksrc2[1], s); };
  auto dmaV8 = [&](int t) { const unsigned m = 65536 + ((t + 1) & 3) * 16384 + wid * 2048; const char* s = vb_ + t * 16384; dma16<0>(m, ksrc2[0], s); dma16<1024>(m, ksrc2[1], s); };
  bf16x8 g[9][4];
  auto loadG1 = [&](auto QQ, auto E) { constexpr int qq = QQ, e = E; gload16<e * 1024>(g[qq][e], lane16, h1w + qq * 4096); };
  auto loadG = [&](auto QQ) { static_for<4>([&](auto E) { loadG1(QQ, E); }); };
  float tb5[5];
  { const int part = tid >> 7; const unsigned to = (unsigned)(head * 128 + (tid & 127)) * 4;
#pragma unroll
    for (int i = 0; i < 5; ++i) { const int sidx = part + 4 * i < 18 ? part + 4 * i : part; asm volatile("global_load_dword %0, %1, %2" : "=v"(tb5[i]) : "v"(to + sidx * 16384), "s"(ws + WS_BP)); } }
  dmaW(0, 0); dmaW(0, 1); loadG(integral_constant<int, 0>{});
  bar();
  dmaW(1, 0); dmaW(1, 1); loadG(integral_constant<int, 1>{}); dmaW(2, 0); dmaW(2, 1); loadG(integral_constant<int, 2>{}); dmaK8(0);
  wait_vm<20>();
#pragma unroll
  for (int i = 0; i < 5; ++i) pin(tb5[i]);
  *(LAS float*)(lds + A_TAB + tid * 4) = (tb5[0] + tb5[1]) + (tb5[2] + tb5[3]) + ((tid >> 7) < 2 ? tb5[4] : 0.f);
  unsigned wb0 = 65536 + lane16;
  pin(wb0);
  f32x16 acc[4] = {};
  {
    constexpr int LA = Q_LA, NIT = 9 * 16;
    bf16x8 wf[LA + 1];
    auto rd = [&](auto I) { constexpr int i = I; constexpr int qq = i / 16, ks = (i % 16) / 4, db = i % 4;
      wf[i % (LA + 1)] = *(LAS bf16x8*)(lds + wb0 + (qq & 3) * 16384 + ks * 4096 + db * 1024); };
    SB(); vm_lgkm_bar<12>();
    static_for<4>([&](auto E) { pin(g[0][E]); });
    SB();
    ST(1);
    static_for<LA>([&](auto I) { rd(I); });
    SB();
    static_for<NIT>([&](auto I) { constexpr int i = I; constexpr int qq = i / 16, rr = i % 16, ks = rr / 4, db = rr % 4;
      if constexpr (rr == 0 && qq > 0) {
        constexpr int NW[9] = {12, 12, 10, 10, 10, 10, 10, 8, 8};
        vm_bar<NW[qq]>();
        static_for<4>([&](auto E) { pin(g[qq][E]); });
        if constexpr (qq == 4) ST(2);
        SB();
      }
      if constexpr (i + LA < NIT) rd(integral_constant<int, i + LA>{});
      acc[db] = mfma32(wf[i % (LA + 1)], g[qq][ks], acc[db]);
      if constexpr (qq + 3 < 9) {
        if constexpr (rr == 1) dmaW(qq + 3, 0);
        if constexpr (rr == 3) dmaW(qq + 3, 1);
        if constexpr (rr >= 5 && rr <= 11 && (rr & 1)) loadG1(integral_constant<int, qq + 3>{}, integral_constant<int, (rr - 5) / 2>{});
      } else {
        if constexpr (rr == 1) dmaV8(qq - 6);
        if constexpr (rr == 7) dmaK8(qq - 5);
      }
      SB();
    });
    const int dsw = (r & 19) | ((r & 4) << 1) | ((r & 8) >> 1);
    const u32x4 one = {h == 0 ? 0x3f803f80u : 0u, 0u, 0u, 0u};
#pragma unroll
    for (int db = 0; db < 4; ++db) {
      const LAS float* tp = (const LAS float*)(lds + A_TAB) + 32 * db + dsw;
      const float bv = (tp[0] + tp[128]) + (tp[256] + tp[384]);
      const unsigned hi = pk2(bv, 0.f) & 0xffffu; const float rem = bv - bflo(hi);
      const u32x4 aw = {h == 0 ? (hi | (pk2(rem, 0.f) << 16)) : 0u, 0u, 0u, 0u};
      acc[db] = mfma32(__builtin_bit_cast(bf16x8, aw), __builtin_bit_cast(bf16x8, one), acc[db]);
    }
  }
  ST(3);
  bf16x8 qf[8];
#pragma unroll
  for (int db = 0; db < 4; ++db)
#pragma unroll
    for (int s = 0; s < 2; ++s) { u32x4 w;
#pragma unroll
      for (int i = 0; i < 4; ++i) w[i] = pk2(acc[db][8 * s + 2 * i], acc[db][8 * s + 2 * i + 1]);
      qf[2 * db + s] = __builtin_bit_cast(bf16x8, w); }
  const unsigned kb0 = 256 * r + 16 * (h ^ (((r & 3) << 2) | ((r >> 2) & 3)));
  const unsigned vb0 = 65536 + 256 * (4 * h + q) + 16 * ((q << 2) | ((2 * blk + (p >> 1)) ^ h)) + 8 * (p & 1);
  unsigned ka[8], va[4][2];
#pragma unroll
  for (int s = 0; s < 8; ++s) ka[s] = kb0 ^ (s << 5);
#pragma unroll
  for (int db = 0; db < 4; ++db) { va[db][0] = vb0 ^ (db << 6); va[db][1] = vb0 ^ (db << 6) ^ 32; }
  f32x16 o[4] = {}; f32x16 S[2][2], negm; unsigned pw[16]; float m; f32x2 l2 = {0.f, 0.f};
  constexpr int LA = A_LA;
  bf16x8 fr[LA + 1];
  struct Map { int g[272]; int n; };
  constexpr Map MP = []() constexpr { Map mp{}; mp.n = 0; for (int G = 0; G < 9 * 32; ++G) { const int t = G / 32 - 1, i = G % 32; if (i < 16 ? (t + 1 <= 7) : (t >= 0 && t <= 7)) mp.g[mp.n++] = G; } return mp; }();
  constexpr int NIT = MP.n;
  auto rdA = [&](auto C) { constexpr int c = C; constexpr int G = MP.g[c], t = G / 32 - 1, i = G % 32;
    if constexpr (i < 16) { constexpr int s = i >> 1, kb = i & 1; fr[c % (LA + 1)] = *(LAS bf16x8*)(lds + ka[s] + ((t + 1) & 3) * 16384 + kb * 8192); }
    else { constexpr int ii = i - 16, kk = ii >> 2, db = ii & 3; constexpr int so = ((t + 1) & 3) * 16384 + kk * 4096;
      fr[c % (LA + 1)] = cat(tr4(lds + va[db][0] + so), tr4(lds + va[db][1] + so + 2048)); } };
  auto rowmax_finish = [&](float a) { auto rr = __builtin_amdgcn_permlane32_swap(__float_as_uint(a), __float_as_uint(a), false, false); return max2f(__uint_as_float(rr[0]), __uint_as_float(rr[1])); };
  float mxa, mxb;
  unsigned pfd[4];
  const unsigned gw = blockIdx.x * 8 + wid;
  const unsigned pfo0 = (gw * 18 + (lane < 18 ? lane : 0)) * 128, pfo1_ = (gw * 21 + (lane < 21 ? lane : 0)) * 128, pfo1 = pfo1_ < 5308416 - 128 ? pfo1_ : 5308416 - 128;
  SB(); vm_bar<8>(); SB();
  ST(4);
  static_for<LA>([&](auto C) { rdA(C); });
  SB();
  static_for<NIT>([&](auto C) { constexpr int c = C; constexpr int G = MP.g[c], t = G / 32 - 1, i = G % 32;
    constexpr bool firstX = (i == 0) || (t == 7 && i == 16);
    if constexpr (firstX && t >= 0) {
      constexpr int NW[8] = {4, 5, 5, 5, 5, 2, 0, 0};
      float a = rowmax_finish(max2f(mxa, mxb));
      if constexpr (t == 0) {
        m = a; negm = splat16(-a);
#pragma unroll
        for (int r2 = 0; r2 < 16; ++r2) { S[0][0][r2] -= a; S[0][1][r2] -= a; }
      } else if (__builtin_expect(__any(a > THR), 0)) {
        const float dl = fmaxf(a, 0.f), f = __builtin_amdgcn_exp2f(-dl);
        m += dl; negm = splat16(-m); l2 *= f;
#pragma unroll
        for (int r2 = 0; r2 < 16; ++r2) { S[t & 1][0][r2] -= dl; S[t & 1][1][r2] -= dl; }
#pragma unroll
        for (int d = 0; d < 4; ++d)
#pragma unroll
          for (int r2 = 0; r2 < 16; ++r2) o[d][r2] *= f;
      }
      SB(); vm_bar<NW[t]>(); SB();
      if constexpr (t == 3) ST(5);
      if constexpr (t == 4) ST(7);
      if constexpr (t == 5) ST(8);
      if constexpr (t == 7) {
#pragma unroll
        for (int e = 0; e < 32; ++e) S[1][e >> 4][e & 15] = __builtin_amdgcn_exp2f(S[1][e >> 4][e & 15]);
#pragma unroll
        for (int e = 0; e < 16; ++e) pw[e] = pk2(S[1][e >> 3][(2 * e) & 15], S[1][e >> 3][((2 * e) & 15) + 1]);
        SB();
      }
    }
    if constexpr (c + LA < NIT) rdA(integral_constant<int, c + LA>{});
    if constexpr (i < 16) {
      constexpr int s = i >> 1, kb = i & 1, X = (t + 1) & 1;
      if constexpr (s == 0) { if constexpr (t < 0) S[X][kb] = mfma32(fr[c % (LA + 1)], qf[s], f32x16{}); else S[X][kb] = mfma32(fr[c % (LA + 1)], qf[s], negm); }
      else S[X][kb] = mfma32(fr[c % (LA + 1)], qf[s], S[X][kb]);
      if constexpr (t >= 0) {
        constexpr int e0 = 2 * i, e1 = 2 * i + 1, Y = t & 1;
        S[Y][e0 >> 4][e0 & 15] = __builtin_amdgcn_exp2f(S[Y][e0 >> 4][e0 & 15]);
        S[Y][e1 >> 4][e1 & 15] = __builtin_amdgcn_exp2f(S[Y][e1 >> 4][e1 & 15]);
        if constexpr (i >= 1) { constexpr int e = i - 1; pw[e] = pk2(S[Y][e >> 3][(2 * e) & 15], S[Y][e >> 3][((2 * e) & 15) + 1]); }
        if constexpr (i == 15) { SB(); pw[15] = pk2(S[Y][1][14], S[Y][1][15]); }
        if constexpr (t <= 3 && i == 1) asm volatile("global_load_dword %0, %1, %2" : "=v"(pfd[t]) : "v"(t == 0 ? pfo0 : pfo1), "s"(t == 0 ? ar.wo : (t == 1 ? ar.wg : (t == 2 ? ar.wu : ar.wd))));
        if constexpr (t <= 3 && i == 6) dma16<0>((t & 3) * 16384 + wid * 2048, ksrc2[0], kb_ + (t + 4) * 16384);
        if constexpr (t <= 3 && i == 9) dma16<1024>((t & 3) * 16384 + wid * 2048, ksrc2[1], kb_ + (t + 4) * 16384);
        if constexpr (t <= 4 && i == 12) dma16<0>(65536 + (t & 3) * 16384 + wid * 2048, ksrc2[0], vb_ + (t + 3) * 16384);
        if constexpr (t <= 4 && i == 15) dma16<1024>(65536 + (t & 3) * 16384 + wid * 2048, ksrc2[1], vb_ + (t + 3) * 16384);
      }
      if constexpr (t < 0 && i == 15) {
        asm volatile("s_nop 11" : "+v"(S[0][0]), "+v"(S[0][1]));
        mxa = max3f(S[0][0][0], S[0][0][1], S[0][0][2]); mxb = max3f(S[0][1][0], S[0][1][1], S[0][1][2]);
#pragma unroll
        for (int r2 = 3; r2 < 15; r2 += 2) { mxa = max3f(mxa, S[0][0][r2], S[0][0][r2 + 1]); mxb = max3f(mxb, S[0][1][r2], S[0][1][r2 + 1]); }
        mxa = max3f(mxa, S[0][0][15], S[0][1][15]);
      }
    } else {
      constexpr int ii = i - 16, kk = ii >> 2, db = ii & 3, Y = t & 1, X = (t + 1) & 1;
      const u32x4 w = {pw[4 * kk], pw[4 * kk + 1], pw[4 * kk + 2], pw[4 * kk + 3]};
      o[db] = mfma32(fr[c % (LA + 1)], __builtin_bit_cast(bf16x8, w), o[db]);
      l2 += (f32x2){S[Y][ii >> 3][(2 * ii) & 15], S[Y][ii >> 3][((2 * ii) & 15) + 1]};
      if constexpr (t < 7) {
        if constexpr (ii == 4) { mxa = max3f(S[X][0][0], S[X][0][1], S[X][0][2]); mxb = max3f(S[X][0][3], S[X][0][4], S[X][0][5]); }
        if constexpr (ii == 5) { mxa = max3f(mxa, S[X][0][6], S[X][0][7]); mxb = max3f(mxb, S[X][0][8], S[X][0][9]); }
        if constexpr (ii == 6) { mxa = max3f(mxa, S[X][0][10], S[X][0][11]); mxb = max3f(mxb, S[X][0][12], S[X][0][13]); }
        if constexpr (ii == 7) { mxa = max3f(mxa, S[X][0][14], S[X][0][15]); mxb = max3f(mxb, S[X][1][0], S[X][1][1]); }
        if constexpr (ii == 8) { mxa = max3f(mxa, S[X][1][2], S[X][1][3]); mxb = max3f(mxb, S[X][1][4], S[X][1][5]); }
        if constexpr (ii == 9) { mxa = max3f(mxa, S[X][1][6], S[X][1][7]); mxb = max3f(mxb, S[X][1][8], S[X][1][9]); }
        if constexpr (ii == 10) { mxa = max3f(mxa, S[X][1][10], S[X][1][11]); mxb = max3f(mxb, S[X][1][12], S[X][1][13]); }
        if constexpr (ii == 11) { mxa = max3f(mxa, S[X][1][14], S[X][1][15]); }
      }
    }
    SB();
  });
  asm volatile("" ::"v"(pfd[0]), "v"(pfd[1]), "v"(pfd[2]), "v"(pfd[3]));
  ST(12);
  float l = l2[0] + l2[1];
  { auto rr = __builtin_amdgcn_permlane32_swap(__float_as_uint(l), __float_as_uint(l), false, false); l = __uint_as_float(rr[0]) + __uint_as_float(rr[1]); }
  const float inv = 1.f / l;
  char* ob = ws + WS_O + ((size_t)rb * 256 + head * 8) * 1024 + lane16;
#pragma unroll
  for (int db = 0; db < 4; ++db)
#pragma unroll
    for (int s = 0; s < 2; ++s) { u32x4 w;
#pragma unroll
      for (int i = 0; i < 4; ++i) w[i] = pk2(o[db][8 * s + 2 * i] * inv, o[db][8 * s + 2 * i + 1] * inv);
      *(u32x4*)(ob + (2 * db + s) * 1024) = w; }
  ST_END(0);
}

constexpr int SK_LDS = 81920 + 8192;
template <bool FINAL>
__global__ __launch_bounds__(256, 2) void k_skinny(char* __restrict__ ws, const char* __restrict__ W, const unsigned short* __restrict__ xin, unsigned short* __restrict__ out DBG_PARAM) {
  ST_DECL
  extern __shared__ __attribute__((aligned(16))) char lds_[];
  LAS char* lds = (LAS char*)lds_;
  constexpr int NS16 = FINAL ? 288 : 256, KS = NS16 / 8;
  const char* act = ws + (FINAL ? WS_H : WS_O);
  const float* mod = (const float*)(ws + WS_MOD);
  const int tid = threadIdx.x, lane = tid & 63, wid = rfl(tid >> 6);
  const int b = blockIdx.x, x = b & 7, jj = b >> 3;
  const int idx = x * 30 + jj;  if (idx >= 234) return;
  const int rt = idx / 9, ct = idx - rt * 9;
  const int g = lane >> 4, m16 = lane & 15, q4 = (lane & 15) >> 2, p = lane & 3;
  unsigned aoff[5];
#pragma unroll
  for (int a = 0; a < 5; ++a) { int row = rt * 80 + a * 16; if (row > M_ - 16) row = M_ - 16; row += m16;
    aoff[a] = (unsigned)(row >> 5) * (NS16 * 1024) + (row & 31) * 16 + (g >> 1) * 1024 + (g & 1) * 512; }
  const int cc = tid & 7, r8 = tid >> 3, n0 = ct * 64 + cc * 8;
  f32x4 gt0, gt1, sc0, sc1, sh0, sh1, rs0[3], rs1[3]; u32x4 rsb[3]; bool ok[3]; int rowg[3];
  gload16<0>(gt0, n0 * 4, mod + (FINAL ? 5 : 2) * D_); gload16<16>(gt1, n0 * 4, mod + (FINAL ? 5 : 2) * D_);
  if (!FINAL) { gload16<0>(sc0, n0 * 4, mod + 4 * D_); gload16<16>(sc1, n0 * 4, mod + 4 * D_); gload16<0>(sh0, n0 * 4, mod + 3 * D_); gload16<16>(sh1, n0 * 4, mod + 3 * D_); }
#pragma unroll
  for (int i = 0; i < 3; ++i) { const int rl = r8 + 32 * i; rowg[i] = rt * 80 + rl; ok[i] = rl < 80 && rowg[i] < M_; const int rr = ok[i] ? rowg[i] : 0;
    if (FINAL) { gload16<0>(rs0[i], (rr * D_ + n0) * 4, ws + WS_X1); gload16<16>(rs1[i], (rr * D_ + n0) * 4, ws + WS_X1); }
    else gload16<0>(rsb[i], (rr * D_ + n0) * 2, xin); }
  const char* actw = act + (size_t)wid * KS * 2048;
  const int rr_ = lane >> 3, pp = lane & 7;
  const unsigned wsrc = rr_ * 1152 + ((((pp >> 1) ^ ((rr_ >> 1) & 3)) << 1) + (pp & 1)) * 16;
  const char* Ww = W + (size_t)wid * KS * 32 * 1152 + ct * 128;
  const unsigned ring = wid * 16384, lane16 = lane * 16;
  bf16x8 af[4][5], wf[2][4];
  auto piece = [&](auto SL, auto E, const char* wsr, const char* as) { constexpr int sl = SL, e = E;
    if constexpr (e < 4) dma16<0>(ring + sl * 4096 + e * 1024, wsrc, wsr + e * 9216);
    else gload16<0>(af[sl][e - 4], aoff[e - 4], as); };
  static_for<3>([&](auto S) { constexpr int s = S; static_for<9>([&](auto E) { piece(S, E, Ww + (size_t)s * 32 * 1152, actw + s * 2048); }); });
  const unsigned xl = 2 * (g & 1) + (q4 >> 1);
  unsigned tb[4];
#pragma unroll
  for (int c = 0; c < 4; ++c) tb[c] = ring + 128 * (16 * (g >> 1) + 4 * (g & 1) + q4) + 32 * (c ^ xl) + 8 * p;
  f32x4 acc[5][4] = {};
  auto rdw = [&](auto SL, auto C) { constexpr int sl = SL, c = C; wf[sl & 1][c] = cat(tr4(lds + tb[c] + sl * 4096), tr4(lds + tb[c] + sl * 4096 + 1024)); };
  SB(); wait_vm<18>(); SB();
  ST(1);
  static_for<4>([&](auto C) { rdw(integral_constant<int, 0>{}, C); });
  const char* wp = Ww + (size_t)3 * 32 * 1152; const char* ap = actw + 3 * 2048;
  auto step = [&](auto SL, auto MORE_, auto NEXT_, auto OUT_) { constexpr int sl = SL; constexpr bool MORE = MORE_, NEXT = NEXT_; constexpr int OUT = OUT_;
#pragma unroll
    for (int a = 0; a < 5; ++a) pin(af[sl][a]);
    SB();
    static_for<20>([&](auto I) { constexpr int i = I, c = i / 5, a = i % 5;
      if constexpr (NEXT && i == 11) { wait_vm<OUT>(); }
      if constexpr (NEXT && i >= 11 && i < 19 && (i & 1)) rdw(integral_constant<int, (sl + 1) & 3>{}, integral_constant<int, (i - 11) / 2>{});
      acc[a][c] = mfma16(wf[sl & 1][c], af[sl][a], acc[a][c]);
      if constexpr (MORE && i < 9) piece(integral_constant<int, (sl + 3) & 3>{}, I, wp, ap);
      SB();
    });
    wp += 32 * 1152; ap += 2048;
  };
  using T = std::true_type; using F = std::false_type;
  for (int it = 0; it < KS / 4 - 1; ++it) {
    step(integral_constant<int, 0>{}, T{}, T{}, integral_constant<int, 18>{});
    step(integral_constant<int, 1>{}, T{}, T{}, integral_constant<int, 18>{});
    step(integral_constant<int, 2>{}, T{}, T{}, integral_constant<int, 18>{});
    step(integral_constant<int, 3>{}, T{}, T{}, integral_constant<int, 18>{});
  }
  step(integral_constant<int, 0>{}, T{}, T{}, integral_constant<int, 18>{});
  step(integral_constant<int, 1>{}, F{}, T{}, integral_constant<int, 9>{});
  step(integral_constant<int, 2>{}, F{}, T{}, integral_constant<int, 0>{});
  step(integral_constant<int, 3>{}, F{}, F{}, integral_constant<int, 0>{});
  ST(5);
  pin(gt0); pin(gt1);
  if (!FINAL) { pin(sc0); pin(sc1); pin(sh0); pin(sh1); }
#pragma unroll
  for (int i = 0; i < 3; ++i) { if (FINAL) { pin(rs0[i]); pin(rs1[i]); } else { pin(rsb[i]);
      rs0[i] = (f32x4){bflo(rsb[i][0]), bfhi(rsb[i][0]), bflo(rsb[i][1]), bfhi(rsb[i][1])}; rs1[i] = (f32x4){bflo(rsb[i][2]), bfhi(rsb[i][2]), bflo(rsb[i][3]), bfhi(rsb[i][3])}; } }
  bar();
#pragma unroll
  for (int a = 0; a < 5; ++a)
#pragma unroll
    for (int c = 0; c < 4; ++c) *(LAS f32x4*)(lds + wid * 20480 + (a * 4 + c) * 1024 + lane16) = acc[a][c];
  __syncthreads();
  ST(7);
  f32x4 v0[3], v1[3];
#pragma unroll
  for (int i = 0; i < 3; ++i) {
    int rl = r8 + 32 * i; if (rl >= 80) rl = r8;
    const unsigned o = ((rl >> 4) * 4 + (cc >> 1)) * 1024 + ((cc & 1) * 32 + (rl & 15)) * 16;
    f32x4 t0 = *(LAS f32x4*)(lds + o), t1 = *(LAS f32x4*)(lds + o + 256);
#pragma unroll
    for (int w = 1; w < 4; ++w) { t0 += *(LAS f32x4*)(lds + w * 20480 + o); t1 += *(LAS f32x4*)(lds + w * 20480 + o + 256); }
    v0[i] = rs0[i] + gt0 * t0; v1[i] = rs1[i] + gt1 * t1;
  }
  if (FINAL) {
#pragma unroll
    for (int i = 0; i < 3; ++i) if (ok[i]) {
      u32x4 w = {pk2(v0[i][0], v0[i][1]), pk2(v0[i][2], v0[i][3]), pk2(v1[i][0], v1[i][1]), pk2(v1[i][2], v1[i][3])};
      *(u32x4*)(out + (size_t)rowg[i] * D_ + n0) = w; }
  } else {
    LAS f32x2* mine = (LAS f32x2*)(lds + 81920);
    LAS f32x2* all9 = (LAS f32x2*)(lds + 81920 + 1024);
#pragma unroll
    for (int i = 0; i < 3; ++i) {
      float s = ((v0[i][0] + v0[i][1]) + (v0[i][2] + v0[i][3])) + ((v1[i][0] + v1[i][1]) + (v1[i][2] + v1[i][3]));
      s = sum8(s);
      const float mt = s * (1.f / 64.f);
      const f32x4 d0 = v0[i] - mt, d1 = v1[i] - mt;
      float q = ((d0[0] * d0[0] + d0[1] * d0[1]) + (d0[2] * d0[2] + d0[3] * d0[3])) + ((d1[0] * d1[0] + d1[1] * d1[1]) + (d1[2] * d1[2] + d1[3] * d1[3]));
      q = sum8(q);
      if (cc == 0 && r8 + 32 * i < 80) mine[r8 + 32 * i] = (f32x2){mt, q};
    }
    asm volatile("s_waitcnt lgkmcnt(0)\n\ts_barrier" ::: "memory");
    unsigned long long* slots = (unsigned long long*)(ws + WS_STAT) + (size_t)rt * 720;
    if (tid < 40) { const u32x4 w = *(LAS u32x4*)((LAS char*)mine + tid * 16);
      asm volatile("global_store_dwordx4 %0, %1, %2 sc1\n\ts_nop 1" ::"v"((unsigned)(ct * 640 + tid * 16)), "v"(w), "s"(slots) : "memory"); }
    ST(8);
    for (unsigned spin = 0; spin < (1u << 20); ++spin) {
      unsigned long long g0 = __hip_atomic_load(slots + tid, __ATOMIC_RELAXED, __HIP_MEMORY_SCOPE_AGENT), g1 = __hip_atomic_load(slots + 256 + tid, __ATOMIC_RELAXED, __HIP_MEMORY_SCOPE_AGENT),
                         g2 = __hip_atomic_load(slots + 512 + (tid < 208 ? tid : 0), __ATOMIC_RELAXED, __HIP_MEMORY_SCOPE_AGENT);
      const bool okk = (unsigned)(g0 >> 32) != ~0u && (unsigned)(g1 >> 32) != ~0u && (unsigned)(g2 >> 32) != ~0u;
      if (__all(okk) || spin == (1u << 20) - 1) { ((LAS unsigned long long*)all9)[tid] = g0; ((LAS unsigned long long*)all9)[256 + tid] = g1; if (tid < 208) ((LAS unsigned long long*)all9)[512 + tid] = g2; break; }
      __builtin_amdgcn_s_sleep(1);
    }
    asm volatile("s_waitcnt lgkmcnt(0)\n\ts_barrier" ::: "memory");
    ST(9);
#pragma unroll
    for (int i = 0; i < 3; ++i) {
      const int rl = r8 + 32 * i < 80 ? r8 + 32 * i : r8;
      const f32x2 pa = all9[cc * 80 + rl], pb = all9[8 * 80 + rl];
      const float mean = (sum8(pa[0]) + pb[0]) * (1.f / 9.f);
      const float q = sum8(pa[1] + 64.f * (pa[0] - mean) * (pa[0] - mean)) + pb[1] + 64.f * (pb[0] - mean) * (pb[0] - mean);
      const float rstd = 1.f / sqrtf(q * (1.f / 576.f) + 1e-5f);
      const f32x4 h0 = (v0[i] - mean) * rstd * (sc0 + 1.f) + sh0, h1 = (v1[i] - mean) * rstd * (sc1 + 1.f) + sh1;
      if (ok[i]) { const int kc = ct * 8 + cc;
        u32x4 w = {pk2(h0[0], h0[1]), pk2(h0[2], h0[3]), pk2(h1[0], h1[1]), pk2(h1[2], h1[3])};
        *(u32x4*)(ws + WS_H2 + ((size_t)(rowg[i] >> 5) * 36 + (kc >> 1)) * 1024 + ((kc & 1) * 32 + (rowg[i] & 31)) * 16) = w;
        float* xp = (float*)(ws + WS_X1) + (size_t)rowg[i] * D_ + n0; *(f32x4*)xp = v0[i]; *(f32x4*)(xp + 4) = v1[i]; }
    }
  }
  ST_END(FINAL ? 3 : 1);
}

constexpr int C_STG = 9216 + 128 + 9216, C_UOFF = 9216 + 128, C_NSTG = 4, C_LDS = C_STG * C_NSTG;
DI float geglu(float g, float u) {
  const float c0 = -2.f * 0.7978845608028654f * 1.4426950408889634f, c1 = c0 * 0.044715f;
  const float e = __builtin_amdgcn_exp2f(g * (c0 + c1 * g * g));
  return g * __builtin_amdgcn_rcpf(1.f + e) * u;
}
#define C_LA 6
__global__ __launch_bounds__(512, 2) void k_ffn1(char* __restrict__ ws, const char* __restrict__ wg, const char* __restrict__ wu DBG_PARAM) {
  ST_DECL
  extern __shared__ __attribute__((aligned(16))) char lds_[];
  LAS char* lds = (LAS char*)lds_;
  const int tid = threadIdx.x, lane = tid & 63, wid = rfl(tid >> 6);
  const int x = blockIdx.x & 7, j = blockIdx.x >> 3, ct = 4 * x + (j & 3), rt = j >> 2;
  const int rb = rt * 8 + wid;
  const int h = lane >> 5, blk = (lane >> 4) & 1, q = (lane & 15) >> 2, p = lane & 3;
  unsigned wsrc3[3];
#pragma unroll
  for (int e = 0; e < 3; ++e) { const int o = 64 * e + lane, row = o / 18, ch = o - row * 18; wsrc3[e] = row * (FF * 2) + ch * 16; }
  const char* Ww = (wid < 4 ? wg : wu) + (size_t)(wid & 3) * 8 * (FF * 2) + ct * 288;
  const unsigned wdst = (wid < 4 ? 0 : C_UOFF) + (wid & 3) * 2304;
  const char* hw = ws + WS_H2 + (size_t)rb * 36 * 1024;
  const unsigned lane16 = lane * 16;
  bf16x8 af[18][2];
  auto issue_piece = [&](auto S, auto E) { constexpr int s = S, e = E;
    const char* wsrc = Ww + (size_t)s * 32 * (FF * 2); const unsigned m = (s % C_NSTG) * C_STG + wdst;
    if constexpr (e == 0) dma16<0>(m, wsrc3[0], wsrc);
    if constexpr (e == 1) dma16<0>(m + 1024, wsrc3[1], wsrc);
    if constexpr (e == 2) { if (lane < 16) dma16<0>(m + 2048, wsrc3[2], wsrc); }
    if constexpr (e == 3) gload16<0>(af[s][0], lane16, hw + s * 2048);
    if constexpr (e == 4) gload16<1024>(af[s][1], lane16, hw + s * 2048); };
  static_for<5>([&](auto E) { issue_piece(integral_constant<int, 0>{}, E); });
  bar();
  static_for<5>([&](auto E) { issue_piece(integral_constant<int, 1>{}, E); });
  static_for<5>([&](auto E) { issue_piece(integral_constant<int, 2>{}, E); });
  unsigned tb[C_NSTG];
#pragma unroll
  for (int i = 0; i < C_NSTG; ++i) tb[i] = blk * C_UOFF + 288 * (8 * h + q) + 8 * p + i * C_STG;
  f32x16 acc[9] = {};
  constexpr int LA = C_LA, NIT = 18 * 18;
  bf16x8 wf[LA + 1];
  auto rd = [&](auto I) { constexpr int i = I; constexpr int s = i / 18, ks = (i % 18) / 9, f = i % 9;
    const bf16x4 lo = tr4(lds + tb[s % C_NSTG] + 288 * 16 * ks + 32 * f), hi = tr4(lds + tb[s % C_NSTG] + 288 * (16 * ks + 4) + 32 * f);
    wf[i % (LA + 1)] = cat(lo, hi); };
  SB(); vm_bar<5>(); pin(af[0][0]); pin(af[0][1]); SB();
  ST(1);
  static_for<LA>([&](auto I) { rd(I); });
  SB();
  static_for<NIT>([&](auto I) { constexpr int i = I; constexpr int s = i / 18, r = i % 18, ks = r / 9, f = r % 9;
    if constexpr (r == 0 && s > 0) {
      constexpr int outst = (s + 2 < 18) ? 5 : 0;
      vm_bar<outst>(); pin(af[s][0]); pin(af[s][1]);
      if constexpr (s == 4) ST(2);
      if constexpr (s == 8) ST(3);
      if constexpr (s == 12) ST(4);
      if constexpr (s == 16) ST(5);
      SB();
    }
    if constexpr (i + LA < NIT) rd(integral_constant<int, i + LA>{});
    acc[f] = mfma32(wf[i % (LA + 1)], af[s][ks], acc[f]);
    if constexpr (s + 3 < 18 && (r % 3) == 1 && r / 3 < 5) issue_piece(integral_constant<int, s + 3>{}, integral_constant<int, r / 3>{});
    SB();
  });
  ST(6);
  char* ob = ws + WS_H + ((size_t)rb * 288 + ct * 9) * 1024 + lane16;
#pragma unroll
  for (int f = 0; f < 9; ++f) { u32x4 w;
#pragma unroll
    for (int i = 0; i < 4; ++i) w[i] = pk2(geglu(acc[f][2 * i], acc[f][8 + 2 * i]), geglu(acc[f][2 * i + 1], acc[f][8 + 2 * i + 1]));
    *(u32x4*)(ob + f * 1024) = w; }
  ST_END(2);
}

extern "C" void kernel_launch(void* const* d_in, const int* in_sizes, int n_in, void* d_out, int out_size, void* d_ws, size_t ws_size, hipStream_t stream) {
  const unsigned short* x = (const unsigned short*)d_in[0];
  const unsigned short* c = (const unsigned short*)d_in[9]; const unsigned short* wmod = (const unsigned short*)d_in[10];
  char* ws = (char*)d_ws;
  (void)hipFuncSetAttribute((const void*)k_pre, hipFuncAttributeMaxDynamicSharedMemorySize, PRE_LDS);
  (void)hipFuncSetAttribute((const void*)k_attn, hipFuncAttributeMaxDynamicSharedMemorySize, A_LDS);
  (void)hipFuncSetAttribute((const void*)k_skinny<false>, hipFuncAttributeMaxDynamicSharedMemorySize, SK_LDS);
  (void)hipFuncSetAttribute((const void*)k_skinny<true>, hipFuncAttributeMaxDynamicSharedMemorySize, SK_LDS);
  (void)hipFuncSetAttribute((const void*)k_ffn1, hipFuncAttributeMaxDynamicSharedMemorySize, C_LDS);
  const char* kc = (const char*)d_in[1]; const char* vc = (const char*)d_in[2];
  const char* wq = (const char*)d_in[3]; const char* wo = (const char*)d_in[4];
  const char* wg = (const char*)d_in[6]; const char* wu = (const char*)d_in[7]; const char* wd = (const char*)d_in[8];
  k_pre<<<PRE_RQ + PRE_MOD + PRE_LN + PRE_MISC, 256, PRE_LDS, stream>>>(c, wmod, x, wq, kc, vc, ws DBG_ARG);
  AttnArgs ar{kc, vc, wg, wu, wo, wd, ws};
  k_attn<<<256, 512, A_LDS, stream>>>(ar DBG_ARG);
  k_skinny<false><<<240, 256, SK_LDS, stream>>>(ws, wo, x, (unsigned short*)d_out DBG_ARG);
  k_ffn1<<<256, 512, C_LDS, stream>>>(ws, wg, wu DBG_ARG);
  k_skinny<true><<<240, 256, SK_LDS, stream>>>(ws, wd, x, (unsigned short*)d_out DBG_ARG);
}
```

```cpp
#include <hip/hip_runtime.h>
#include <stdint.h>
#include <utility>

#define DI __device__ __forceinline__
#define LAS __attribute__((address_space(3)))
#define GAS __attribute__((address_space(1)))
typedef __bf16 bf16x8 __attribute__((ext_vector_type(8)));
typedef __bf16 bf16x4 __attribute__((ext_vector_type(4)));
typedef __bf16 bf16x2 __attribute__((ext_vector_type(2)));
typedef float f32x2 __attribute__((ext_vector_type(2)));
typedef float f32x4 __attribute__((ext_vector_type(4)));
typedef float f32x16 __attribute__((ext_vector_type(16)));
typedef unsigned u32x2 __attribute__((ext_vector_type(2)));
typedef unsigned u32x4 __attribute__((ext_vector_type(4)));
using std::integral_constant;

constexpr int M_ = 2048, D_ = 576, SK = 2048, WIN = 512, FF = 4608;

constexpr size_t WS_MOD = 0;
constexpr size_t WS_STAT = 16384;
constexpr size_t WS_H1 = WS_STAT + 149760 + 3840;
constexpr size_t WS_O = WS_H1 + (size_t)64 * 36 * 1024;
constexpr size_t WS_X1 = WS_O + (size_t)64 * 256 * 1024;
constexpr size_t WS_H2 = WS_X1 + (size_t)M_ * D_ * 4;
constexpr size_t WS_H = WS_H2 + (size_t)64 * 36 * 1024;
constexpr size_t WS_WQ = WS_H + (size_t)64 * 288 * 1024;
constexpr size_t WS_BP = WS_WQ + (size_t)32 * 36 * 4 * 1024;
constexpr size_t WS_CNT = WS_BP + (size_t)18 * 4096 * 4;
constexpr size_t WS_X1R = WS_CNT + 128 * 256;
constexpr size_t WS_PB = WS_X1R + (size_t)256 * 576 * 32;
constexpr size_t WS_PD = WS_PB + (size_t)16 * 16 * 16 * 2304;
constexpr size_t WS_END = WS_PD + (size_t)16 * 16 * 16 * 2304;

template <int... Is, class F> DI void static_for_impl(std::integer_sequence<int, Is...>, F&& f) { (f(std::integral_constant<int, Is>{}), ...); }
template <int N, class F> DI void static_for(F&& f) { static_for_impl(std::make_integer_sequence<int, N>{}, f); }

DI float bflo(unsigned w) { return __uint_as_float(w << 16); }
DI float bfhi(unsigned w) { return __uint_as_float(w & 0xffff0000u); }
DI unsigned pk2(float lo, float hi) { f32x2 v = {lo, hi}; bf16x2 b = __builtin_convertvector(v, bf16x2); return __builtin_bit_cast(unsigned, b); }
DI unsigned rfl(unsigned x) { return __builtin_amdgcn_readfirstlane(x); }
DI int lane_id() { int l; asm volatile("v_mbcnt_lo_u32_b32 %0, -1, 0\n\tv_mbcnt_hi_u32_b32 %0, -1, %0" : "=v"(l)); return l; }
DI void SB() { __builtin_amdgcn_sched_barrier(0); }

template <int IMM> DI void dma16(unsigned m0v, unsigned voff, const void* sbase) {
  asm volatile("s_mov_b32 m0, %0\n\ts_nop 0\n\tglobal_load_lds_dwordx4 %1, %2 offset:%3" ::"s"(m0v), "v"(voff), "s"(sbase), "i"(IMM) : "memory");
}
DI void dma4(unsigned m0v, unsigned voff, const void* sbase) {
  asm volatile("s_mov_b32 m0, %0\n\ts_nop 0\n\tglobal_load_lds_dword %1, %2" ::"s"(m0v), "v"(voff), "s"(sbase) : "memory");
}
template <int IMM, class T> DI void gload16(T& dst, unsigned voff, const void* sbase) {
  asm volatile("global_load_dwordx4 %0, %1, %2 offset:%3" : "=v"(dst) : "v"(voff), "s"(sbase), "i"(IMM));
}
#define LDF " sc1"
#define STF " sc1"
#define SC1L_S LDF
#define SC1S 1
template <int IMM, class T> DI void gload16c(T& dst, unsigned voff, const void* sbase) {
  asm volatile("global_load_dwordx4 %0, %1, %2 offset:%3" SC1L_S : "=v"(dst) : "v"(voff), "s"(sbase), "i"(IMM));
}
DI void st16c(char* base, unsigned voff, u32x4 v) {
  asm volatile("global_store_dwordx4 %0, %1, %2" STF "\n\ts_nop 1" ::"v"(voff), "v"(v), "s"(base) : "memory");
}
template <int N> DI void wait_vm() { asm volatile("s_waitcnt vmcnt(%0)" ::"i"(N) : "memory"); }
template <int N> DI void vm_bar() { asm volatile("s_waitcnt vmcnt(%0)\n\ts_barrier" ::"i"(N) : "memory"); }
template <int N> DI void vm_lgkm_bar() { asm volatile("s_waitcnt vmcnt(%0) lgkmcnt(0)\n\ts_barrier" ::"i"(N) : "memory"); }
DI void bar() { asm volatile("s_barrier" ::: "memory"); }
template <class T> DI void pin(T& a) { asm volatile("" : "+v"(a)); }
DI bf16x4 tr4(LAS char* p) { return __builtin_amdgcn_ds_read_tr16_b64_v4bf16((LAS bf16x4*)p); }
DI bf16x8 cat(bf16x4 a, bf16x4 b) { return __builtin_shufflevector(a, b, 0, 1, 2, 3, 4, 5, 6, 7); }
DI f32x16 mfma32(bf16x8 a, bf16x8 b, f32x16 c) { return __builtin_amdgcn_mfma_f32_32x32x16_bf16(a, b, c, 0, 0, 0); }
DI f32x4 mfma16(bf16x8 a, bf16x8 b, f32x4 c) { return __builtin_amdgcn_mfma_f32_16x16x32_bf16(a, b, c, 0, 0, 0); }
DI float max3f(float a, float b, float c) { float r; asm("v_max3_f32 %0, %1, %2, %3" : "=v"(r) : "v"(a), "v"(b), "v"(c)); return r; }
DI float max2f(float a, float b) { float r; asm("v_max_f32_e32 %0, %1, %2" : "=v"(r) : "v"(a), "v"(b)); return r; }
template <int CTRL> DI float dppf(float v) { return __builtin_bit_cast(float, __builtin_amdgcn_update_dpp(0, __builtin_bit_cast(int, v), CTRL, 0xf, 0xf, true)); }
DI float sum8(float v) { v += dppf<0xB1>(v); v += dppf<0x4E>(v); v += dppf<0x141>(v); return v; }
DI f32x16 splat16(float v) { f32x16 r; for (int i = 0; i < 16; ++i) r[i] = v; return r; }

#define ST_DECL
#define ST(i)
#define ST_END(kid)
#define DBG_PARAM
#define DBG_ARG
#define PH_BEGIN
#define PH_DBG

constexpr float QSCALE = 0.08838834764831845f * 1.4426950408889634f;
constexpr int PRE_RQ = 144, PRE_MOD = 36, PRE_LN = 32;
constexpr int PRE_LDS = 32768 + 2304 + 2048 + 256;
DI float silu_f(float cv) { return cv * __builtin_amdgcn_rcpf(1.f + __builtin_amdgcn_exp2f(-1.4426950408889634f * cv)); }
DI float sum_x8_16_32(float v) {
  v += dppf<0x128>(v);
  { auto rr = __builtin_amdgcn_permlane16_swap(__float_as_uint(v), __float_as_uint(v), false, false); v = __uint_as_float(rr[0]) + __uint_as_float(rr[1]); }
  { auto rr = __builtin_amdgcn_permlane32_swap(__float_as_uint(v), __float_as_uint(v), false, false); v = __uint_as_float(rr[0]) + __uint_as_float(rr[1]); }
  return v;
}
DI float mod_columns(const unsigned short* __restrict__ c, const unsigned short* __restrict__ wmod, int col0, int tid, int lane, int wid, LAS float* sl, LAS float* red) {
  const int rg = tid >> 3;
  u32x4 w[9];
#pragma unroll
  for (int i = 0; i < 9; ++i) w[i] = *(const u32x4*)(wmod + (size_t)(rg + 64 * i) * 3456 + col0);
  if (tid < 288) { const unsigned cw = ((const unsigned*)c)[tid]; *(LAS f32x2*)(sl + 2 * tid) = (f32x2){silu_f(bflo(cw)), silu_f(bfhi(cw))}; }
  asm volatile("s_waitcnt lgkmcnt(0)\n\ts_barrier" ::: "memory");
  float acc[8] = {};
#pragma unroll
  for (int i = 0; i < 9; ++i) { const float s = sl[rg + 64 * i];
#pragma unroll
    for (int j = 0; j < 4; ++j) { acc[2 * j] += s * bflo(w[i][j]); acc[2 * j + 1] += s * bfhi(w[i][j]); } }
#pragma unroll
  for (int j = 0; j < 8; ++j) acc[j] = sum_x8_16_32(acc[j]);
  if (lane < 8) { *(LAS f32x4*)(red + wid * 64 + lane * 8) = (f32x4){acc[0], acc[1], acc[2], acc[3]}; *(LAS f32x4*)(red + wid * 64 + lane * 8 + 4) = (f32x4){acc[4], acc[5], acc[6], acc[7]}; }
  asm volatile("s_waitcnt lgkmcnt(0)\n\ts_barrier" ::: "memory");
  float t = 0.f;
  if (tid < 64) {
#pragma unroll
    for (int w2 = 0; w2 < 8; ++w2) t += red[w2 * 64 + tid]; }
  return t;
}
__global__ __launch_bounds__(512) void k_pre(const unsigned short* __restrict__ c, const unsigned short* __restrict__ wmod, const unsigned short* __restrict__ xin,
                                             const char* __restrict__ wq, char* __restrict__ ws DBG_PARAM) {
  ST_DECL
  extern __shared__ __attribute__((aligned(16))) char lds_[];
  LAS char* lds = (LAS char*)lds_;
  LAS float* sl = (LAS float*)(lds + 32768); LAS float* red = (LAS float*)(lds + 32768 + 2304); LAS float* tab = (LAS float*)(lds + 32768 + 2304 + 2048);
  const int tid = threadIdx.x, lane = tid & 63, wid = rfl(tid >> 6);
  int b = blockIdx.x;
  if (b < PRE_RQ) {
    const int s32 = b >> 3, cg = (b & 7) * 8 + wid;
    { const int rr = lane >> 3, pp = lane & 7;
      const unsigned v = rr * 8192 + ((((pp >> 1) ^ ((rr >> 1) & 3)) << 1) + (pp & 1)) * 16;
      const char* src = wq + (size_t)s32 * 32 * 8192 + cg * 128; const unsigned m = wid * 4096;
      dma16<0>(m, v, src); dma16<0>(m + 1024, v, src + 8 * 8192); dma16<0>(m + 2048, v, src + 16 * 8192); dma16<0>(m + 3072, v, src + 24 * 8192); }
    const int cc = tid & 7;
    ST(1);
    const float t = mod_columns(c, wmod, (cc < 4 ? 0 : 576) + 32 * s32 + (cc & 3) * 8, tid, lane, wid, sl, red);
    if (tid < 64) tab[tid] = (tid < 32 ? t : 1.f + t) * QSCALE;
    asm volatile("s_waitcnt vmcnt(0) lgkmcnt(0)\n\ts_barrier" ::: "memory");
    ST(3);
    const int g = lane >> 4, q = (lane & 15) >> 2, p = lane & 3, h = lane >> 5, blk = g & 1, pc = ((p & 1) << 1) | (p >> 1);
    f32x4 sh[2][2], sc[2][2];
#pragma unroll
    for (int ks = 0; ks < 2; ++ks) { sh[ks][0] = *(LAS f32x4*)(tab + 16 * ks + 8 * h); sh[ks][1] = *(LAS f32x4*)(tab + 16 * ks + 8 * h + 4);
                                     sc[ks][0] = *(LAS f32x4*)(tab + 32 + 16 * ks + 8 * h); sc[ks][1] = *(LAS f32x4*)(tab + 32 + 16 * ks + 8 * h + 4); }
    const int i32 = lane & 31, dsw = (i32 & 19) | ((i32 & 4) << 1) | ((i32 & 8) >> 1);
    LAS char* scr = lds + wid * 4096;
    u32x4 wv[2][2];
#pragma unroll
    for (int dbl = 0; dbl < 2; ++dbl)
#pragma unroll
      for (int ks = 0; ks < 2; ++ks) { const int r0 = 16 * ks + 8 * h + q, r1 = r0 + 4, cx = 2 * dbl + blk;
        wv[dbl][ks] = __builtin_bit_cast(u32x4, cat(tr4(scr + 128 * r0 + 32 * (cx ^ ((r0 >> 1) & 3)) + 8 * pc), tr4(scr + 128 * r1 + 32 * (cx ^ ((r1 >> 1) & 3)) + 8 * pc))); }
#pragma unroll
    for (int dbl = 0; dbl < 2; ++dbl) { float bias = 0.f;
#pragma unroll
      for (int ks = 0; ks < 2; ++ks) { u32x4 o;
#pragma unroll
        for (int jj = 0; jj < 4; ++jj) { const float lo = bflo(wv[dbl][ks][jj]), hi = bfhi(wv[dbl][ks][jj]); const int e = (2 * jj) & 3;
          o[jj] = pk2(lo * sc[ks][jj >> 1][e], hi * sc[ks][jj >> 1][e + 1]); bias += lo * sh[ks][jj >> 1][e] + hi * sh[ks][jj >> 1][e + 1]; }
        st16c(ws + WS_WQ, (unsigned)((((cg >> 1) * 36 + 2 * s32 + ks) * 4 + 2 * (cg & 1) + dbl) * 1024 + lane * 16), o);
      }
      { auto rr = __builtin_amdgcn_permlane32_swap(__float_as_uint(bias), __float_as_uint(bias), false, false); bias = __uint_as_float(rr[0]) + __uint_as_float(rr[1]); }
      if (h == 0) ((float*)(ws + WS_BP))[s32 * 4096 + cg * 64 + 32 * dbl + dsw] = bias;
    }
    ST_END(4);
    return;
  }
  b -= PRE_RQ;
  if (b < PRE_MOD) {
    const float t = mod_columns(c, wmod, 1152 + b * 64 + (tid & 7) * 8, tid, lane, wid, sl, red);
    if (tid < 64) ((float*)(ws + WS_MOD))[1152 + b * 64 + tid] = t;
    ST_END(5);
    return;
  }
  b -= PRE_MOD;
  {
    if (tid < 64) *(u32x4*)(ws + WS_CNT + (size_t)(b * 64 + tid) * 16) = (u32x4){0u, 0u, 0u, 0u};
    const int cc = lane & 7, rr = lane >> 3, row = (b * 8 + wid) * 8 + rr;
    const unsigned short* x = xin + (size_t)row * D_;
    float v[9][8];
#pragma unroll
    for (int i = 0; i < 9; ++i) { const u32x4 a = *(const u32x4*)(x + (cc + 8 * i) * 8);
#pragma unroll
      for (int j = 0; j < 4; ++j) { v[i][2 * j] = bflo(a[j]); v[i][2 * j + 1] = bfhi(a[j]); } }
    float s = 0.f;
#pragma unroll
    for (int i = 0; i < 9; ++i)
#pragma unroll
      for (int j = 0; j < 8; ++j) s += v[i][j];
    s = sum8(s);
    const float mean = s * (1.f / 576.f);
    float q = 0.f;
#pragma unroll
    for (int i = 0; i < 9; ++i)
#pragma unroll
      for (int j = 0; j < 8; ++j) { const float d = v[i][j] - mean; q += d * d; }
    q = sum8(q);
    const float rstd = __builtin_amdgcn_rsqf(q * (1.f / 576.f) + 1e-5f);
    const unsigned ob = (unsigned)((row >> 5) * 36 * 1024 + (row & 31) * 16);
#pragma unroll
    for (int i = 0; i < 9; ++i) { const int ch = cc + 8 * i;
      u32x4 w = {pk2((v[i][0] - mean) * rstd, (v[i][1] - mean) * rstd), pk2((v[i][2] - mean) * rstd, (v[i][3] - mean) * rstd),
                 pk2((v[i][4] - mean) * rstd, (v[i][5] - mean) * rstd), pk2((v[i][6] - mean) * rstd, (v[i][7] - mean) * rstd)};
      st16c(ws + WS_H1, ob + (ch >> 1) * 1024 + (ch & 1) * 512, w); }
    ST_END(6);
  }
}

constexpr float THR = 8.0f;
constexpr int A_TAB = 131072;
constexpr int A_LDS = 131072 + 2048;
#define A_LA 4
#define Q_LA 5
struct MainArgs { const char* kc; const char* vc; const char* wg; const char* wu; const char* wo; const char* wd; const unsigned short* x; unsigned short* out; char* ws; };
constexpr int P_STG = 9216, P_NSTG = 4, P_T = 73728;
constexpr int C_BASE = 81920;
constexpr int MAIN_LDS = 163840;
constexpr int CNT_CLEAR = 0;
constexpr int CNT_O = 8, CNT_H = 40;
DI void signal_done(char* ws, int idx, int wid) {
  vm_bar<0>();
  if (wid == 0 && lane_id() == 0) __hip_atomic_fetch_add((unsigned*)(ws + WS_CNT) + idx * 64, 1u, __ATOMIC_RELAXED, __HIP_MEMORY_SCOPE_AGENT);
}
DI void wait_count(const char* ws, int idx, unsigned target, int wid) {
  if (wid == 0) {
    const unsigned* cnt = (const unsigned*)(ws + WS_CNT) + idx * 64;
    for (unsigned spin = 0; spin < (1u << 15); ++spin) {
      if (rfl(__hip_atomic_load(cnt, __ATOMIC_RELAXED, __HIP_MEMORY_SCOPE_AGENT)) >= target) break;
      __builtin_amdgcn_s_sleep(1);
    }
  }
  bar();
}
DI void st_empty(char* base, unsigned voff) { st16c(base, voff, (u32x4){~0u, ~0u, ~0u, ~0u}); }
DI bool has_empty(const bf16x8& a, const bf16x8& b) {
  const u32x4 x = __builtin_bit_cast(u32x4, a), y = __builtin_bit_cast(u32x4, b);
  unsigned m = x[0] > x[1] ? x[0] : x[1]; m = m > x[2] ? m : x[2]; m = m > x[3] ? m : x[3]; m = m > y[0] ? m : y[0]; m = m > y[1] ? m : y[1]; m = m > y[2] ? m : y[2]; m = m > y[3] ? m : y[3];
  return __any(m == ~0u);
}
template <int IMM1> DI void revalidate(bf16x8& a, bf16x8& b, unsigned oa, unsigned ob, const char* base) {
  if (__builtin_expect(has_empty(a, b), 0)) {
    for (unsigned spin = 0; spin < (1u << 15); ++spin) {
      __builtin_amdgcn_s_sleep(3);
      asm volatile("global_load_dwordx4 %0, %2, %4 sc1\n\tglobal_load_dwordx4 %1, %3, %4 offset:%5 sc1\n\ts_waitcnt vmcnt(0)" : "+v"(a), "+v"(b) : "v"(oa), "v"(ob), "s"(base), "i"(IMM1) : "memory");
      if (!has_empty(a, b)) break;
    }
  }
}
DI void proj_prefetch(const char* W, int nst, int wid) {
  const int lane = lane_id();
  const int x = blockIdx.x & 7, j = blockIdx.x >> 3, ks = x >> 1, ct = j & 3, rg = wid & 3, kh = wid >> 2;
  unsigned wsrc3[3];
#pragma unroll
  for (int e = 0; e < 3; ++e) { const int o = 64 * e + lane, row = o / 18, ch = o - row * 18; wsrc3[e] = row * 1152 + ch * 16; }
  const char* Ww = W + (size_t)((ks * 2 + kh) * nst * 32 + rg * 8) * 1152 + ct * 288;
  const unsigned wdst = kh * (P_NSTG * P_STG) + rg * 2304;
#pragma unroll
  for (int s = 0; s < 3; ++s) { const char* wsrc = Ww + (size_t)s * 32 * 1152; const unsigned m = s * P_STG + wdst;
    dma16<0>(m, wsrc3[0], wsrc); dma16<0>(m + 1024, wsrc3[1], wsrc); if (lane < 16) dma16<0>(m + 2048, wsrc3[2], wsrc); }
}
DI void phase_attn(const MainArgs& ar, LAS char* lds, const int wid DBG_PARAM) {
  PH_BEGIN
  char* const ws = ar.ws;
  const int lane = lane_id(), tid = wid * 64 + lane;
  const int x = blockIdx.x & 7, j = blockIdx.x >> 3, head = 4 * x + (j & 3), rt = j >> 2, kvh = head >> 1;
  const int rb = rt * 8 + wid;
  const int prb = 8 * (x & 1) + (j >> 2), pown = (j & 3) * 4 + (x >> 1);
  const int h = lane >> 5, r = lane & 31, blk = (lane >> 4) & 1, q = (lane & 15) >> 2, p = lane & 3;
  const int l4 = lane >> 4, pos = lane & 15;
  unsigned ksrc2[2];
#pragma unroll
  for (int e = 0; e < 2; ++e) { const int n = 2 * wid + e, row = 4 * n + l4, ch = pos ^ ((l4 << 2) | (n & 3)); ksrc2[e] = row * 256 + ch * 16 - e * 1024; }
  const char* wq_h = ws + WS_WQ + (size_t)head * 36 * 4096 + wid * 2048;
  const char* kb_ = ar.kc + ((size_t)kvh * SK + (SK - WIN)) * 256;
  const char* vb_ = ar.vc + ((size_t)kvh * SK + (SK - WIN)) * 256;
  const char* h1w = ws + WS_H1 + (size_t)rb * 36 * 1024;
  const unsigned lane16 = lane * 16;
  auto dmaW = [&](int qq, int e) { const unsigned m = 65536 + (qq & 3) * 16384 + wid * 2048; if (e == 0) dma16<0>(m, lane16, wq_h + qq * 16384); else dma16<1024>(m, lane16, wq_h + qq * 16384); };
  auto dmaK8 = [&](int t) { const unsigned m = (t & 3) * 16384 + wid * 2048; const char* s = kb_ + t * 16384; dma16<0>(m, ksrc2[0], s); dma16<1024>(m, ksrc2[1], s); };
  auto dmaV8 = [&](int t) { const unsigned m = 65536 + ((t + 1) & 3) * 16384 + wid * 2048; const char* s = vb_ + t * 16384; dma16<0>(m, ksrc2[0], s); dma16<1024>(m, ksrc2[1], s); };
  bf16x8 g[9][4];
  auto loadG1 = [&](auto QQ, auto E) { constexpr int qq = QQ, e = E; gload16<e * 1024>(g[qq][e], lane16, h1w + qq * 4096); };
  auto loadG = [&](auto QQ) { static_for<4>([&](auto E) { loadG1(QQ, E); }); };
  auto mark = [&](auto MI) { constexpr int mi = MI;
    if constexpr (mi < 2) { const int rown = prb * 128 + pown * 8 + (tid & 7), kc = mi == 0 ? (tid >> 3) : 64 + (lane >> 3);
      st_empty(ws + WS_H2, (unsigned)(((rown >> 5) * 36 + (kc >> 1)) * 1024 + ((kc & 1) * 32 + (rown & 31)) * 16)); }
    else { const int n = tid + 512 * (mi - 2), n2 = n < 2304 ? n : n - 2304;
      st_empty(ws + WS_PB + (size_t)prb * 16 * 16 * 2304 + (size_t)pown * 2304, (unsigned)((n < 2304 ? 0 : (unsigned)(WS_PD - WS_PB)) + (n2 / 144) * 36864 + (n2 % 144) * 16)); } };
  float tb5[5];
  { const int part = tid >> 7; const unsigned to = (unsigned)(head * 128 + (tid & 127)) * 4;
#pragma unroll
    for (int i = 0; i < 5; ++i) { const int sidx = part + 4 * i < 18 ? part + 4 * i : part; asm volatile("global_load_dword %0, %1, %2" : "=v"(tb5[i]) : "v"(to + sidx * 16384), "s"(ws + WS_BP)); } }
  dmaW(0, 0); dmaW(0, 1); loadG(integral_constant<int, 0>{});
  bar();
  dmaW(1, 0); dmaW(1, 1); loadG(integral_constant<int, 1>{}); dmaW(2, 0); dmaW(2, 1); loadG(integral_constant<int, 2>{}); dmaK8(0);
  wait_vm<20>();
#pragma unroll
  for (int i = 0; i < 5; ++i) pin(tb5[i]);
  *(LAS float*)(lds + A_TAB + tid * 4) = (tb5[0] + tb5[1]) + (tb5[2] + tb5[3]) + ((tid >> 7) < 2 ? tb5[4] : 0.f);
  unsigned wb0 = 65536 + lane16;
  pin(wb0);
  f32x16 acc[4] = {};
  {
    constexpr int LA = Q_LA, NIT = 9 * 16;
    bf16x8 wf[LA + 1];
    auto rd = [&](auto I) { constexpr int i = I; constexpr int qq = i / 16, ks = (i % 16) / 4, db = i % 4;
      wf[i % (LA + 1)] = *(LAS bf16x8*)(lds + wb0 + (qq & 3) * 16384 + ks * 4096 + db * 1024); };
    SB(); vm_lgkm_bar<12>();
    static_for<4>([&](auto E) { pin(g[0][E]); });
    SB();
    ST(1);
    static_for<LA>([&](auto I) { rd(I); });
    SB();
    static_for<NIT>([&](auto I) { constexpr int i = I; constexpr int qq = i / 16, rr = i % 16, ks = rr / 4, db = rr % 4;
      if constexpr (rr == 0 && qq > 0) {
        constexpr int NW[9] = {12, 12, 10, 10, 10, 10, 10, 8, 8};
        vm_bar<NW[qq]>();
        static_for<4>([&](auto E) { pin(g[qq][E]); });
        if constexpr (qq == 4) ST(2);
        SB();
      }
      if constexpr (i + LA < NIT) rd(integral_constant<int, i + LA>{});
      acc[db] = mfma32(wf[i % (LA + 1)], g[qq][ks], acc[db]);
      if constexpr (qq + 3 < 9) {
        if constexpr (rr == 1) dmaW(qq + 3, 0);
        if constexpr (rr == 3) dmaW(qq + 3, 1);
        if constexpr (rr >= 5 && rr <= 11 && (rr & 1)) loadG1(integral_constant<int, qq + 3>{}, integral_constant<int, (rr - 5) / 2>{});
      } else {
        if constexpr (rr == 1) dmaV8(qq - 6);
        if constexpr (rr == 7) dmaK8(qq - 5);
      }
      SB();
    });
    const int dsw = (r & 19) | ((r & 4) << 1) | ((r & 8) >> 1);
    const u32x4 one = {h == 0 ? 0x3f803f80u : 0u, 0u, 0u, 0u};
#pragma unroll
    for (int db = 0; db < 4; ++db) {
      const LAS float* tp = (const LAS float*)(lds + A_TAB) + 32 * db + dsw;
      const float bv = (tp[0] + tp[128]) + (tp[256] + tp[384]);
      const unsigned hi = pk2(bv, 0.f) & 0xffffu; const float rem = bv - bflo(hi);
      const u32x4 aw = {h == 0 ? (hi | (pk2(rem, 0.f) << 16)) : 0u, 0u, 0u, 0u};
      acc[db] = mfma32(__builtin_bit_cast(bf16x8, aw), __builtin_bit_cast(bf16x8, one), acc[db]);
    }
  }
  ST(3);
  bf16x8 qf[8];
#pragma unroll
  for (int db = 0; db < 4; ++db)
#pragma unroll
    for (int s = 0; s < 2; ++s) { u32x4 w;
#pragma unroll
      for (int i = 0; i < 4; ++i) w[i] = pk2(acc[db][8 * s + 2 * i], acc[db][8 * s + 2 * i + 1]);
      qf[2 * db + s] = __builtin_bit_cast(bf16x8, w); }
  const unsigned kb0 = 256 * r + 16 * (h ^ (((r & 3) << 2) | ((r >> 2) & 3)));
  const unsigned vb0 = 65536 + 256 * (4 * h + q) + 16 * ((q << 2) | ((2 * blk + (p >> 1)) ^ h)) + 8 * (p & 1);
  unsigned ka[8], va[4][2];
#pragma unroll
  for (int s = 0; s < 8; ++s) ka[s] = kb0 ^ (s << 5);
#pragma unroll
  for (int db = 0; db < 4; ++db) { va[db][0] = vb0 ^ (db << 6); va[db][1] = vb0 ^ (db << 6) ^ 32; }
  f32x16 o[4] = {}; f32x16 S[2][2], negm; unsigned pw[16]; float m; f32x2 l2 = {0.f, 0.f};
  constexpr int LA = A_LA;
  bf16x8 fr[LA + 1];
  struct Map { int g[272]; int n; };
  constexpr Map MP = []() constexpr { Map mp{}; mp.n = 0; for (int G = 0; G < 9 * 32; ++G) { const int t = G / 32 - 1, i = G % 32; if (i < 16 ? (t + 1 <= 7) : (t >= 0 && t <= 7)) mp.g[mp.n++] = G; } return mp; }();
  constexpr int NIT = MP.n;
  auto rdA = [&](auto C) { constexpr int c = C; constexpr int G = MP.g[c], t = G / 32 - 1, i = G % 32;
    if constexpr (i < 16) { constexpr int s = i >> 1, kb = i & 1; fr[c % (LA + 1)] = *(LAS bf16x8*)(lds + ka[s] + ((t + 1) & 3) * 16384 + kb * 8192); }
    else { constexpr int ii = i - 16, kk = ii >> 2, db = ii & 3; constexpr int so = ((t + 1) & 3) * 16384 + kk * 4096;
      fr[c % (LA + 1)] = cat(tr4(lds + va[db][0] + so), tr4(lds + va[db][1] + so + 2048)); } };
  auto rowmax_finish = [&](float a) { auto rr = __builtin_amdgcn_permlane32_swap(__float_as_uint(a), __float_as_uint(a), false, false); return max2f(__uint_as_float(rr[0]), __uint_as_float(rr[1])); };
  float mxa, mxb;
  const unsigned gw = blockIdx.x * 8 + wid;
  auto touch = [&](auto TI) { constexpr int ti = TI; constexpr int m = ti >> 1, half = ti & 1;
    constexpr unsigned per = m == 0 ? 9 : 11, tot = m == 0 ? 36864 : 41472;
    unsigned line = (gw * 2 + half) * per + (lane < (int)per ? lane : 0); if (line >= tot) line = tot - 1;
    dma4(A_TAB + 2048 + wid * 256, line * 128, m == 0 ? ar.wo : (m == 1 ? ar.wg : (m == 2 ? ar.wu : ar.wd))); };
  auto background = [&](auto BI) { constexpr int bi = BI;
    if constexpr (bi < 16) { if constexpr ((bi & 3) < 2) touch(integral_constant<int, (bi >> 2) * 2 + (bi & 1)>{}); else mark(integral_constant<int, (bi >> 2) * 2 + (bi & 1)>{}); }
    else mark(integral_constant<int, bi - 8>{}); };
  SB(); vm_bar<8>(); SB();
  ST(4);
  static_for<LA>([&](auto C) { rdA(C); });
  SB();
  static_for<NIT>([&](auto C) { constexpr int c = C; constexpr int G = MP.g[c], t = G / 32 - 1, i = G % 32;
    constexpr bool firstX = (i == 0) || (t == 7 && i == 16);
    if constexpr (firstX && t >= 0) {
      constexpr int NW[8] = {4, 8, 12, 12, 12, 9, 3, 0};
      float a = rowmax_finish(max2f(mxa, mxb));
      if constexpr (t == 0) {
        m = a; negm = splat16(-a);
#pragma unroll
        for (int r2 = 0; r2 < 16; ++r2) { S[0][0][r2] -= a; S[0][1][r2] -= a; }
      } else if (__builtin_expect(__any(a > THR), 0)) {
        const float dl = fmaxf(a, 0.f), f = __builtin_amdgcn_exp2f(-dl);
        m += dl; negm = splat16(-m); l2 *= f;
#pragma unroll
        for (int r2 = 0; r2 < 16; ++r2) { S[t & 1][0][r2] -= dl; S[t & 1][1][r2] -= dl; }
#pragma unroll
        for (int d = 0; d < 4; ++d)
#pragma unroll
          for (int r2 = 0; r2 < 16; ++r2) o[d][r2] *= f;
      }
      SB(); vm_bar<NW[t]>(); SB();
      if constexpr (t == 7) { if (wid == 0 && lane == 0) __hip_atomic_fetch_add((unsigned*)(ws + WS_CNT) + (CNT_CLEAR + x) * 64, 1u, __ATOMIC_RELAXED, __HIP_MEMORY_SCOPE_AGENT); }
      if constexpr (t == 3) ST(5);
      if constexpr (t == 4) ST(7);
      if constexpr (t == 5) ST(8);
      if constexpr (t == 7) proj_prefetch(ar.wo, 16, wid);
      if constexpr (t == 7) {
#pragma unroll
        for (int e = 0; e < 32; ++e) S[1][e >> 4][e & 15] = __builtin_amdgcn_exp2f(S[1][e >> 4][e & 15]);
#pragma unroll
        for (int e = 0; e < 16; ++e) pw[e] = pk2(S[1][e >> 3][(2 * e) & 15], S[1][e >> 3][((2 * e) & 15) + 1]);
        SB();
      }
    }
    if constexpr (c + LA < NIT) rdA(integral_constant<int, c + LA>{});
    if constexpr (i < 16) {
      constexpr int s = i >> 1, kb = i & 1, X = (t + 1) & 1;
      if constexpr (s == 0) { if constexpr (t < 0) S[X][kb] = mfma32(fr[c % (LA + 1)], qf[s], f32x16{}); else S[X][kb] = mfma32(fr[c % (LA + 1)], qf[s], negm); }
      else S[X][kb] = mfma32(fr[c % (LA + 1)], qf[s], S[X][kb]);
      if constexpr (t >= 0) {
        constexpr int e0 = 2 * i, e1 = 2 * i + 1, Y = t & 1;
        S[Y][e0 >> 4][e0 & 15] = __builtin_amdgcn_exp2f(S[Y][e0 >> 4][e0 & 15]);
        S[Y][e1 >> 4][e1 & 15] = __builtin_amdgcn_exp2f(S[Y][e1 >> 4][e1 & 15]);
        if constexpr (i >= 1) { constexpr int e = i - 1; pw[e] = pk2(S[Y][e >> 3][(2 * e) & 15], S[Y][e >> 3][((2 * e) & 15) + 1]); }
        if constexpr (i == 15) { SB(); pw[15] = pk2(S[Y][1][14], S[Y][1][15]); }
        if constexpr (t <= 3 && i == 6) dma16<0>((t & 3) * 16384 + wid * 2048, ksrc2[0], kb_ + (t + 4) * 16384);
        if constexpr (t <= 3 && i == 9) dma16<1024>((t & 3) * 16384 + wid * 2048, ksrc2[1], kb_ + (t + 4) * 16384);
        if constexpr (t <= 4 && i == 12) dma16<0>(65536 + (t & 3) * 16384 + wid * 2048, ksrc2[0], vb_ + (t + 3) * 16384);
        if constexpr (t <= 4 && i == 15) dma16<1024>(65536 + (t & 3) * 16384 + wid * 2048, ksrc2[1], vb_ + (t + 3) * 16384);
      }
      if constexpr (t < 0 && i == 15) {
        asm volatile("s_nop 11" : "+v"(S[0][0]), "+v"(S[0][1]));
        mxa = max3f(S[0][0][0], S[0][0][1], S[0][0][2]); mxb = max3f(S[0][1][0], S[0][1][1], S[0][1][2]);
#pragma unroll
        for (int r2 = 3; r2 < 15; r2 += 2) { mxa = max3f(mxa, S[0][0][r2], S[0][0][r2 + 1]); mxb = max3f(mxb, S[0][1][r2], S[0][1][r2 + 1]); }
        mxa = max3f(mxa, S[0][0][15], S[0][1][15]);
      }
    } else {
      constexpr int ii = i - 16, kk = ii >> 2, db = ii & 3, Y = t & 1, X = (t + 1) & 1;
      const u32x4 w = {pw[4 * kk], pw[4 * kk + 1], pw[4 * kk + 2], pw[4 * kk + 3]};
      o[db] = mfma32(fr[c % (LA + 1)], __builtin_bit_cast(bf16x8, w), o[db]);
      if constexpr (t <= 4 && (ii & 3) == 1 && 4 * t + (ii >> 2) < 19) background(integral_constant<int, 4 * t + (ii >> 2)>{});
      l2 += (f32x2){S[Y][ii >> 3][(2 * ii) & 15], S[Y][ii >> 3][((2 * ii) & 15) + 1]};
      if constexpr (t < 7) {
        if constexpr (ii == 4) { mxa = max3f(S[X][0][0], S[X][0][1], S[X][0][2]); mxb = max3f(S[X][0][3], S[X][0][4], S[X][0][5]); }
        if constexpr (ii == 5) { mxa = max3f(mxa, S[X][0][6], S[X][0][7]); mxb = max3f(mxb, S[X][0][8], S[X][0][9]); }
        if constexpr (ii == 6) { mxa = max3f(mxa, S[X][0][10], S[X][0][11]); mxb = max3f(mxb, S[X][0][12], S[X][0][13]); }
        if constexpr (ii == 7) { mxa = max3f(mxa, S[X][0][14], S[X][0][15]); mxb = max3f(mxb, S[X][1][0], S[X][1][1]); }
        if constexpr (ii == 8) { mxa = max3f(mxa, S[X][1][2], S[X][1][3]); mxb = max3f(mxb, S[X][1][4], S[X][1][5]); }
        if constexpr (ii == 9) { mxa = max3f(mxa, S[X][1][6], S[X][1][7]); mxb = max3f(mxb, S[X][1][8], S[X][1][9]); }
        if constexpr (ii == 10) { mxa = max3f(mxa, S[X][1][10], S[X][1][11]); mxb = max3f(mxb, S[X][1][12], S[X][1][13]); }
        if constexpr (ii == 11) { mxa = max3f(mxa, S[X][1][14], S[X][1][15]); }
      }
    }
    SB();
  });
  ST(12);
  float l = l2[0] + l2[1];
  { auto rr = __builtin_amdgcn_permlane32_swap(__float_as_uint(l), __float_as_uint(l), false, false); l = __uint_as_float(rr[0]) + __uint_as_float(rr[1]); }
  const float inv = 1.f / l;
  char* ob = ws + WS_O + ((size_t)rb * 256 + head * 8) * 1024;
#pragma unroll
  for (int db = 0; db < 4; ++db)
#pragma unroll
    for (int s = 0; s < 2; ++s) { u32x4 w;
#pragma unroll
      for (int i = 0; i < 4; ++i) w[i] = pk2(o[db][8 * s + 2 * i] * inv, o[db][8 * s + 2 * i + 1] * inv);
      st16c(ob, lane16 + (2 * db + s) * 1024, w); }
  signal_done(ws, CNT_O + rt * 4 + (head >> 3), wid);
  ST_END(0);
}

#define P_LA 4
DI void ffn1_prefetch(const MainArgs& ar, int wid);
template <bool FINAL>
DI void phase_proj(const MainArgs& ar, LAS char* lds, const int wid DBG_PARAM) {
  PH_BEGIN
  char* const ws = ar.ws; const char* W = FINAL ? ar.wd : ar.wo; const unsigned short* xin = ar.x; unsigned short* out = ar.out;
  constexpr int NS16 = FINAL ? 288 : 256, NST = NS16 / 16;
  const char* act = ws + (FINAL ? WS_H : WS_O);
  const float* mod = (const float*)(ws + WS_MOD);
  char* part = ws + (FINAL ? WS_PD : WS_PB);
  const int lane = lane_id(), tid = wid * 64 + lane, rg = wid & 3, kh = wid >> 2;
  const int x = blockIdx.x & 7, j = blockIdx.x >> 3, ks = x >> 1, rb = 8 * (x & 1) + (j >> 2), ct = j & 3;
  const int own = ct * 4 + ks;
  const int g = lane >> 4, m16 = lane & 15, q4 = (lane & 15) >> 2, p = lane & 3;
  const int kb32 = (ks * 2 + kh) * NST;
  unsigned aoff[2];
#pragma unroll
  for (int a = 0; a < 2; ++a) { const int row = rb * 128 + rg * 32 + a * 16 + m16; aoff[a] = (unsigned)(row >> 5) * (NS16 * 1024) + (row & 31) * 16 + (g >> 1) * 1024 + (g & 1) * 512; }
  const char* actw = act + (size_t)kb32 * 2048;
  unsigned wsrc3[3];
#pragma unroll
  for (int e = 0; e < 3; ++e) { const int o = 64 * e + lane, row = o / 18, ch = o - row * 18; wsrc3[e] = row * 1152 + ch * 16; }
  const char* Ww = W + (size_t)(kb32 * 32 + rg * 8) * 1152 + ct * 288;
  const unsigned wdst = kh * (P_NSTG * P_STG) + rg * 2304;
  bf16x8 af[NST][2];
  auto issue_piece = [&](auto S, auto E) { constexpr int s = S, e = E;
    const char* wsrc = Ww + (size_t)s * 32 * 1152; const unsigned m = (s % P_NSTG) * P_STG + wdst;
    if constexpr (e == 0) dma16<0>(m, wsrc3[0], wsrc);
    if constexpr (e == 1) dma16<0>(m + 1024, wsrc3[1], wsrc);
    if constexpr (e == 2) { if (lane < 16) dma16<0>(m + 2048, wsrc3[2], wsrc); }
    if constexpr (e == 3) gload16c<0>(af[s][0], aoff[0], actw + s * 2048);
    if constexpr (e == 4) gload16c<0>(af[s][1], aoff[1], actw + s * 2048); };
  if (!FINAL && wid != 0) { ffn1_prefetch(ar, wid); if (wid == 1) ffn1_prefetch(ar, 0); }
  wait_count(ws, (FINAL ? CNT_H : CNT_O) + (rb >> 1) * 4 + ks, 8u, wid);
  static_for<3>([&](auto S) { issue_piece(S, integral_constant<int, 3>{}); issue_piece(S, integral_constant<int, 4>{}); });
  unsigned tb[P_NSTG];
#pragma unroll
  for (int i = 0; i < P_NSTG; ++i) tb[i] = kh * (P_NSTG * P_STG) + i * P_STG + 288 * (16 * (g >> 1) + 4 * (g & 1) + q4) + 8 * p;
  f32x4 acc[2][9] = {};
  constexpr int LA = P_LA, NIT = NST * 9;
  bf16x8 wf[LA + 1];
  auto rd = [&](auto I) { constexpr int i = I; constexpr int s = i / 9, c = i % 9;
    wf[i % (LA + 1)] = cat(tr4(lds + tb[s % P_NSTG] + 32 * c), tr4(lds + tb[s % P_NSTG] + 288 * 8 + 32 * c)); };
  constexpr int NPF = 0, NTB = FINAL ? 3 : 2;
  constexpr int TB = FINAL ? 81920 : 137600;
  SB(); vm_bar<4 + NPF>(); pin(af[0][0]); pin(af[0][1]);
#pragma unroll
  for (int e = 0; e < NTB; ++e) { const int n = NTB * wid + e;
    if (!FINAL) dma16<0>(TB + n * 1024, lane * 16, n < 7 ? (const char*)(mod + 1152) + n * 1024 : (const char*)xin + (size_t)(rb * 128 + own * 8) * 1152 + (n - 7) * 1024);
    else { const int n2 = n < 21 ? n : 0; dma16<0>(TB + n2 * 1024, lane * 16, n2 < 3 ? (const char*)(mod + 2880) + n2 * 1024 : ws + WS_X1R + (size_t)blockIdx.x * 576 * 32 + (n2 - 3) * 1024); } }
  SB();
  ST(1);
  static_for<LA>([&](auto I) { rd(I); });
  SB();
  static_for<NIT>([&](auto I) { constexpr int i = I; constexpr int s = i / 9, c = i % 9;
    if constexpr (c == 0 && s > 0) {
      constexpr int outst = s == 1 ? 2 + NPF + NTB + 5 : ((s + 2 < NST) ? 5 : 0);
      vm_bar<outst>(); pin(af[s][0]); pin(af[s][1]);
      if constexpr (s == 8) ST(2);
      SB();
    }
    if constexpr (i + LA < NIT) rd(integral_constant<int, i + LA>{});
    acc[0][c] = mfma16(wf[i % (LA + 1)], af[s][0], acc[0][c]);
    acc[1][c] = mfma16(wf[i % (LA + 1)], af[s][1], acc[1][c]);
    if constexpr (s + 3 < NST && c >= 1 && c <= 5) issue_piece(integral_constant<int, s + 3>{}, integral_constant<int, c - 1>{});
    SB();
  });
  ST(5);
  const int r8 = tid & 7, rown = rb * 128 + own * 8 + r8;
  int colA[2], colB[2];
  colA[0] = 144 * (tid >> 7) + 32 * ((tid >> 5) & 3) + 4 * ((tid >> 3) & 3); colB[0] = colA[0] + 16;
  colA[1] = 288 * ((tid >> 5) & 1) + 128 + 4 * ((tid >> 3) & 3); colB[1] = colA[1] + 144;
  unsigned marked;
  if (!FINAL) asm volatile("global_load_dword %0, %1, %2 sc1" : "=v"(marked) : "v"((unsigned)(lane & 7) * 256), "s"(ws + WS_CNT + CNT_CLEAR * 256));
  {
    LAS char* exw = lds + (rg * 2 + kh) * 9216 + lane * 16; LAS char* exr = lds + (rg * 2 + (kh ^ 1)) * 9216 + lane * 16;
    bar();
    ST(3);
    f32x4 fin[9];
    if (kh) {
#pragma unroll
      for (int c = 0; c < 9; ++c) { *(LAS f32x4*)(exw + c * 1024) = acc[0][c]; fin[c] = acc[1][c]; }
    } else {
#pragma unroll
      for (int c = 0; c < 9; ++c) { *(LAS f32x4*)(exw + c * 1024) = acc[1][c]; fin[c] = acc[0][c]; }
    }
    asm volatile("s_waitcnt lgkmcnt(0)\n\ts_barrier" ::: "memory");
    ST(4);
    char* pw = part + (size_t)rb * 16 * 16 * 2304 + (size_t)own * 2304;
#pragma unroll
    for (int c = 0; c < 9; ++c) fin[c] += *(LAS f32x4*)(exr + c * 1024);
    const unsigned jo = (unsigned)(rg * 4 + kh * 2 + (m16 >> 3)) * 36864, lo = g * 8 + (m16 & 7);
    u32x4 cw[4];
#pragma unroll
    for (int cp = 0; cp < 4; ++cp) cw[cp] = (u32x4){pk2(fin[2 * cp][0], fin[2 * cp][1]), pk2(fin[2 * cp][2], fin[2 * cp][3]), pk2(fin[2 * cp + 1][0], fin[2 * cp + 1][1]), pk2(fin[2 * cp + 1][2], fin[2 * cp + 1][3])};
    const u32x2 w8 = {pk2(fin[8][0], fin[8][1]), pk2(fin[8][2], fin[8][3])};
#pragma unroll
    for (int cp = 0; cp < 4; ++cp) st16c(pw, jo + (cp * 32 + lo) * 16, cw[cp]);
    asm volatile("global_store_dwordx2 %0, %1, %2" STF "\n\ts_nop 1" ::"v"(jo + 2048 + lo * 8), "v"(w8), "s"(pw) : "memory");
  }
  ST(7);
  const char* pr = part + ((size_t)rb * 16 + own) * 16 * 2304;
  const bool two = wid == 0;
  u32x4 pv[4]; u32x2 pq[4][2];
  if (!FINAL) {
    asm volatile("s_waitcnt vmcnt(5)" : "+v"(marked) :: "memory");
    if (!__all(marked >= 32u)) {
      const unsigned* cnt = (const unsigned*)(ws + WS_CNT) + (CNT_CLEAR + (lane & 7)) * 64;
      for (unsigned spin = 0; spin < (1u << 15); ++spin) { if (__all(__hip_atomic_load(cnt, __ATOMIC_RELAXED, __HIP_MEMORY_SCOPE_AGENT) >= 32u)) break; __builtin_amdgcn_s_sleep(1); }
    }
  }
#define P_SLEEP 20
  __builtin_amdgcn_s_sleep(P_SLEEP);
  ST(8);
  for (unsigned spin = 0; spin < (1u << 15); ++spin) {
    const unsigned o0 = (unsigned)((tid >> 7) * 4 * 2304 + (tid & 127) * 16), o1 = (unsigned)(((tid >> 5) & 1) * 8 * 2304 + 2048 + (tid & 31) * 8);
#pragma unroll
    for (int k = 0; k < 4; ++k) gload16c<0>(pv[k], o0 + k * 2304, pr);
    if (two) {
#pragma unroll
      for (int k = 0; k < 4; ++k) {
        asm volatile("global_load_dwordx2 %0, %1, %2" LDF : "=v"(pq[k][0]) : "v"(o1 + k * 2304), "s"(pr));
        asm volatile("global_load_dwordx2 %0, %1, %2" LDF : "=v"(pq[k][1]) : "v"(o1 + (4 + k) * 2304), "s"(pr)); } }
    wait_vm<0>();
    bool full = true;
#pragma unroll
    for (int k = 0; k < 4; ++k) { pin(pv[k]); full &= pv[k][0] != ~0u && pv[k][1] != ~0u && pv[k][2] != ~0u && pv[k][3] != ~0u; }
    if (two) {
#pragma unroll
      for (int k = 0; k < 4; ++k) { pin(pq[k][0]); pin(pq[k][1]); full &= pq[k][0][0] != ~0u && pq[k][0][1] != ~0u && pq[k][1][0] != ~0u && pq[k][1][1] != ~0u; } }
    if (__all(full)) break;
    __builtin_amdgcn_s_sleep(4);
  }
  ST(9);
  f32x4 v[2][2], sc[2][2], sh[2][2];
#pragma unroll
  for (int i = 0; i < 2; ++i) if (i == 0 || two) {
#pragma unroll
    for (int hh = 0; hh < 2; ++hh) { const int col = hh ? colB[i] : colA[i]; f32x4 rs;
      const f32x4 gt = *(LAS f32x4*)(lds + TB + col * 4);
      if (FINAL) rs = *(LAS f32x4*)(lds + TB + 3072 + (i ? 512 + (tid & 63) : tid) * 32 + hh * 16);
      else { const u32x2 xr = *(LAS u32x2*)(lds + TB + 7168 + r8 * 1152 + col * 2); rs = (f32x4){bflo(xr[0]), bfhi(xr[0]), bflo(xr[1]), bfhi(xr[1])};
        sh[i][hh] = *(LAS f32x4*)(lds + TB + (576 + col) * 4); sc[i][hh] = *(LAS f32x4*)(lds + TB + (1152 + col) * 4); }
      f32x4 t = {0.f, 0.f, 0.f, 0.f};
#pragma unroll
      for (int k = 0; k < 4; ++k) { const unsigned w0 = i ? pq[k][hh][0] : pv[k][2 * hh], w1 = i ? pq[k][hh][1] : pv[k][2 * hh + 1]; t += (f32x4){bflo(w0), bfhi(w0), bflo(w1), bfhi(w1)}; }
      v[i][hh] = rs + gt * t; }
  }
  ST(10);
  if (FINAL) {
#pragma unroll
    for (int i = 0; i < 2; ++i) if (i == 0 || two) {
      *(u32x2*)(out + (size_t)rown * D_ + colA[i]) = (u32x2){pk2(v[i][0][0], v[i][0][1]), pk2(v[i][0][2], v[i][0][3])};
      *(u32x2*)(out + (size_t)rown * D_ + colB[i]) = (u32x2){pk2(v[i][1][0], v[i][1][1]), pk2(v[i][1][2], v[i][1][3])}; }
  } else {
    LAS float* st = (LAS float*)(lds + P_T);
    auto s8 = [](const f32x4& a, const f32x4& b) { return ((a[0] + a[1]) + (a[2] + a[3])) + ((b[0] + b[1]) + (b[2] + b[3])); };
    float s = s8(v[0][0], v[0][1]) + (two ? s8(v[1][0], v[1][1]) : 0.f);
    s = sum_x8_16_32(s);
    if (lane < 8) st[wid * 8 + lane] = s;
    asm volatile("s_waitcnt lgkmcnt(0)\n\ts_barrier" ::: "memory");
    float tot = 0.f;
#pragma unroll
    for (int w = 0; w < 8; ++w) tot += st[w * 8 + r8];
    const float mean = tot * (1.f / 576.f);
    f32x4 d[2][2];
#pragma unroll
    for (int i = 0; i < 2; ++i) { d[i][0] = v[i][0] - mean; d[i][1] = v[i][1] - mean; }
    float qq = s8(d[0][0] * d[0][0], d[0][1] * d[0][1]) + (two ? s8(d[1][0] * d[1][0], d[1][1] * d[1][1]) : 0.f);
    qq = sum_x8_16_32(qq);
    if (lane < 8) st[64 + wid * 8 + lane] = qq;
    asm volatile("s_waitcnt lgkmcnt(0)\n\ts_barrier" ::: "memory");
    float tq = 0.f;
#pragma unroll
    for (int w = 0; w < 8; ++w) tq += st[64 + w * 8 + r8];
    const float rstd = __builtin_amdgcn_rsqf(tq * (1.f / 576.f) + 1e-5f);
#pragma unroll
    for (int i = 0; i < 2; ++i) if (i == 0 || two) {
      const f32x4 h0 = d[i][0] * rstd * (sc[i][0] + 1.f) + sh[i][0], h1 = d[i][1] * rstd * (sc[i][1] + 1.f) + sh[i][1];
      const int kc = i ? 64 + (tid >> 3) : (tid >> 3);
      st16c(ws + WS_H2, (unsigned)(((rown >> 5) * 36 + (kc >> 1)) * 1024 + ((kc & 1) * 32 + (rown & 31)) * 16), (u32x4){pk2(h0[0], h0[1]), pk2(h0[2], h0[3]), pk2(h1[0], h1[1]), pk2(h1[2], h1[3])});
      char* xb = ws + WS_X1R + (size_t)blockIdx.x * 576 * 32 + (i ? 512 + (tid & 63) : tid) * 32;
      *(f32x4*)xb = v[i][0]; *(f32x4*)(xb + 16) = v[i][1]; }
  }
  ST_END(FINAL ? 3 : 1);
}

constexpr int C_STG = 9216 + 128 + 9216, C_UOFF = 9216 + 128, C_NSTG = 4;
DI float geglu(float g, float u) {
  const float c0 = -2.f * 0.7978845608028654f * 1.4426950408889634f, c1 = c0 * 0.044715f;
  const float e = __builtin_amdgcn_exp2f(g * (c0 + c1 * g * g));
  return g * __builtin_amdgcn_rcpf(1.f + e) * u;
}
#define C_LA 6
DI void ffn1_prefetch(const MainArgs& ar, int wid) {
  const int lane = lane_id();
  const int x = blockIdx.x & 7, j = blockIdx.x >> 3, ct = 4 * x + (j & 3);
  unsigned wsrc3[3];
#pragma unroll
  for (int e = 0; e < 3; ++e) { const int o = 64 * e + lane, row = o / 18, ch = o - row * 18; wsrc3[e] = (16 * (row >> 2) + (row & 3)) * (FF * 2) + ch * 16; }
  const char* Ww = (wid < 4 ? ar.wg : ar.wu) + (size_t)(wid & 3) * 4 * (FF * 2) + ct * 288;
  const unsigned wdst = C_BASE + (wid < 4 ? 0 : C_UOFF) + (wid & 3) * 2304;
#pragma unroll
  for (int s = 0; s < 3; ++s) { const char* wsrc = Ww + (size_t)(32 * s) * (FF * 2); const unsigned m = s * C_STG + wdst;
    dma16<0>(m, wsrc3[0], wsrc); dma16<0>(m + 1024, wsrc3[1], wsrc); if (lane < 16) dma16<0>(m + 2048, wsrc3[2], wsrc); }
}
DI void phase_ffn1(const MainArgs& ar, LAS char* lds, const int wid DBG_PARAM) {
  PH_BEGIN
  char* const ws = ar.ws; const char* wg = ar.wg; const char* wu = ar.wu;
  const int lane = lane_id(), tid = wid * 64 + lane;
  const int x = blockIdx.x & 7, j = blockIdx.x >> 3, ct = 4 * x + (j & 3), rt = j >> 2;
  const int rb = rt * 8 + wid;
  const int h = lane >> 5, blk = (lane >> 4) & 1, q = (lane & 15) >> 2, p = lane & 3;
  unsigned wsrc3[3], wsrc3b[3];
#pragma unroll
  for (int e = 0; e < 3; ++e) { const int o = 64 * e + lane, row = o / 18, ch = o - row * 18;
    wsrc3[e] = (16 * (row >> 2) + (row & 3)) * (FF * 2) + ch * 16; wsrc3b[e] = (144 * (row >> 2) + (row & 3)) * (FF * 2) + ch * 16; }
  const char* Ww = (wid < 4 ? wg : wu) + (size_t)(wid & 3) * 4 * (FF * 2) + ct * 288;
  const unsigned wdst = C_BASE + (wid < 4 ? 0 : C_UOFF) + (wid & 3) * 2304;
  const char* hw = ws + WS_H2 + (size_t)rb * 36 * 1024;
  const unsigned lane16 = lane * 16;
  bf16x8 af[18][2];
  auto issue_piece = [&](auto S, auto E) { constexpr int s = S, e = E;
    constexpr int kbase = s < 16 ? 144 * (s >> 2) + 32 * (s & 3) : 288 * (s - 16) + 128;
    const char* wsrc = Ww + (size_t)kbase * (FF * 2); const unsigned m = (s % C_NSTG) * C_STG + wdst;
    if constexpr (e == 0) dma16<0>(m, s < 16 ? wsrc3[0] : wsrc3b[0], wsrc);
    if constexpr (e == 1) dma16<0>(m + 1024, s < 16 ? wsrc3[1] : wsrc3b[1], wsrc);
    if constexpr (e == 2) { if (lane < 16) dma16<0>(m + 2048, s < 16 ? wsrc3[2] : wsrc3b[2], wsrc); }
    if constexpr (e == 3) gload16c<0>(af[s][0], lane16, hw + s * 2048);
    if constexpr (e == 4) gload16c<1024>(af[s][1], lane16, hw + s * 2048); };
  static_for<3>([&](auto S) { issue_piece(S, integral_constant<int, 3>{}); issue_piece(S, integral_constant<int, 4>{}); });
  unsigned tb[C_NSTG];
#pragma unroll
  for (int i = 0; i < C_NSTG; ++i) { tb[i] = C_BASE + blk * C_UOFF + 288 * (8 * h + q) + 8 * p + i * C_STG; pin(tb[i]); }
  f32x16 acc[9] = {};
  constexpr int LA = C_LA, NIT = 18 * 18;
  bf16x8 wf[LA + 1];
  auto rd = [&](auto I) { constexpr int i = I; constexpr int s = i / 18, ks = (i % 18) / 9, f = i % 9;
    const bf16x4 lo = tr4(lds + tb[s % C_NSTG] + 288 * 16 * ks + 32 * f), hi = tr4(lds + tb[s % C_NSTG] + 288 * (16 * ks + 4) + 32 * f);
    wf[i % (LA + 1)] = cat(lo, hi); };
  SB(); vm_bar<4>(); pin(af[0][0]); pin(af[0][1]); revalidate<1024>(af[0][0], af[0][1], lane16, lane16, hw); SB();
  ST(1);
  static_for<LA>([&](auto I) { rd(I); });
  SB();
  static_for<NIT>([&](auto I) { constexpr int i = I; constexpr int s = i / 18, r = i % 18, ks = r / 9, f = r % 9;
    if constexpr (r == 0 && s > 0) {
      constexpr int outst = (s + 2 < 18) ? 5 : 0;
      vm_bar<outst>(); pin(af[s][0]); pin(af[s][1]);
      revalidate<1024>(af[s][0], af[s][1], lane16, lane16, hw + s * 2048);
      if constexpr (s == 4) ST(2);
      if constexpr (s == 8) ST(3);
      if constexpr (s == 12) ST(4);
      if constexpr (s == 16) ST(5);
      if constexpr (s == 17) proj_prefetch(ar.wd, 18, wid);
      SB();
    }
    if constexpr (i + LA < NIT) rd(integral_constant<int, i + LA>{});
    acc[f] = mfma32(wf[i % (LA + 1)], af[s][ks], acc[f]);
    if constexpr (s + 3 < 18 && (r % 3) == 1 && r / 3 < 5) issue_piece(integral_constant<int, s + 3>{}, integral_constant<int, r / 3>{});
    SB();
  });
  ST(6);
  char* ob = ws + WS_H + ((size_t)rb * 288 + ct * 9) * 1024;
#pragma unroll
  for (int f = 0; f < 9; ++f) { u32x4 w;
#pragma unroll
    for (int i = 0; i < 4; ++i) w[i] = pk2(geglu(acc[f][2 * i], acc[f][8 + 2 * i]), geglu(acc[f][2 * i + 1], acc[f][8 + 2 * i + 1]));
    st16c(ob, lane16 + f * 1024, w); }
  signal_done(ws, CNT_H + rt * 4 + (ct >> 3), wid);
  ST_END(2);
}

__global__ __launch_bounds__(512, 2) void k_main(MainArgs ar DBG_PARAM) {
  extern __shared__ __attribute__((aligned(16))) char lds_[];
  LAS char* lds = (LAS char*)lds_;
  const int wid = rfl(threadIdx.x >> 6);
  phase_attn(ar, lds, wid PH_DBG);
  phase_proj<false>(ar, lds, wid PH_DBG);
  phase_ffn1(ar, lds, wid PH_DBG);
  phase_proj<true>(ar, lds, wid PH_DBG);
}

extern "C" void kernel_launch(void* const* d_in, const int* in_sizes, int n_in, void* d_out, int out_size, void* d_ws, size_t ws_size, hipStream_t stream) {
  const unsigned short* x = (const unsigned short*)d_in[0];
  const unsigned short* c = (const unsigned short*)d_in[9]; const unsigned short* wmod = (const unsigned short*)d_in[10];
  char* ws = (char*)d_ws;
  (void)hipFuncSetAttribute((const void*)k_pre, hipFuncAttributeMaxDynamicSharedMemorySize, PRE_LDS);
  (void)hipFuncSetAttribute((const void*)k_main, hipFuncAttributeMaxDynamicSharedMemorySize, MAIN_LDS);
  const char* kc = (const char*)d_in[1]; const char* vc = (const char*)d_in[2];
  const char* wq = (const char*)d_in[3]; const char* wo = (const char*)d_in[4];
  const char* wg = (const char*)d_in[6]; const char* wu = (const char*)d_in[7]; const char* wd = (const char*)d_in[8];
  k_pre<<<PRE_RQ + PRE_MOD + PRE_LN, 512, PRE_LDS, stream>>>(c, wmod, x, wq, ws DBG_ARG);
  MainArgs ar{kc, vc, wg, wu, wo, wd, x, (unsigned short*)d_out, ws};
  k_main<<<256, 512, MAIN_LDS, stream>>>(ar DBG_ARG);
}
```

```cpp
#include <hip/hip_runtime.h>
#include <stdint.h>
#include <utility>

#define DI __device__ __forceinline__
#define LAS __attribute__((address_space(3)))
#define GAS __attribute__((address_space(1)))
typedef __bf16 bf16x8 __attribute__((ext_vector_type(8)));
typedef __bf16 bf16x4 __attribute__((ext_vector_type(4)));
typedef __bf16 bf16x2 __attribute__((ext_vector_type(2)));
typedef float f32x2 __attribute__((ext_vector_type(2)));
typedef float f32x4 __attribute__((ext_vector_type(4)));
typedef float f32x16 __attribute__((ext_vector_type(16)));
typedef unsigned u32x2 __attribute__((ext_vector_type(2)));
typedef unsigned u32x4 __attribute__((ext_vector_type(4)));
using std::integral_constant;

constexpr int M_ = 2048, D_ = 576, SK = 2048, WIN = 512, FF = 4608;

constexpr size_t WS_MOD = 0;
constexpr size_t WS_STAT = 16384;
constexpr size_t WS_H1 = WS_STAT + 149760 + 3840;
constexpr size_t WS_O = WS_H1 + (size_t)64 * 36 * 1024;
constexpr size_t WS_X1 = WS_O + (size_t)64 * 256 * 1024;
constexpr size_t WS_H2 = WS_X1 + (size_t)M_ * D_ * 4;
constexpr size_t WS_H = WS_H2 + (size_t)64 * 36 * 1024;
constexpr size_t WS_WQ = WS_H + (size_t)64 * 288 * 1024;
constexpr size_t WS_BP = WS_WQ + (size_t)32 * 36 * 4 * 1024;
constexpr size_t WS_CNT = WS_BP + (size_t)18 * 4096 * 4;
constexpr size_t WS_X1R = WS_CNT + 128 * 256;
constexpr size_t WS_PB = WS_X1R + (size_t)256 * 576 * 32;
constexpr size_t WS_PD = WS_PB + (size_t)16 * 16 * 16 * 2304;
constexpr size_t WS_END = WS_PD + (size_t)16 * 16 * 16 * 2304;

template <int... Is, class F> DI void static_for_impl(std::integer_sequence<int, Is...>, F&& f) { (f(std::integral_constant<int, Is>{}), ...); }
template <int N, class F> DI void static_for(F&& f) { static_for_impl(std::make_integer_sequence<int, N>{}, f); }

DI float bflo(unsigned w) { return __uint_as_float(w << 16); }
DI float bfhi(unsigned w) { return __uint_as_float(w & 0xffff0000u); }
DI unsigned pk2(float lo, float hi) { f32x2 v = {lo, hi}; bf16x2 b = __builtin_convertvector(v, bf16x2); return __builtin_bit_cast(unsigned, b); }
DI unsigned rfl(unsigned x) { return __builtin_amdgcn_readfirstlane(x); }
DI int lane_id() { int l; asm volatile("v_mbcnt_lo_u32_b32 %0, -1, 0\n\tv_mbcnt_hi_u32_b32 %0, -1, %0" : "=v"(l)); return l; }
DI void SB() { __builtin_amdgcn_sched_barrier(0); }

template <int IMM> DI void dma16(unsigned m0v, unsigned voff, const void* sbase) {
  asm volatile("s_mov_b32 m0, %0\n\ts_nop 0\n\tglobal_load_lds_dwordx4 %1, %2 offset:%3" ::"s"(m0v), "v"(voff), "s"(sbase), "i"(IMM) : "memory");
}
DI void dma4(unsigned m0v, unsigned voff, const void* sbase) {
  asm volatile("s_mov_b32 m0, %0\n\ts_nop 0\n\tglobal_load_lds_dword %1, %2" ::"s"(m0v), "v"(voff), "s"(sbase) : "memory");
}
template <int IMM, class T> DI void gload16(T& dst, unsigned voff, const void* sbase) {
  asm volatile("global_load_dwordx4 %0, %1, %2 offset:%3" : "=v"(dst) : "v"(voff), "s"(sbase), "i"(IMM));
}
#define LDF " sc1"
#define STF " sc1"
#define SC1L_S LDF
#define SC1S 1
template <int IMM, class T> DI void gload16c(T& dst, unsigned voff, const void* sbase) {
  asm volatile("global_load_dwordx4 %0, %1, %2 offset:%3" SC1L_S : "=v"(dst) : "v"(voff), "s"(sbase), "i"(IMM));
}
DI void st16c(char* base, unsigned voff, u32x4 v) {
  asm volatile("global_store_dwordx4 %0, %1, %2" STF "\n\ts_nop 1" ::"v"(voff), "v"(v), "s"(base) : "memory");
}
template <int N> DI void wait_vm() { asm volatile("s_waitcnt vmcnt(%0)" ::"i"(N) : "memory"); }
template <int N> DI void vm_bar() { asm volatile("s_waitcnt vmcnt(%0)\n\ts_barrier" ::"i"(N) : "memory"); }
template <int N> DI void vm_lgkm_bar() { asm volatile("s_waitcnt vmcnt(%0) lgkmcnt(0)\n\ts_barrier" ::"i"(N) : "memory"); }
DI void bar() { asm volatile("s_barrier" ::: "memory"); }
template <class T> DI void pin(T& a) { asm volatile("" : "+v"(a)); }
DI bf16x4 tr4(LAS char* p) { return __builtin_amdgcn_ds_read_tr16_b64_v4bf16((LAS bf16x4*)p); }
DI bf16x8 cat(bf16x4 a, bf16x4 b) { return __builtin_shufflevector(a, b, 0, 1, 2, 3, 4, 5, 6, 7); }
DI f32x16 mfma32(bf16x8 a, bf16x8 b, f32x16 c) { return __builtin_amdgcn_mfma_f32_32x32x16_bf16(a, b, c, 0, 0, 0); }
DI f32x4 mfma16(bf16x8 a, bf16x8 b, f32x4 c) { return __builtin_amdgcn_mfma_f32_16x16x32_bf16(a, b, c, 0, 0, 0); }
DI float max3f(float a, float b, float c) { float r; asm("v_max3_f32 %0, %1, %2, %3" : "=v"(r) : "v"(a), "v"(b), "v"(c)); return r; }
DI float max2f(float a, float b) { float r; asm("v_max_f32_e32 %0, %1, %2" : "=v"(r) : "v"(a), "v"(b)); return r; }
template <int CTRL> DI float dppf(float v) { return __builtin_bit_cast(float, __builtin_amdgcn_update_dpp(0, __builtin_bit_cast(int, v), CTRL, 0xf, 0xf, true)); }
DI float sum8(float v) { v += dppf<0xB1>(v); v += dppf<0x4E>(v); v += dppf<0x141>(v); return v; }
DI f32x16 splat16(float v) { f32x16 r; for (int i = 0; i < 16; ++i) r[i] = v; return r; }

#define ST_DECL
#define ST(i)
#define ST_END(kid)
#define DBG_PARAM
#define DBG_ARG
#define PH_BEGIN
#define PH_DBG

constexpr float QSCALE = 0.08838834764831845f * 1.4426950408889634f;
constexpr int PRE_RQ = 144, PRE_MOD = 54, PRE_CLR = 32;
constexpr int PRE_LDS = 32768;
DI float silu_f(float cv) { return cv * __builtin_amdgcn_rcpf(1.f + __builtin_amdgcn_exp2f(-1.4426950408889634f * cv)); }
DI float sum_x8_16_32(float v) {
  v += dppf<0x128>(v);
  { auto rr = __builtin_amdgcn_permlane16_swap(__float_as_uint(v), __float_as_uint(v), false, false); v = __uint_as_float(rr[0]) + __uint_as_float(rr[1]); }
  { auto rr = __builtin_amdgcn_permlane32_swap(__float_as_uint(v), __float_as_uint(v), false, false); v = __uint_as_float(rr[0]) + __uint_as_float(rr[1]); }
  return v;
}
__global__ __launch_bounds__(512) void k_pre(const unsigned short* __restrict__ c, const unsigned short* __restrict__ wmod, const unsigned short* __restrict__ xin, const char* __restrict__ wq, char* __restrict__ ws DBG_PARAM) {
  ST_DECL
  extern __shared__ __attribute__((aligned(16))) char lds_[];
  LAS char* lds = (LAS char*)lds_;
  const int tid = threadIdx.x, lane = tid & 63, wid = rfl(tid >> 6);
  int b = blockIdx.x;
  if (b < PRE_MOD) {
    LAS float* sl = (LAS float*)lds; LAS float* red = (LAS float*)(lds + 2304);
    const int rg = tid >> 3, col0 = b * 64 + (tid & 7) * 8;
    u32x4 w[9]; unsigned cw;
    asm volatile("global_load_dword %0, %1, %2" : "=v"(cw) : "v"((unsigned)(tid < 288 ? tid : 0) * 4), "s"(c));
#pragma unroll
    for (int i = 0; i < 9; ++i) gload16<0>(w[i], (unsigned)((rg + 64 * i) * 3456 + col0) * 2, wmod);
    asm volatile("s_waitcnt vmcnt(9)" : "+v"(cw));
    if (tid < 288) *(LAS f32x2*)(sl + 2 * tid) = (f32x2){silu_f(bflo(cw)), silu_f(bfhi(cw))};
    asm volatile("s_waitcnt vmcnt(0) lgkmcnt(0)\n\ts_barrier" ::: "memory");
    float acc[8] = {};
#pragma unroll
    for (int i = 0; i < 9; ++i) { pin(w[i]); const float s = sl[rg + 64 * i];
#pragma unroll
      for (int j = 0; j < 4; ++j) { acc[2 * j] += s * bflo(w[i][j]); acc[2 * j + 1] += s * bfhi(w[i][j]); } }
#pragma unroll
    for (int j = 0; j < 8; ++j) acc[j] = sum_x8_16_32(acc[j]);
    if (lane < 8) { *(LAS f32x4*)(red + wid * 64 + lane * 8) = (f32x4){acc[0], acc[1], acc[2], acc[3]}; *(LAS f32x4*)(red + wid * 64 + lane * 8 + 4) = (f32x4){acc[4], acc[5], acc[6], acc[7]}; }
    asm volatile("s_waitcnt lgkmcnt(0)\n\ts_barrier" ::: "memory");
    if (tid < 64) { float t = 0.f;
#pragma unroll
      for (int w2 = 0; w2 < 8; ++w2) t += red[w2 * 64 + tid];
      ((float*)(ws + WS_MOD))[b * 64 + tid] = t; }
    ST_END(5);
    return;
  }
  b -= PRE_MOD;
  if (b < PRE_CLR) {
    const int cc = lane & 7, row = (b * 8 + wid) * 8 + (lane >> 3);
    u32x4 xv[9];
#pragma unroll
    for (int i = 0; i < 9; ++i) xv[i] = *(const u32x4*)(xin + (size_t)row * D_ + (cc + 8 * i) * 8);
    if (tid < 64) *(u32x4*)(ws + WS_CNT + (size_t)(b * 64 + tid) * 16) = (u32x4){0u, 0u, 0u, 0u};
#pragma unroll
    for (int i = 0; i < 9; ++i) st16c(ws + WS_H1, (unsigned)((b * 9 + i) * 8192 + tid * 16), (u32x4){~0u, ~0u, ~0u, ~0u});
    float sm = 0.f;
#pragma unroll
    for (int i = 0; i < 9; ++i)
#pragma unroll
      for (int j = 0; j < 4; ++j) sm += bflo(xv[i][j]) + bfhi(xv[i][j]);
    const float mean = sum8(sm) * (1.f / 576.f);
    float q = 0.f;
#pragma unroll
    for (int i = 0; i < 9; ++i)
#pragma unroll
      for (int j = 0; j < 4; ++j) { const float d0 = bflo(xv[i][j]) - mean, d1 = bfhi(xv[i][j]) - mean; q += d0 * d0 + d1 * d1; }
    const float rstd = __builtin_amdgcn_rsqf(sum8(q) * (1.f / 576.f) + 1e-5f);
    if (cc == 0) ((f32x2*)(ws + WS_STAT))[row] = (f32x2){mean, rstd};
    ST_END(6);
    return;
  }
  b -= PRE_CLR;
  {
    const int u = b * 8 + wid, s32 = u >> 6, cg = u & 63;
    { const int rr = lane >> 3, pp = lane & 7;
      const unsigned v = rr * 8192 + ((((pp >> 1) ^ ((rr >> 1) & 3)) << 1) + (pp & 1)) * 16;
      const char* src = wq + (size_t)s32 * 32 * 8192 + cg * 128; const unsigned m = wid * 4096;
      dma16<0>(m, v, src); dma16<0>(m + 1024, v, src + 8 * 8192); dma16<0>(m + 2048, v, src + 16 * 8192); dma16<0>(m + 3072, v, src + 24 * 8192); }
    wait_vm<0>();
    const int g = lane >> 4, q = (lane & 15) >> 2, p = lane & 3, h = lane >> 5, blk = g & 1, pc = ((p & 1) << 1) | (p >> 1);
    LAS char* scr = lds + wid * 4096;
#pragma unroll
    for (int dbl = 0; dbl < 2; ++dbl)
#pragma unroll
      for (int ks = 0; ks < 2; ++ks) { const int r0 = 16 * ks + 8 * h + q, r1 = r0 + 4, cx = 2 * dbl + blk;
        const bf16x8 w = cat(tr4(scr + 128 * r0 + 32 * (cx ^ ((r0 >> 1) & 3)) + 8 * pc), tr4(scr + 128 * r1 + 32 * (cx ^ ((r1 >> 1) & 3)) + 8 * pc));
        st16c(ws + WS_WQ, (unsigned)((((cg >> 1) * 36 + 2 * s32 + ks) * 4 + 2 * (cg & 1) + dbl) * 1024 + lane * 16), __builtin_bit_cast(u32x4, w)); }
    ST_END(4);
  }
}

constexpr float THR = 8.0f;
constexpr int A_TAB = 131072;
constexpr int A_LDS = 131072 + 2048;
#define A_LA 4
#define Q_LA 5
struct MainArgs { const char* kc; const char* vc; const char* wg; const char* wu; const char* wo; const char* wd; const unsigned short* x; unsigned short* out; char* ws; };
constexpr int P_STG = 9216, P_NSTG = 4, P_T = 73728;
constexpr int C_BASE = 81920;
constexpr int MAIN_LDS = 163840;
constexpr int CNT_CLEAR = 0;
constexpr int CNT_O = 8, CNT_H = 40;
DI void signal_done(char* ws, int idx, int wid) {
  vm_bar<0>();
  if (wid == 0 && lane_id() == 0) __hip_atomic_fetch_add((unsigned*)(ws + WS_CNT) + idx * 64, 1u, __ATOMIC_RELAXED, __HIP_MEMORY_SCOPE_AGENT);
}
DI void wait_count(const char* ws, int idx, unsigned target, int wid) {
  if (wid == 0) {
    const unsigned* cnt = (const unsigned*)(ws + WS_CNT) + idx * 64;
    for (unsigned spin = 0; spin < (1u << 15); ++spin) {
      if (rfl(__hip_atomic_load(cnt, __ATOMIC_RELAXED, __HIP_MEMORY_SCOPE_AGENT)) >= target) break;
      __builtin_amdgcn_s_sleep(1);
    }
  }
  bar();
}
DI void st_empty(char* base, unsigned voff) { st16c(base, voff, (u32x4){~0u, ~0u, ~0u, ~0u}); }
DI bool has_empty(const bf16x8& a, const bf16x8& b) {
  const u32x4 x = __builtin_bit_cast(u32x4, a), y = __builtin_bit_cast(u32x4, b);
  unsigned m = x[0] > x[1] ? x[0] : x[1]; m = m > x[2] ? m : x[2]; m = m > x[3] ? m : x[3]; m = m > y[0] ? m : y[0]; m = m > y[1] ? m : y[1]; m = m > y[2] ? m : y[2]; m = m > y[3] ? m : y[3];
  return __any(m == ~0u);
}
template <int IMM1> DI void revalidate(bf16x8& a, bf16x8& b, unsigned oa, unsigned ob, const char* base) {
  if (__builtin_expect(has_empty(a, b), 0)) {
    for (unsigned spin = 0; spin < (1u << 15); ++spin) {
      __builtin_amdgcn_s_sleep(3);
      asm volatile("global_load_dwordx4 %0, %2, %4 sc1\n\tglobal_load_dwordx4 %1, %3, %4 offset:%5 sc1\n\ts_waitcnt vmcnt(0)" : "+v"(a), "+v"(b) : "v"(oa), "v"(ob), "s"(base), "i"(IMM1) : "memory");
      if (!has_empty(a, b)) break;
    }
  }
}
DI void proj_prefetch(const char* W, int nst, int wid) {
  const int lane = lane_id();
  const int x = blockIdx.x & 7, j = blockIdx.x >> 3, ks = x >> 1, ct = j & 3, rg = wid & 3, kh = wid >> 2;
  unsigned wsrc3[3];
#pragma unroll
  for (int e = 0; e < 3; ++e) { const int o = 64 * e + lane, row = o / 18, ch = o - row * 18; wsrc3[e] = row * 1152 + ch * 16; }
  const char* Ww = W + (size_t)((ks * 2 + kh) * nst * 32 + rg * 8) * 1152 + ct * 288;
  const unsigned wdst = kh * (P_NSTG * P_STG) + rg * 2304;
#pragma unroll
  for (int s = 0; s < 3; ++s) { const char* wsrc = Ww + (size_t)s * 32 * 1152; const unsigned m = s * P_STG + wdst;
    dma16<0>(m, wsrc3[0], wsrc); dma16<0>(m + 1024, wsrc3[1], wsrc); if (lane < 16) dma16<0>(m + 2048, wsrc3[2], wsrc); }
}
DI void phase_attn(const MainArgs& ar, LAS char* lds, const int wid DBG_PARAM) {
  PH_BEGIN
  char* const ws = ar.ws;
  const int lane = lane_id(), tid = wid * 64 + lane;
  const int x = blockIdx.x & 7, j = blockIdx.x >> 3, head = 4 * x + (j & 3), rt = j >> 2, kvh = head >> 1;
  const int rb = rt * 8 + wid;
  const int prb = 2 * (j >> 2) + (x & 1), pown = (j & 3) * 4 + (x >> 1);
  const int h = lane >> 5, r = lane & 31, blk = (lane >> 4) & 1, q = (lane & 15) >> 2, p = lane & 3;
  const int l4 = lane >> 4, pos = lane & 15;
  unsigned ksrc2[2];
#pragma unroll
  for (int e = 0; e < 2; ++e) { const int n = 2 * wid + e, row = 4 * n + l4, ch = pos ^ ((l4 << 2) | (n & 3)); ksrc2[e] = row * 256 + ch * 16 - e * 1024; }
  const char* wq_h = ws + WS_WQ + (size_t)head * 36 * 4096 + wid * 2048;
  const char* kb_ = ar.kc + ((size_t)kvh * SK + (SK - WIN)) * 256;
  const char* vb_ = ar.vc + ((size_t)kvh * SK + (SK - WIN)) * 256;
  const char* h1w = ws + WS_H1 + (size_t)rb * 36 * 1024;
  const unsigned lane16 = lane * 16;
  auto dmaW = [&](int qq, int e) { const unsigned m = 65536 + (qq & 3) * 16384 + wid * 2048; if (e == 0) dma16<0>(m, lane16, wq_h + qq * 16384); else dma16<1024>(m, lane16, wq_h + qq * 16384); };
  auto dmaK8 = [&](int t) { const unsigned m = (t & 3) * 16384 + wid * 2048; const char* s = kb_ + t * 16384; dma16<0>(m, ksrc2[0], s); dma16<1024>(m, ksrc2[1], s); };
  auto dmaV8 = [&](int t) { const unsigned m = 65536 + ((t + 1) & 3) * 16384 + wid * 2048; const char* s = vb_ + t * 16384; dma16<0>(m, ksrc2[0], s); dma16<1024>(m, ksrc2[1], s); };
  bf16x8 g[9][4];
  auto loadG1 = [&](auto QQ, auto E) { constexpr int qq = QQ, e = E; gload16c<e * 1024>(g[qq][e], lane16, h1w + qq * 4096); };
  auto checkG = [&](auto QQ) { constexpr int qq = QQ; static_for<4>([&](auto E) { pin(g[qq][E]); });
    revalidate<1024>(g[qq][0], g[qq][1], lane16, lane16, h1w + qq * 4096); revalidate<1024>(g[qq][2], g[qq][3], lane16, lane16, h1w + qq * 4096 + 2048); };
  auto loadG = [&](auto QQ) { static_for<4>([&](auto E) { loadG1(QQ, E); }); };
  auto mark = [&](auto MI) { constexpr int mi = MI;
    if constexpr (mi < 2) { const int rown = prb * 128 + pown * 8 + (tid & 7), kc = mi == 0 ? (tid >> 3) : 64 + (lane >> 3);
      st_empty(ws + WS_H2, (unsigned)(((rown >> 5) * 36 + (kc >> 1)) * 1024 + ((kc & 1) * 32 + (rown & 31)) * 16)); }
    else { const int n = tid + 512 * (mi - 2), n2 = n < 2304 ? n : n - 2304;
      st_empty(ws + WS_PB + (size_t)prb * 16 * 16 * 2304 + (size_t)pown * 2304, (unsigned)((n < 2304 ? 0 : (unsigned)(WS_PD - WS_PB)) + (n2 / 144) * 36864 + (n2 % 144) * 16)); } };
  {
    const int row = rt * 256 + (8 * (j & 3) + x) * 8 + (tid & 7), u0 = tid >> 3, u1 = 64 + (lane >> 3);
    u32x4 xv[2]; f32x4 sa[2][2], ca[2][2]; f32x2 mr;
    asm volatile("global_load_dwordx2 %0, %1, %2" : "=v"(mr) : "v"((unsigned)row * 8), "s"(ws + WS_STAT));
    gload16<0>(xv[0], (unsigned)(row * D_ + u0 * 8) * 2, ar.x); gload16<0>(xv[1], (unsigned)(row * D_ + u1 * 8) * 2, ar.x);
#pragma unroll
    for (int i = 0; i < 2; ++i) { const unsigned o = (i ? u1 : u0) * 32; gload16<0>(sa[i][0], o, ws + WS_MOD); gload16<16>(sa[i][1], o, ws + WS_MOD); gload16<0>(ca[i][0], o, ws + WS_MOD + 2304); gload16<16>(ca[i][1], o, ws + WS_MOD + 2304); }
    dmaW(0, 0); dmaW(0, 1); dmaW(1, 0); dmaW(1, 1); dmaW(2, 0); dmaW(2, 1); dmaK8(0);
    wait_vm<8>(); pin(mr);
    const float rq = mr[1] * QSCALE;
#pragma unroll
    for (int i = 0; i < 2; ++i) { pin(xv[i]); pin(sa[i][0]); pin(sa[i][1]); pin(ca[i][0]); pin(ca[i][1]);
      const f32x4 v0 = {bflo(xv[i][0]), bfhi(xv[i][0]), bflo(xv[i][1]), bfhi(xv[i][1])}, v1 = {bflo(xv[i][2]), bfhi(xv[i][2]), bflo(xv[i][3]), bfhi(xv[i][3])};
      const f32x4 h0 = (v0 - mr[0]) * rq * (ca[i][0] + 1.f) + sa[i][0] * QSCALE, h1v = (v1 - mr[0]) * rq * (ca[i][1] + 1.f) + sa[i][1] * QSCALE; const int u = i ? u1 : u0;
      st16c(ws + WS_H1, (unsigned)(((row >> 5) * 36 + (u >> 1)) * 1024 + ((u & 1) * 32 + (row & 31)) * 16), (u32x4){pk2(h0[0], h0[1]), pk2(h0[2], h0[3]), pk2(h1v[0], h1v[1]), pk2(h1v[2], h1v[3])}); }
  }
  ST(10);
#define H1_SLEEP 16
  __builtin_amdgcn_s_sleep(H1_SLEEP);
  for (unsigned spin = 0; spin < (1u << 15); ++spin) {
    loadG(integral_constant<int, 0>{}); wait_vm<0>();
    static_for<4>([&](auto E) { pin(g[0][E]); });
    if (!has_empty(g[0][0], g[0][1]) && !has_empty(g[0][2], g[0][3])) break;
    __builtin_amdgcn_s_sleep(2);
  }
  ST(11);
  loadG(integral_constant<int, 1>{}); loadG(integral_constant<int, 2>{});
  unsigned wb0 = 65536 + lane16;
  pin(wb0);
  f32x16 acc[4] = {};
  {
    constexpr int LA = Q_LA, NIT = 9 * 16;
    bf16x8 wf[LA + 1];
    auto rd = [&](auto I) { constexpr int i = I; constexpr int qq = i / 16, ks = (i % 16) / 4, db = i % 4;
      wf[i % (LA + 1)] = *(LAS bf16x8*)(lds + wb0 + (qq & 3) * 16384 + ks * 4096 + db * 1024); };
    SB(); bar();
    SB();
    ST(1);
    static_for<LA>([&](auto I) { rd(I); });
    SB();
    static_for<NIT>([&](auto I) { constexpr int i = I; constexpr int qq = i / 16, rr = i % 16, ks = rr / 4, db = rr % 4;
      if constexpr (rr == 0 && qq > 0) {
        constexpr int NW[9] = {8, 10, 10, 10, 10, 10, 10, 8, 8};
        vm_bar<NW[qq]>();
        checkG(integral_constant<int, qq>{});
        if constexpr (qq == 4) ST(2);
        SB();
      }
      if constexpr (i + LA < NIT) rd(integral_constant<int, i + LA>{});
      acc[db] = mfma32(wf[i % (LA + 1)], g[qq][ks], acc[db]);
      if constexpr (qq + 3 < 9) {
        if constexpr (rr == 1) dmaW(qq + 3, 0);
        if constexpr (rr == 3) dmaW(qq + 3, 1);
        if constexpr (rr >= 5 && rr <= 11 && (rr & 1)) loadG1(integral_constant<int, qq + 3>{}, integral_constant<int, (rr - 5) / 2>{});
      } else {
        if constexpr (rr == 1) dmaV8(qq - 6);
        if constexpr (rr == 7) dmaK8(qq - 5);
      }
      SB();
    });
  }
  ST(3);
  bf16x8 qf[8];
#pragma unroll
  for (int db = 0; db < 4; ++db)
#pragma unroll
    for (int s = 0; s < 2; ++s) { u32x4 w;
#pragma unroll
      for (int i = 0; i < 4; ++i) w[i] = pk2(acc[db][8 * s + 2 * i], acc[db][8 * s + 2 * i + 1]);
      qf[2 * db + s] = __builtin_bit_cast(bf16x8, w); }
  const unsigned kb0 = 256 * r + 16 * (h ^ (((r & 3) << 2) | ((r >> 2) & 3)));
  const unsigned vb0 = 65536 + 256 * (4 * h + q) + 16 * ((q << 2) | ((2 * blk + (p >> 1)) ^ h)) + 8 * (p & 1);
  unsigned ka[8], va[4][2];
#pragma unroll
  for (int s = 0; s < 8; ++s) ka[s] = kb0 ^ (s << 5);
#pragma unroll
  for (int db = 0; db < 4; ++db) { va[db][0] = vb0 ^ (db << 6); va[db][1] = vb0 ^ (db << 6) ^ 32; }
  f32x16 o[4] = {}; f32x16 S[2][2], negm; unsigned pw[16]; float m; f32x2 l2 = {0.f, 0.f};
  constexpr int LA = A_LA;
  bf16x8 fr[LA + 1];
  struct Map { int g[272]; int n; };
  constexpr Map MP = []() constexpr { Map mp{}; mp.n = 0; for (int G = 0; G < 9 * 32; ++G) { const int t = G / 32 - 1, i = G % 32; if (i < 16 ? (t + 1 <= 7) : (t >= 0 && t <= 7)) mp.g[mp.n++] = G; } return mp; }();
  constexpr int NIT = MP.n;
  auto rdA = [&](auto C) { constexpr int c = C; constexpr int G = MP.g[c], t = G / 32 - 1, i = G % 32;
    if constexpr (i < 16) { constexpr int s = i >> 1, kb = i & 1; fr[c % (LA + 1)] = *(LAS bf16x8*)(lds + ka[s] + ((t + 1) & 3) * 16384 + kb * 8192); }
    else { constexpr int ii = i - 16, kk = ii >> 2, db = ii & 3; constexpr int so = ((t + 1) & 3) * 16384 + kk * 4096;
      fr[c % (LA + 1)] = cat(tr4(lds + va[db][0] + so), tr4(lds + va[db][1] + so + 2048)); } };
  auto rowmax_finish = [&](float a) { auto rr = __builtin_amdgcn_permlane32_swap(__float_as_uint(a), __float_as_uint(a), false, false); return max2f(__uint_as_float(rr[0]), __uint_as_float(rr[1])); };
  float mxa, mxb;
  const unsigned gw = blockIdx.x * 8 + wid;
  auto touch = [&](auto TI) { constexpr int ti = TI; constexpr int m = ti >> 1, half = ti & 1;
    constexpr unsigned per = m == 0 ? 9 : 11, tot = m == 0 ? 36864 : 41472;
    unsigned line = (gw * 2 + half) * per + (lane < (int)per ? lane : 0); if (line >= tot) line = tot - 1;
    dma4(A_TAB + 2048 + wid * 256, line * 128, m == 0 ? ar.wo : (m == 1 ? ar.wg : (m == 2 ? ar.wu : ar.wd))); };
  auto background = [&](auto BI) { constexpr int bi = BI;
    if constexpr (bi < 16) { if constexpr ((bi & 3) < 2) touch(integral_constant<int, (bi >> 2) * 2 + (bi & 1)>{}); else mark(integral_constant<int, (bi >> 2) * 2 + (bi & 1)>{}); }
    else mark(integral_constant<int, bi - 8>{}); };
  SB(); vm_bar<8>(); SB();
  ST(4);
  static_for<LA>([&](auto C) { rdA(C); });
  SB();
  static_for<NIT>([&](auto C) { constexpr int c = C; constexpr int G = MP.g[c], t = G / 32 - 1, i = G % 32;
    constexpr bool firstX = (i == 0) || (t == 7 && i == 16);
    if constexpr (firstX && t >= 0) {
      constexpr int NW[8] = {4, 8, 12, 12, 12, 9, 3, 0};
      float a = rowmax_finish(max2f(mxa, mxb));
      if constexpr (t == 0) {
        m = a; negm = splat16(-a);
#pragma unroll
        for (int r2 = 0; r2 < 16; ++r2) { S[0][0][r2] -= a; S[0][1][r2] -= a; }
      } else if (__builtin_expect(__any(a > THR), 0)) {
        const float dl = fmaxf(a, 0.f), f = __builtin_amdgcn_exp2f(-dl);
        m += dl; negm = splat16(-m); l2 *= f;
#pragma unroll
        for (int r2 = 0; r2 < 16; ++r2) { S[t & 1][0][r2] -= dl; S[t & 1][1][r2] -= dl; }
#pragma unroll
        for (int d = 0; d < 4; ++d)
#pragma unroll
          for (int r2 = 0; r2 < 16; ++r2) o[d][r2] *= f;
      }
      SB(); vm_bar<NW[t]>(); SB();
      if constexpr (t == 7) { if (wid == 0 && lane == 0) __hip_atomic_fetch_add((unsigned*)(ws + WS_CNT) + (CNT_CLEAR + rt) * 64, 1u, __ATOMIC_RELAXED, __HIP_MEMORY_SCOPE_AGENT); }
      if constexpr (t == 3) ST(5);
      if constexpr (t == 4) ST(7);
      if constexpr (t == 5) ST(8);
      if constexpr (t == 7) proj_prefetch(ar.wo, 16, wid);
      if constexpr (t == 7) {
#pragma unroll
        for (int e = 0; e < 32; ++e) S[1][e >> 4][e & 15] = __builtin_amdgcn_exp2f(S[1][e >> 4][e & 15]);
#pragma unroll
        for (int e = 0; e < 16; ++e) pw[e] = pk2(S[1][e >> 3][(2 * e) & 15], S[1][e >> 3][((2 * e) & 15) + 1]);
        SB();
      }
    }
    if constexpr (c + LA < NIT) rdA(integral_constant<int, c + LA>{});
    if constexpr (i < 16) {
      constexpr int s = i >> 1, kb = i & 1, X = (t + 1) & 1;
      if constexpr (s == 0) { if constexpr (t < 0) S[X][kb] = mfma32(fr[c % (LA + 1)], qf[s], f32x16{}); else S[X][kb] = mfma32(fr[c % (LA + 1)], qf[s], negm); }
      else S[X][kb] = mfma32(fr[c % (LA + 1)], qf[s], S[X][kb]);
      if constexpr (t >= 0) {
        constexpr int e0 = 2 * i, e1 = 2 * i + 1, Y = t & 1;
        S[Y][e0 >> 4][e0 & 15] = __builtin_amdgcn_exp2f(S[Y][e0 >> 4][e0 & 15]);
        S[Y][e1 >> 4][e1 & 15] = __builtin_amdgcn_exp2f(S[Y][e1 >> 4][e1 & 15]);
        if constexpr (i >= 1) { constexpr int e = i - 1; pw[e] = pk2(S[Y][e >> 3][(2 * e) & 15], S[Y][e >> 3][((2 * e) & 15) + 1]); }
        if constexpr (i == 15) { SB(); pw[15] = pk2(S[Y][1][14], S[Y][1][15]); }
        if constexpr (t <= 3 && i == 6) dma16<0>((t & 3) * 16384 + wid * 2048, ksrc2[0], kb_ + (t + 4) * 16384);
        if constexpr (t <= 3 && i == 9) dma16<1024>((t & 3) * 16384 + wid * 2048, ksrc2[1], kb_ + (t + 4) * 16384);
        if constexpr (t <= 4 && i == 12) dma16<0>(65536 + (t & 3) * 16384 + wid * 2048, ksrc2[0], vb_ + (t + 3) * 16384);
        if constexpr (t <= 4 && i == 15) dma16<1024>(65536 + (t & 3) * 16384 + wid * 2048, ksrc2[1], vb_ + (t + 3) * 16384);
      }
      if constexpr (t < 0 && i == 15) {
        asm volatile("s_nop 11" : "+v"(S[0][0]), "+v"(S[0][1]));
        mxa = max3f(S[0][0][0], S[0][0][1], S[0][0][2]); mxb = max3f(S[0][1][0], S[0][1][1], S[0][1][2]);
#pragma unroll
        for (int r2 = 3; r2 < 15; r2 += 2) { mxa = max3f(mxa, S[0][0][r2], S[0][0][r2 + 1]); mxb = max3f(mxb, S[0][1][r2], S[0][1][r2 + 1]); }
        mxa = max3f(mxa, S[0][0][15], S[0][1][15]);
      }
    } else {
      constexpr int ii = i - 16, kk = ii >> 2, db = ii & 3, Y = t & 1, X = (t + 1) & 1;
      const u32x4 w = {pw[4 * kk], pw[4 * kk + 1], pw[4 * kk + 2], pw[4 * kk + 3]};
      o[db] = mfma32(fr[c % (LA + 1)], __builtin_bit_cast(bf16x8, w), o[db]);
      if constexpr (t <= 4 && (ii & 3) == 1 && 4 * t + (ii >> 2) < 19) background(integral_constant<int, 4 * t + (ii >> 2)>{});
      l2 += (f32x2){S[Y][ii >> 3][(2 * ii) & 15], S[Y][ii >> 3][((2 * ii) & 15) + 1]};
      if constexpr (t < 7) {
        if constexpr (ii == 4) { mxa = max3f(S[X][0][0], S[X][0][1], S[X][0][2]); mxb = max3f(S[X][0][3], S[X][0][4], S[X][0][5]); }
        if constexpr (ii == 5) { mxa = max3f(mxa, S[X][0][6], S[X][0][7]); mxb = max3f(mxb, S[X][0][8], S[X][0][9]); }
        if constexpr (ii == 6) { mxa = max3f(mxa, S[X][0][10], S[X][0][11]); mxb = max3f(mxb, S[X][0][12], S[X][0][13]); }
        if constexpr (ii == 7) { mxa = max3f(mxa, S[X][0][14], S[X][0][15]); mxb = max3f(mxb, S[X][1][0], S[X][1][1]); }
        if constexpr (ii == 8) { mxa = max3f(mxa, S[X][1][2], S[X][1][3]); mxb = max3f(mxb, S[X][1][4], S[X][1][5]); }
        if constexpr (ii == 9) { mxa = max3f(mxa, S[X][1][6], S[X][1][7]); mxb = max3f(mxb, S[X][1][8], S[X][1][9]); }
        if constexpr (ii == 10) { mxa = max3f(mxa, S[X][1][10], S[X][1][11]); mxb = max3f(mxb, S[X][1][12], S[X][1][13]); }
        if constexpr (ii == 11) { mxa = max3f(mxa, S[X][1][14], S[X][1][15]); }
      }
    }
    SB();
  });
  ST(12);
  float l = l2[0] + l2[1];
  { auto rr = __builtin_amdgcn_permlane32_swap(__float_as_uint(l), __float_as_uint(l), false, false); l = __uint_as_float(rr[0]) + __uint_as_float(rr[1]); }
  const float inv = __builtin_amdgcn_rcpf(l);
  char* ob = ws + WS_O + ((size_t)rb * 256 + head * 8) * 1024;
#pragma unroll
  for (int db = 0; db < 4; ++db)
#pragma unroll
    for (int s = 0; s < 2; ++s) { u32x4 w;
#pragma unroll
      for (int i = 0; i < 4; ++i) w[i] = pk2(o[db][8 * s + 2 * i] * inv, o[db][8 * s + 2 * i + 1] * inv);
      st16c(ob, lane16 + (2 * db + s) * 1024, w); }
  signal_done(ws, CNT_O + rt * 4 + (head >> 3), wid);
  ST_END(0);
}

#define P_LA 4
DI void ffn1_prefetch(const MainArgs& ar, int wid);
template <bool FINAL>
DI void phase_proj(const MainArgs& ar, LAS char* lds, const int wid DBG_PARAM) {
  PH_BEGIN
  char* const ws = ar.ws; const char* W = FINAL ? ar.wd : ar.wo; const unsigned short* xin = ar.x; unsigned short* out = ar.out;
  constexpr int NS16 = FINAL ? 288 : 256, NST = NS16 / 16;
  const char* act = ws + (FINAL ? WS_H : WS_O);
  const float* mod = (const float*)(ws + WS_MOD);
  char* part = ws + (FINAL ? WS_PD : WS_PB);
  const int lane = lane_id(), tid = wid * 64 + lane, rg = wid & 3, kh = wid >> 2;
  const int x = blockIdx.x & 7, j = blockIdx.x >> 3, ks = x >> 1, rb = 2 * (j >> 2) + (x & 1), ct = j & 3;
  const int own = ct * 4 + ks;
  const int g = lane >> 4, m16 = lane & 15, q4 = (lane & 15) >> 2, p = lane & 3;
  const int kb32 = (ks * 2 + kh) * NST;
  unsigned aoff[2];
#pragma unroll
  for (int a = 0; a < 2; ++a) { const int row = rb * 128 + rg * 32 + a * 16 + m16; aoff[a] = (unsigned)(row >> 5) * (NS16 * 1024) + (row & 31) * 16 + (g >> 1) * 1024 + (g & 1) * 512; }
  const char* actw = act + (size_t)kb32 * 2048;
  unsigned wsrc3[3];
#pragma unroll
  for (int e = 0; e < 3; ++e) { const int o = 64 * e + lane, row = o / 18, ch = o - row * 18; wsrc3[e] = row * 1152 + ch * 16; }
  const char* Ww = W + (size_t)(kb32 * 32 + rg * 8) * 1152 + ct * 288;
  const unsigned wdst = kh * (P_NSTG * P_STG) + rg * 2304;
  bf16x8 af[NST][2];
  auto issue_piece = [&](auto S, auto E) { constexpr int s = S, e = E;
    const char* wsrc = Ww + (size_t)s * 32 * 1152; const unsigned m = (s % P_NSTG) * P_STG + wdst;
    if constexpr (e == 0) dma16<0>(m, wsrc3[0], wsrc);
    if constexpr (e == 1) dma16<0>(m + 1024, wsrc3[1], wsrc);
    if constexpr (e == 2) { if (lane < 16) dma16<0>(m + 2048, wsrc3[2], wsrc); }
    if constexpr (e == 3) gload16c<0>(af[s][0], aoff[0], actw + s * 2048);
    if constexpr (e == 4) gload16c<0>(af[s][1], aoff[1], actw + s * 2048); };
  if (!FINAL && wid != 0) { ffn1_prefetch(ar, wid); if (wid == 1) ffn1_prefetch(ar, 0); }
  wait_count(ws, (FINAL ? CNT_H : CNT_O) + (rb >> 1) * 4 + ks, 8u, wid);
  static_for<3>([&](auto S) { issue_piece(S, integral_constant<int, 3>{}); issue_piece(S, integral_constant<int, 4>{}); });
  unsigned tb[P_NSTG];
#pragma unroll
  for (int i = 0; i < P_NSTG; ++i) tb[i] = kh * (P_NSTG * P_STG) + i * P_STG + 288 * (16 * (g >> 1) + 4 * (g & 1) + q4) + 8 * p;
  f32x4 acc[2][9] = {};
  constexpr int LA = P_LA, NIT = NST * 9;
  bf16x8 wf[LA + 1];
  auto rd = [&](auto I) { constexpr int i = I; constexpr int s = i / 9, c = i % 9;
    wf[i % (LA + 1)] = cat(tr4(lds + tb[s % P_NSTG] + 32 * c), tr4(lds + tb[s % P_NSTG] + 288 * 8 + 32 * c)); };
  constexpr int NPF = 0, NTB = FINAL ? 3 : 2;
  constexpr int TB = FINAL ? 81920 : 137600;
  SB(); vm_bar<4 + NPF>(); pin(af[0][0]); pin(af[0][1]);
#pragma unroll
  for (int e = 0; e < NTB; ++e) { const int n = NTB * wid + e;
    if (!FINAL) dma16<0>(TB + n * 1024, lane * 16, n < 7 ? (const char*)(mod + 1152) + n * 1024 : (const char*)xin + (size_t)(rb * 128 + own * 8) * 1152 + (n - 7) * 1024);
    else { const int n2 = n < 21 ? n : 0; dma16<0>(TB + n2 * 1024, lane * 16, n2 < 3 ? (const char*)(mod + 2880) + n2 * 1024 : ws + WS_X1R + (size_t)blockIdx.x * 576 * 32 + (n2 - 3) * 1024); } }
  SB();
  ST(1);
  static_for<LA>([&](auto I) { rd(I); });
  SB();
  static_for<NIT>([&](auto I) { constexpr int i = I; constexpr int s = i / 9, c = i % 9;
    if constexpr (c == 0 && s > 0) {
      constexpr int outst = s == 1 ? 2 + NPF + NTB + 5 : ((s + 2 < NST) ? 5 : 0);
      vm_bar<outst>(); pin(af[s][0]); pin(af[s][1]);
      if constexpr (s == 8) ST(2);
      SB();
    }
    if constexpr (i + LA < NIT) rd(integral_constant<int, i + LA>{});
    acc[0][c] = mfma16(wf[i % (LA + 1)], af[s][0], acc[0][c]);
    acc[1][c] = mfma16(wf[i % (LA + 1)], af[s][1], acc[1][c]);
    if constexpr (s + 3 < NST && c >= 1 && c <= 5) issue_piece(integral_constant<int, s + 3>{}, integral_constant<int, c - 1>{});
    SB();
  });
  ST(5);
  const int r8 = tid & 7, rown = rb * 128 + own * 8 + r8;
  int colA[2], colB[2];
  colA[0] = 144 * (tid >> 7) + 32 * ((tid >> 5) & 3) + 4 * ((tid >> 3) & 3); colB[0] = colA[0] + 16;
  colA[1] = 288 * ((tid >> 5) & 1) + 128 + 4 * ((tid >> 3) & 3); colB[1] = colA[1] + 144;
  unsigned marked;
  if (!FINAL) asm volatile("global_load_dword %0, %1, %2 sc1" : "=v"(marked) : "v"(0u), "s"(ws + WS_CNT + (size_t)(CNT_CLEAR + (rb >> 1)) * 256));
  {
    LAS char* exw = lds + (rg * 2 + kh) * 9216 + lane * 16; LAS char* exr = lds + (rg * 2 + (kh ^ 1)) * 9216 + lane * 16;
    bar();
    ST(3);
    f32x4 fin[9];
    if (kh) {
#pragma unroll
      for (int c = 0; c < 9; ++c) { *(LAS f32x4*)(exw + c * 1024) = acc[0][c]; fin[c] = acc[1][c]; }
    } else {
#pragma unroll
      for (int c = 0; c < 9; ++c) { *(LAS f32x4*)(exw + c * 1024) = acc[1][c]; fin[c] = acc[0][c]; }
    }
    asm volatile("s_waitcnt lgkmcnt(0)\n\ts_barrier" ::: "memory");
    ST(4);
    char* pw = part + (size_t)rb * 16 * 16 * 2304 + (size_t)own * 2304;
#pragma unroll
    for (int c = 0; c < 9; ++c) fin[c] += *(LAS f32x4*)(exr + c * 1024);
    const unsigned jo = (unsigned)(rg * 4 + kh * 2 + (m16 >> 3)) * 36864, lo = g * 8 + (m16 & 7);
    u32x4 cw[4];
#pragma unroll
    for (int cp = 0; cp < 4; ++cp) cw[cp] = (u32x4){pk2(fin[2 * cp][0], fin[2 * cp][1]), pk2(fin[2 * cp][2], fin[2 * cp][3]), pk2(fin[2 * cp + 1][0], fin[2 * cp + 1][1]), pk2(fin[2 * cp + 1][2], fin[2 * cp + 1][3])};
    const u32x2 w8 = {pk2(fin[8][0], fin[8][1]), pk2(fin[8][2], fin[8][3])};
#pragma unroll
    for (int cp = 0; cp < 4; ++cp) st16c(pw, jo + (cp * 32 + lo) * 16, cw[cp]);
    asm volatile("global_store_dwordx2 %0, %1, %2" STF "\n\ts_nop 1" ::"v"(jo + 2048 + lo * 8), "v"(w8), "s"(pw) : "memory");
  }
  ST(7);
  const char* pr = part + ((size_t)rb * 16 + own) * 16 * 2304;
  const bool two = wid == 0;
  u32x4 pv[4]; u32x2 pq[4][2];
  if (!FINAL) {
    asm volatile("s_waitcnt vmcnt(5)" : "+v"(marked) :: "memory");
    if (!__all(marked >= 32u)) {
      const unsigned* cnt = (const unsigned*)(ws + WS_CNT) + (CNT_CLEAR + (rb >> 1)) * 64;
      for (unsigned spin = 0; spin < (1u << 15); ++spin) { if (__all(__hip_atomic_load(cnt, __ATOMIC_RELAXED, __HIP_MEMORY_SCOPE_AGENT) >= 32u)) break; __builtin_amdgcn_s_sleep(1); }
    }
  }
#define POLLMODE 1
#define P_SLEEP 20
  if (wid == 7) {
    for (unsigned spin = 0; spin < (1u << 15); ++spin) {
      unsigned t; asm volatile("global_load_dword %0, %1, %2 sc1\n\ts_waitcnt vmcnt(0)" : "=&v"(t) : "v"((unsigned)((lane & 15) * 2304 + 2300)), "s"(pr) : "memory");
      if (__all(t != ~0u)) break;
      __builtin_amdgcn_s_sleep(1);
    } }
  bar();
  ST(8);
  for (unsigned spin = 0; spin < (1u << 15); ++spin) {
    const unsigned o0 = (unsigned)((tid >> 7) * 4 * 2304 + (tid & 127) * 16), o1 = (unsigned)(((tid >> 5) & 1) * 8 * 2304 + 2048 + (tid & 31) * 8);
#pragma unroll
    for (int k = 0; k < 4; ++k) gload16c<0>(pv[k], o0 + k * 2304, pr);
    if (two) {
#pragma unroll
      for (int k = 0; k < 4; ++k) {
        asm volatile("global_load_dwordx2 %0, %1, %2" LDF : "=v"(pq[k][0]) : "v"(o1 + k * 2304), "s"(pr));
        asm volatile("global_load_dwordx2 %0, %1, %2" LDF : "=v"(pq[k][1]) : "v"(o1 + (4 + k) * 2304), "s"(pr)); } }
    wait_vm<0>();
    bool full = true;
#pragma unroll
    for (int k = 0; k < 4; ++k) { pin(pv[k]); full &= pv[k][0] != ~0u && pv[k][1] != ~0u && pv[k][2] != ~0u && pv[k][3] != ~0u; }
    if (two) {
#pragma unroll
      for (int k = 0; k < 4; ++k) { pin(pq[k][0]); pin(pq[k][1]); full &= pq[k][0][0] != ~0u && pq[k][0][1] != ~0u && pq[k][1][0] != ~0u && pq[k][1][1] != ~0u; } }
    if (__all(full)) break;
    __builtin_amdgcn_s_sleep(4);
  }
  ST(9);
  f32x4 v[2][2], sc[2][2], sh[2][2];
#pragma unroll
  for (int i = 0; i < 2; ++i) if (i == 0 || two) {
#pragma unroll
    for (int hh = 0; hh < 2; ++hh) { const int col = hh ? colB[i] : colA[i]; f32x4 rs;
      const f32x4 gt = *(LAS f32x4*)(lds + TB + col * 4);
      if (FINAL) rs = *(LAS f32x4*)(lds + TB + 3072 + (i ? 512 + (tid & 63) : tid) * 32 + hh * 16);
      else { const u32x2 xr = *(LAS u32x2*)(lds + TB + 7168 + r8 * 1152 + col * 2); rs = (f32x4){bflo(xr[0]), bfhi(xr[0]), bflo(xr[1]), bfhi(xr[1])};
        sh[i][hh] = *(LAS f32x4*)(lds + TB + (576 + col) * 4); sc[i][hh] = *(LAS f32x4*)(lds + TB + (1152 + col) * 4); }
      f32x4 t = {0.f, 0.f, 0.f, 0.f};
#pragma unroll
      for (int k = 0; k < 4; ++k) { const unsigned w0 = i ? pq[k][hh][0] : pv[k][2 * hh], w1 = i ? pq[k][hh][1] : pv[k][2 * hh + 1]; t += (f32x4){bflo(w0), bfhi(w0), bflo(w1), bfhi(w1)}; }
      v[i][hh] = rs + gt * t; }
  }
  ST(10);
  if (FINAL) {
#pragma unroll
    for (int i = 0; i < 2; ++i) if (i == 0 || two) {
      *(u32x2*)(out + (size_t)rown * D_ + colA[i]) = (u32x2){pk2(v[i][0][0], v[i][0][1]), pk2(v[i][0][2], v[i][0][3])};
      *(u32x2*)(out + (size_t)rown * D_ + colB[i]) = (u32x2){pk2(v[i][1][0], v[i][1][1]), pk2(v[i][1][2], v[i][1][3])}; }
  } else {
    LAS float* st = (LAS float*)(lds + P_T);
    auto s8 = [](const f32x4& a, const f32x4& b) { return ((a[0] + a[1]) + (a[2] + a[3])) + ((b[0] + b[1]) + (b[2] + b[3])); };
    float s = s8(v[0][0], v[0][1]) + (two ? s8(v[1][0], v[1][1]) : 0.f);
    s = sum_x8_16_32(s);
    if (lane < 8) st[wid * 8 + lane] = s;
    asm volatile("s_waitcnt lgkmcnt(0)\n\ts_barrier" ::: "memory");
    float tot = 0.f;
#pragma unroll
    for (int w = 0; w < 8; ++w) tot += st[w * 8 + r8];
    const float mean = tot * (1.f / 576.f);
    f32x4 d[2][2];
#pragma unroll
    for (int i = 0; i < 2; ++i) { d[i][0] = v[i][0] - mean; d[i][1] = v[i][1] - mean; }
    float qq = s8(d[0][0] * d[0][0], d[0][1] * d[0][1]) + (two ? s8(d[1][0] * d[1][0], d[1][1] * d[1][1]) : 0.f);
    qq = sum_x8_16_32(qq);
    if (lane < 8) st[64 + wid * 8 + lane] = qq;
    asm volatile("s_waitcnt lgkmcnt(0)\n\ts_barrier" ::: "memory");
    float tq = 0.f;
#pragma unroll
    for (int w = 0; w < 8; ++w) tq += st[64 + w * 8 + r8];
    const float rstd = __builtin_amdgcn_rsqf(tq * (1.f / 576.f) + 1e-5f);
#pragma unroll
    for (int i = 0; i < 2; ++i) if (i == 0 || two) {
      const f32x4 h0 = d[i][0] * rstd * (sc[i][0] + 1.f) + sh[i][0], h1 = d[i][1] * rstd * (sc[i][1] + 1.f) + sh[i][1];
      const int kc = i ? 64 + (tid >> 3) : (tid >> 3);
      st16c(ws + WS_H2, (unsigned)(((rown >> 5) * 36 + (kc >> 1)) * 1024 + ((kc & 1) * 32 + (rown & 31)) * 16), (u32x4){pk2(h0[0], h0[1]), pk2(h0[2], h0[3]), pk2(h1[0], h1[1]), pk2(h1[2], h1[3])});
      char* xb = ws + WS_X1R + (size_t)blockIdx.x * 576 * 32 + (i ? 512 + (tid & 63) : tid) * 32;
      *(f32x4*)xb = v[i][0]; *(f32x4*)(xb + 16) = v[i][1]; }
  }
  ST_END(FINAL ? 3 : 1);
}

constexpr int C_STG = 9216 + 128 + 9216, C_UOFF = 9216 + 128, C_NSTG = 4;
DI unsigned geglu2(f32x2 g, f32x2 u) {
  const float c0 = -2.f * 0.7978845608028654f * 1.4426950408889634f, c1 = c0 * 0.044715f;
  const f32x2 z = g * (g * g * c1 + c0);
  const f32x2 d = (f32x2){__builtin_amdgcn_exp2f(z[0]), __builtin_amdgcn_exp2f(z[1])} + 1.f;
  const f32x2 o = (g * u) * (f32x2){__builtin_amdgcn_rcpf(d[0]), __builtin_amdgcn_rcpf(d[1])};
  return pk2(o[0], o[1]);
}
#define C_LA 6
DI void ffn1_prefetch(const MainArgs& ar, int wid) {
  const int lane = lane_id();
  const int x = blockIdx.x & 7, j = blockIdx.x >> 3, ct = 4 * x + (j & 3);
  unsigned wsrc3[3];
#pragma unroll
  for (int e = 0; e < 3; ++e) { const int o = 64 * e + lane, row = o / 18, ch = o - row * 18; wsrc3[e] = (16 * (row >> 2) + (row & 3)) * (FF * 2) + ch * 16; }
  const char* Ww = (wid < 4 ? ar.wg : ar.wu) + (size_t)(wid & 3) * 4 * (FF * 2) + ct * 288;
  const unsigned wdst = C_BASE + (wid < 4 ? 0 : C_UOFF) + (wid & 3) * 2304;
#pragma unroll
  for (int s = 0; s < 3; ++s) { const char* wsrc = Ww + (size_t)(32 * s) * (FF * 2); const unsigned m = s * C_STG + wdst;
    dma16<0>(m, wsrc3[0], wsrc); dma16<0>(m + 1024, wsrc3[1], wsrc); if (lane < 16) dma16<0>(m + 2048, wsrc3[2], wsrc); }
}
DI void phase_ffn1(const MainArgs& ar, LAS char* lds, const int wid DBG_PARAM) {
  PH_BEGIN
  char* const ws = ar.ws; const char* wg = ar.wg; const char* wu = ar.wu;
  const int lane = lane_id(), tid = wid * 64 + lane;
  const int x = blockIdx.x & 7, j = blockIdx.x >> 3, ct = 4 * x + (j & 3), rt = j >> 2;
  const int rb = rt * 8 + wid;
  const int h = lane >> 5, blk = (lane >> 4) & 1, q = (lane & 15) >> 2, p = lane & 3;
  unsigned wsrc3[3], wsrc3b[3];
#pragma unroll
  for (int e = 0; e < 3; ++e) { const int o = 64 * e + lane, row = o / 18, ch = o - row * 18;
    wsrc3[e] = (16 * (row >> 2) + (row & 3)) * (FF * 2) + ch * 16; wsrc3b[e] = (144 * (row >> 2) + (row & 3)) * (FF * 2) + ch * 16; }
  const char* Ww = (wid < 4 ? wg : wu) + (size_t)(wid & 3) * 4 * (FF * 2) + ct * 288;
  const unsigned wdst = C_BASE + (wid < 4 ? 0 : C_UOFF) + (wid & 3) * 2304;
  const char* hw = ws + WS_H2 + (size_t)rb * 36 * 1024;
  const unsigned lane16 = lane * 16;
  bf16x8 af[18][2];
  auto issue_piece = [&](auto S, auto E) { constexpr int s = S, e = E;
    constexpr int kbase = s < 16 ? 144 * (s >> 2) + 32 * (s & 3) : 288 * (s - 16) + 128;
    const char* wsrc = Ww + (size_t)kbase * (FF * 2); const unsigned m = (s % C_NSTG) * C_STG + wdst;
    if constexpr (e == 0) dma16<0>(m, s < 16 ? wsrc3[0] : wsrc3b[0], wsrc);
    if constexpr (e == 1) dma16<0>(m + 1024, s < 16 ? wsrc3[1] : wsrc3b[1], wsrc);
    if constexpr (e == 2) { if (lane < 16) dma16<0>(m + 2048, s < 16 ? wsrc3[2] : wsrc3b[2], wsrc); }
    if constexpr (e == 3) gload16c<0>(af[s][0], lane16, hw + s * 2048);
    if constexpr (e == 4) gload16c<1024>(af[s][1], lane16, hw + s * 2048); };
  static_for<3>([&](auto S) { issue_piece(S, integral_constant<int, 3>{}); issue_piece(S, integral_constant<int, 4>{}); });
  unsigned tb[C_NSTG];
#pragma unroll
  for (int i = 0; i < C_NSTG; ++i) { tb[i] = C_BASE + blk * C_UOFF + 288 * (8 * h + q) + 8 * p + i * C_STG; pin(tb[i]); }
  f32x16 acc[9] = {};
  constexpr int LA = C_LA, NIT = 18 * 18;
  bf16x8 wf[LA + 1];
  auto rd = [&](auto I) { constexpr int i = I; constexpr int s = i / 18, ks = (i % 18) / 9, f = i % 9;
    const bf16x4 lo = tr4(lds + tb[s % C_NSTG] + 288 * 16 * ks + 32 * f), hi = tr4(lds + tb[s % C_NSTG] + 288 * (16 * ks + 4) + 32 * f);
    wf[i % (LA + 1)] = cat(lo, hi); };
  SB(); vm_bar<4>(); pin(af[0][0]); pin(af[0][1]); revalidate<1024>(af[0][0], af[0][1], lane16, lane16, hw); SB();
  ST(1);
  static_for<LA>([&](auto I) { rd(I); });
  SB();
  static_for<NIT>([&](auto I) { constexpr int i = I; constexpr int s = i / 18, r = i % 18, ks = r / 9, f = r % 9;
    if constexpr (r == 0 && s > 0) {
      constexpr int outst = (s + 2 < 18) ? 5 : 0;
      vm_bar<outst>(); pin(af[s][0]); pin(af[s][1]);
      revalidate<1024>(af[s][0], af[s][1], lane16, lane16, hw + s * 2048);
      if constexpr (s == 4) ST(2);
      if constexpr (s == 8) ST(3);
      if constexpr (s == 12) ST(4);
      if constexpr (s == 16) ST(5);
      if constexpr (s == 17) proj_prefetch(ar.wd, 18, wid);
      SB();
    }
    if constexpr (i + LA < NIT) rd(integral_constant<int, i + LA>{});
    acc[f] = mfma32(wf[i % (LA + 1)], af[s][ks], acc[f]);
    if constexpr (s + 3 < 18 && (r % 3) == 1 && r / 3 < 5) issue_piece(integral_constant<int, s + 3>{}, integral_constant<int, r / 3>{});
    SB();
  });
  ST(6);
  char* ob = ws + WS_H + ((size_t)rb * 288 + ct * 9) * 1024;
#pragma unroll
  for (int f = 0; f < 9; ++f) { u32x4 w;
#pragma unroll
    for (int i = 0; i < 4; ++i) w[i] = geglu2((f32x2){acc[f][2 * i], acc[f][2 * i + 1]}, (f32x2){acc[f][8 + 2 * i], acc[f][8 + 2 * i + 1]});
    st16c(ob, lane16 + f * 1024, w); }
  signal_done(ws, CNT_H + rt * 4 + (ct >> 3), wid);
  ST_END(2);
}

__global__ __launch_bounds__(512, 2) void k_main(MainArgs ar DBG_PARAM) {
  extern __shared__ __attribute__((aligned(16))) char lds_[];
  LAS char* lds = (LAS char*)lds_;
  const int wid = rfl(threadIdx.x >> 6);
  phase_attn(ar, lds, wid PH_DBG);
  phase_proj<false>(ar, lds, wid PH_DBG);
  phase_ffn1(ar, lds, wid PH_DBG);
  phase_proj<true>(ar, lds, wid PH_DBG);
}

extern "C" void kernel_launch(void* const* d_in, const int* in_sizes, int n_in, void* d_out, int out_size, void* d_ws, size_t ws_size, hipStream_t stream) {
  const unsigned short* x = (const unsigned short*)d_in[0];
  const unsigned short* c = (const unsigned short*)d_in[9]; const unsigned short* wmod = (const unsigned short*)d_in[10];
  char* ws = (char*)d_ws;
  (void)hipFuncSetAttribute((const void*)k_pre, hipFuncAttributeMaxDynamicSharedMemorySize, PRE_LDS);
  (void)hipFuncSetAttribute((const void*)k_main, hipFuncAttributeMaxDynamicSharedMemorySize, MAIN_LDS);
  const char* kc = (const char*)d_in[1]; const char* vc = (const char*)d_in[2];
  const char* wq = (const char*)d_in[3]; const char* wo = (const char*)d_in[4];
  const char* wg = (const char*)d_in[6]; const char* wu = (const char*)d_in[7]; const char* wd = (const char*)d_in[8];
  k_pre<<<PRE_RQ + PRE_MOD + PRE_CLR, 512, PRE_LDS, stream>>>(c, wmod, x, wq, ws DBG_ARG);
  MainArgs ar{kc, vc, wg, wu, wo, wd, x, (unsigned short*)d_out, ws};
  k_main<<<256, 512, MAIN_LDS, stream>>>(ar DBG_ARG);
}
```

```cpp
#include <hip/hip_runtime.h>
#include <stdint.h>
#include <utility>
#include <stdio.h>

#define DI __device__ __forceinline__
#define LAS __attribute__((address_space(3)))
#define GAS __attribute__((address_space(1)))
typedef __bf16 bf16x8 __attribute__((ext_vector_type(8)));
typedef __bf16 bf16x4 __attribute__((ext_vector_type(4)));
typedef __bf16 bf16x2 __attribute__((ext_vector_type(2)));
typedef float f32x2 __attribute__((ext_vector_type(2)));
typedef float f32x4 __attribute__((ext_vector_type(4)));
typedef float f32x16 __attribute__((ext_vector_type(16)));
typedef unsigned u32x2 __attribute__((ext_vector_type(2)));
typedef unsigned u32x4 __attribute__((ext_vector_type(4)));
using std::integral_constant;

constexpr int M_ = 2048, D_ = 576, SK = 2048, WIN = 512, FF = 4608;

constexpr size_t WS_MOD = 0;
constexpr size_t WS_STAT = 16384;
constexpr size_t WS_H1 = WS_STAT + 149760 + 3840;
constexpr size_t WS_O = WS_H1 + (size_t)64 * 36 * 1024;
constexpr size_t WS_X1 = WS_O + (size_t)64 * 256 * 1024;
constexpr size_t WS_H2 = WS_X1 + (size_t)M_ * D_ * 4;
constexpr size_t WS_H = WS_H2 + (size_t)64 * 36 * 1024;
constexpr size_t WS_WQ = WS_H + (size_t)64 * 288 * 1024;
constexpr size_t WS_BP = WS_WQ + (size_t)32 * 36 * 4 * 1024;
constexpr size_t WS_CNT = WS_BP + (size_t)18 * 4096 * 4;
constexpr size_t WS_X1R = WS_CNT + 128 * 256;
constexpr size_t WS_PB = WS_X1R + (size_t)256 * 576 * 32;
constexpr size_t WS_PD = WS_PB + (size_t)16 * 16 * 16 * 2304;
constexpr size_t WS_END = WS_PD + (size_t)16 * 16 * 16 * 2304;

template <int... Is, class F> DI void static_for_impl(std::integer_sequence<int, Is...>, F&& f) { (f(std::integral_constant<int, Is>{}), ...); }
template <int N, class F> DI void static_for(F&& f) { static_for_impl(std::make_integer_sequence<int, N>{}, f); }

DI float bflo(unsigned w) { return __uint_as_float(w << 16); }
DI float bfhi(unsigned w) { return __uint_as_float(w & 0xffff0000u); }
DI unsigned pk2(float lo, float hi) { f32x2 v = {lo, hi}; bf16x2 b = __builtin_convertvector(v, bf16x2); return __builtin_bit_cast(unsigned, b); }
DI unsigned rfl(unsigned x) { return __builtin_amdgcn_readfirstlane(x); }
DI int lane_id() { int l; asm volatile("v_mbcnt_lo_u32_b32 %0, -1, 0\n\tv_mbcnt_hi_u32_b32 %0, -1, %0" : "=v"(l)); return l; }
DI void SB() { __builtin_amdgcn_sched_barrier(0); }

template <int IMM> DI void dma16(unsigned m0v, unsigned voff, const void* sbase) {
  asm volatile("s_mov_b32 m0, %0\n\ts_nop 0\n\tglobal_load_lds_dwordx4 %1, %2 offset:%3" ::"s"(m0v), "v"(voff), "s"(sbase), "i"(IMM) : "memory");
}
DI void dma4(unsigned m0v, unsigned voff, const void* sbase) {
  asm volatile("s_mov_b32 m0, %0\n\ts_nop 0\n\tglobal_load_lds_dword %1, %2" ::"s"(m0v), "v"(voff), "s"(sbase) : "memory");
}
template <int IMM, class T> DI void gload16(T& dst, unsigned voff, const void* sbase) {
  asm volatile("global_load_dwordx4 %0, %1, %2 offset:%3" : "=v"(dst) : "v"(voff), "s"(sbase), "i"(IMM));
}
#define LDF " sc1"
#define STF " sc1"
#define SC1L_S LDF
#define SC1S 1
template <int IMM, class T> DI void gload16c(T& dst, unsigned voff, const void* sbase) {
  asm volatile("global_load_dwordx4 %0, %1, %2 offset:%3" SC1L_S : "=v"(dst) : "v"(voff), "s"(sbase), "i"(IMM));
}
DI void st16c(char* base, unsigned voff, u32x4 v) {
  asm volatile("global_store_dwordx4 %0, %1, %2" STF "\n\ts_nop 1" ::"v"(voff), "v"(v), "s"(base) : "memory");
}
template <int N> DI void wait_vm() { asm volatile("s_waitcnt vmcnt(%0)" ::"i"(N) : "memory"); }
template <int N> DI void vm_bar() { asm volatile("s_waitcnt vmcnt(%0)\n\ts_barrier" ::"i"(N) : "memory"); }
template <int N> DI void vm_lgkm_bar() { asm volatile("s_waitcnt vmcnt(%0) lgkmcnt(0)\n\ts_barrier" ::"i"(N) : "memory"); }
DI void bar() { asm volatile("s_barrier" ::: "memory"); }
template <class T> DI void pin(T& a) { asm volatile("" : "+v"(a)); }
DI bf16x4 tr4(LAS char* p) { return __builtin_amdgcn_ds_read_tr16_b64_v4bf16((LAS bf16x4*)p); }
DI bf16x8 cat(bf16x4 a, bf16x4 b) { return __builtin_shufflevector(a, b, 0, 1, 2, 3, 4, 5, 6, 7); }
DI f32x16 mfma32(bf16x8 a, bf16x8 b, f32x16 c) { return __builtin_amdgcn_mfma_f32_32x32x16_bf16(a, b, c, 0, 0, 0); }
DI f32x4 mfma16(bf16x8 a, bf16x8 b, f32x4 c) { return __builtin_amdgcn_mfma_f32_16x16x32_bf16(a, b, c, 0, 0, 0); }
DI float max3f(float a, float b, float c) { float r; asm("v_max3_f32 %0, %1, %2, %3" : "=v"(r) : "v"(a), "v"(b), "v"(c)); return r; }
DI float max2f(float a, float b) { float r; asm("v_max_f32_e32 %0, %1, %2" : "=v"(r) : "v"(a), "v"(b)); return r; }
template <int CTRL> DI float dppf(float v) { return __builtin_bit_cast(float, __builtin_amdgcn_update_dpp(0, __builtin_bit_cast(int, v), CTRL, 0xf, 0xf, true)); }
DI float sum8(float v) { v += dppf<0xB1>(v); v += dppf<0x4E>(v); v += dppf<0x141>(v); return v; }
DI f32x16 splat16(float v) { f32x16 r; for (int i = 0; i < 16; ++i) r[i] = v; return r; }

#define ST_DECL
#define ST(i)
#define ST_END(kid)
#define DBG_PARAM
#define DBG_ARG
#define PH_BEGIN
#define PH_DBG

constexpr float QSCALE = 0.08838834764831845f * 1.4426950408889634f;
constexpr int PRE_RQ = 144, PRE_MOD = 54, PRE_CLR = 32;
constexpr int PRE_LDS = 32768;
DI float silu_f(float cv) { return cv * __builtin_amdgcn_rcpf(1.f + __builtin_amdgcn_exp2f(-1.4426950408889634f * cv)); }
DI float sum_x8_16_32(float v) {
  v += dppf<0x128>(v);
  { auto rr = __builtin_amdgcn_permlane16_swap(__float_as_uint(v), __float_as_uint(v), false, false); v = __uint_as_float(rr[0]) + __uint_as_float(rr[1]); }
  { auto rr = __builtin_amdgcn_permlane32_swap(__float_as_uint(v), __float_as_uint(v), false, false); v = __uint_as_float(rr[0]) + __uint_as_float(rr[1]); }
  return v;
}
__global__ __launch_bounds__(512) void k_pre(const unsigned short* __restrict__ c, const unsigned short* __restrict__ wmod, const unsigned short* __restrict__ xin, const char* __restrict__ wq, char* __restrict__ ws DBG_PARAM) {
  ST_DECL
  extern __shared__ __attribute__((aligned(16))) char lds_[];
  LAS char* lds = (LAS char*)lds_;
  const int tid = threadIdx.x, lane = tid & 63, wid = rfl(tid >> 6);
  int b = blockIdx.x;
  if (b == 0 && tid == 0) { unsigned long long t; asm volatile("s_memrealtime %0\n\ts_waitcnt lgkmcnt(0)" : "=s"(t) :: "memory"); *(unsigned long long*)(ws + WS_STAT + 32768) = t; }
  if (b < PRE_MOD) {
    LAS float* sl = (LAS float*)lds; LAS float* red = (LAS float*)(lds + 2304);
    const int rg = tid >> 3, col0 = b * 64 + (tid & 7) * 8;
    u32x4 w[9]; unsigned cw;
    asm volatile("global_load_dword %0, %1, %2" : "=v"(cw) : "v"((unsigned)(tid < 288 ? tid : 0) * 4), "s"(c));
#pragma unroll
    for (int i = 0; i < 9; ++i) gload16<0>(w[i], (unsigned)((rg + 64 * i) * 3456 + col0) * 2, wmod);
    asm volatile("s_waitcnt vmcnt(9)" : "+v"(cw));
    if (tid < 288) *(LAS f32x2*)(sl + 2 * tid) = (f32x2){silu_f(bflo(cw)), silu_f(bfhi(cw))};
    asm volatile("s_waitcnt vmcnt(0) lgkmcnt(0)\n\ts_barrier" ::: "memory");
    float acc[8] = {};
#pragma unroll
    for (int i = 0; i < 9; ++i) { pin(w[i]); const float s = sl[rg + 64 * i];
#pragma unroll
      for (int j = 0; j < 4; ++j) { acc[2 * j] += s * bflo(w[i][j]); acc[2 * j + 1] += s * bfhi(w[i][j]); } }
#pragma unroll
    for (int j = 0; j < 8; ++j) acc[j] = sum_x8_16_32(acc[j]);
    if (lane < 8) { *(LAS f32x4*)(red + wid * 64 + lane * 8) = (f32x4){acc[0], acc[1], acc[2], acc[3]}; *(LAS f32x4*)(red + wid * 64 + lane * 8 + 4) = (f32x4){acc[4], acc[5], acc[6], acc[7]}; }
    asm volatile("s_waitcnt lgkmcnt(0)\n\ts_barrier" ::: "memory");
    if (tid < 64) { float t = 0.f;
#pragma unroll
      for (int w2 = 0; w2 < 8; ++w2) t += red[w2 * 64 + tid];
      ((float*)(ws + WS_MOD))[b * 64 + tid] = t; }
    ST_END(5);
    return;
  }
  b -= PRE_MOD;
  if (b < PRE_CLR) {
    const int cc = lane & 7, row = (b * 8 + wid) * 8 + (lane >> 3);
    u32x4 xv[9];
#pragma unroll
    for (int i = 0; i < 9; ++i) xv[i] = *(const u32x4*)(xin + (size_t)row * D_ + (cc + 8 * i) * 8);
    if (tid < 64) *(u32x4*)(ws + WS_CNT + (size_t)(b * 64 + tid) * 16) = (u32x4){0u, 0u, 0u, 0u};
#pragma unroll
    for (int i = 0; i < 9; ++i) st16c(ws + WS_H1, (unsigned)((b * 9 + i) * 8192 + tid * 16), (u32x4){~0u, ~0u, ~0u, ~0u});
    float sm = 0.f;
#pragma unroll
    for (int i = 0; i < 9; ++i)
#pragma unroll
      for (int j = 0; j < 4; ++j) sm += bflo(xv[i][j]) + bfhi(xv[i][j]);
    const float mean = sum8(sm) * (1.f / 576.f);
    float q = 0.f;
#pragma unroll
    for (int i = 0; i < 9; ++i)
#pragma unroll
      for (int j = 0; j < 4; ++j) { const float d0 = bflo(xv[i][j]) - mean, d1 = bfhi(xv[i][j]) - mean; q += d0 * d0 + d1 * d1; }
    const float rstd = __builtin_amdgcn_rsqf(sum8(q) * (1.f / 576.f) + 1e-5f);
    if (cc == 0) ((f32x2*)(ws + WS_STAT))[row] = (f32x2){mean, rstd};
    ST_END(6);
    return;
  }
  b -= PRE_CLR;
  {
    const int u = b * 8 + wid, s32 = u >> 6, cg = u & 63;
    { const int rr = lane >> 3, pp = lane & 7;
      const unsigned v = rr * 8192 + ((((pp >> 1) ^ ((rr >> 1) & 3)) << 1) + (pp & 1)) * 16;
      const char* src = wq + (size_t)s32 * 32 * 8192 + cg * 128; const unsigned m = wid * 4096;
      dma16<0>(m, v, src); dma16<0>(m + 1024, v, src + 8 * 8192); dma16<0>(m + 2048, v, src + 16 * 8192); dma16<0>(m + 3072, v, src + 24 * 8192); }
    wait_vm<0>();
    const int g = lane >> 4, q = (lane & 15) >> 2, p = lane & 3, h = lane >> 5, blk = g & 1, pc = ((p & 1) << 1) | (p >> 1);
    LAS char* scr = lds + wid * 4096;
#pragma unroll
    for (int dbl = 0; dbl < 2; ++dbl)
#pragma unroll
      for (int ks = 0; ks < 2; ++ks) { const int r0 = 16 * ks + 8 * h + q, r1 = r0 + 4, cx = 2 * dbl + blk;
        const bf16x8 w = cat(tr4(scr + 128 * r0 + 32 * (cx ^ ((r0 >> 1) & 3)) + 8 * pc), tr4(scr + 128 * r1 + 32 * (cx ^ ((r1 >> 1) & 3)) + 8 * pc));
        st16c(ws + WS_WQ, (unsigned)((((cg >> 1) * 36 + 2 * s32 + ks) * 4 + 2 * (cg & 1) + dbl) * 1024 + lane * 16), __builtin_bit_cast(u32x4, w)); }
    ST_END(4);
  }
}

constexpr float THR = 8.0f;
constexpr int A_TAB = 131072;
constexpr int A_LDS = 131072 + 2048;
#define A_LA 4
#define Q_LA 5
struct MainArgs { const char* kc; const char* vc; const char* wg; const char* wu; const char* wo; const char* wd; const unsigned short* x; unsigned short* out; char* ws; };
constexpr int P_STG = 9216, P_NSTG = 4, P_T = 73728;
constexpr int C_BASE = 81920;
constexpr int MAIN_LDS = 163840;
constexpr int CNT_CLEAR = 0;
constexpr int CNT_O = 8, CNT_H = 40;
DI void signal_done(char* ws, int idx, int wid) {
  vm_bar<0>();
  if (wid == 0 && lane_id() == 0) __hip_atomic_fetch_add((unsigned*)(ws + WS_CNT) + idx * 64, 1u, __ATOMIC_RELAXED, __HIP_MEMORY_SCOPE_AGENT);
}
DI void wait_count(const char* ws, int idx, unsigned target, int wid) {
  if (wid == 0) {
    const unsigned* cnt = (const unsigned*)(ws + WS_CNT) + idx * 64;
    for (unsigned spin = 0; spin < (1u << 15); ++spin) {
      if (rfl(__hip_atomic_load(cnt, __ATOMIC_RELAXED, __HIP_MEMORY_SCOPE_AGENT)) >= target) break;
      __builtin_amdgcn_s_sleep(1);
    }
  }
  bar();
}
DI void st_empty(char* base, unsigned voff) { st16c(base, voff, (u32x4){~0u, ~0u, ~0u, ~0u}); }
DI bool has_empty(const bf16x8& a, const bf16x8& b) {
  const u32x4 x = __builtin_bit_cast(u32x4, a), y = __builtin_bit_cast(u32x4, b);
  unsigned m = x[0] > x[1] ? x[0] : x[1]; m = m > x[2] ? m : x[2]; m = m > x[3] ? m : x[3]; m = m > y[0] ? m : y[0]; m = m > y[1] ? m : y[1]; m = m > y[2] ? m : y[2]; m = m > y[3] ? m : y[3];
  return __any(m == ~0u);
}
template <int IMM1> DI void revalidate(bf16x8& a, bf16x8& b, unsigned oa, unsigned ob, const char* base) {
  if (__builtin_expect(has_empty(a, b), 0)) {
    for (unsigned spin = 0; spin < (1u << 15); ++spin) {
      __builtin_amdgcn_s_sleep(3);
      asm volatile("global_load_dwordx4 %0, %2, %4 sc1\n\tglobal_load_dwordx4 %1, %3, %4 offset:%5 sc1\n\ts_waitcnt vmcnt(0)" : "+v"(a), "+v"(b) : "v"(oa), "v"(ob), "s"(base), "i"(IMM1) : "memory");
      if (!has_empty(a, b)) break;
    }
  }
}
DI void proj_prefetch(const char* W, int nst, int wid) {
  const int lane = lane_id();
  const int x = blockIdx.x & 7, j = blockIdx.x >> 3, ks = x >> 1, ct = j & 3, rg = wid & 3, kh = wid >> 2;
  unsigned wsrc3[3];
#pragma unroll
  for (int e = 0; e < 3; ++e) { const int o = 64 * e + lane, row = o / 18, ch = o - row * 18; wsrc3[e] = row * 1152 + ch * 16; }
  const char* Ww = W + (size_t)((ks * 2 + kh) * nst * 32 + rg * 8) * 1152 + ct * 288;
  const unsigned wdst = kh * (P_NSTG * P_STG) + rg * 2304;
#pragma unroll
  for (int s = 0; s < 3; ++s) { const char* wsrc = Ww + (size_t)s * 32 * 1152; const unsigned m = s * P_STG + wdst;
    dma16<0>(m, wsrc3[0], wsrc); dma16<0>(m + 1024, wsrc3[1], wsrc); if (lane < 16) dma16<0>(m + 2048, wsrc3[2], wsrc); }
}
DI void phase_attn(const MainArgs& ar, LAS char* lds, const int wid DBG_PARAM) {
  PH_BEGIN
  char* const ws = ar.ws;
  const int lane = lane_id(), tid = wid * 64 + lane;
  const int x = blockIdx.x & 7, j = blockIdx.x >> 3, head = 4 * x + (j & 3), rt = j >> 2, kvh = head >> 1;
  const int rb = rt * 8 + wid;
  const int prb = 2 * (j >> 2) + (x & 1), pown = (j & 3) * 4 + (x >> 1);
  const int h = lane >> 5, r = lane & 31, blk = (lane >> 4) & 1, q = (lane & 15) >> 2, p = lane & 3;
  const int l4 = lane >> 4, pos = lane & 15;
  unsigned ksrc2[2];
#pragma unroll
  for (int e = 0; e < 2; ++e) { const int n = 2 * wid + e, row = 4 * n + l4, ch = pos ^ ((l4 << 2) | (n & 3)); ksrc2[e] = row * 256 + ch * 16 - e * 1024; }
  const char* wq_h = ws + WS_WQ + (size_t)head * 36 * 4096 + wid * 2048;
  const char* kb_ = ar.kc + ((size_t)kvh * SK + (SK - WIN)) * 256;
  const char* vb_ = ar.vc + ((size_t)kvh * SK + (SK - WIN)) * 256;
  const char* h1w = ws + WS_H1 + (size_t)rb * 36 * 1024;
  const unsigned lane16 = lane * 16;
  auto dmaW = [&](int qq, int e) { const unsigned m = 65536 + (qq & 3) * 16384 + wid * 2048; if (e == 0) dma16<0>(m, lane16, wq_h + qq * 16384); else dma16<1024>(m, lane16, wq_h + qq * 16384); };
  auto dmaK8 = [&](int t) { const unsigned m = (t & 3) * 16384 + wid * 2048; const char* s = kb_ + t * 16384; dma16<0>(m, ksrc2[0], s); dma16<1024>(m, ksrc2[1], s); };
  auto dmaV8 = [&](int t) { const unsigned m = 65536 + ((t + 1) & 3) * 16384 + wid * 2048; const char* s = vb_ + t * 16384; dma16<0>(m, ksrc2[0], s); dma16<1024>(m, ksrc2[1], s); };
  bf16x8 g[9][4];
  auto loadG1 = [&](auto QQ, auto E) { constexpr int qq = QQ, e = E; gload16c<e * 1024>(g[qq][e], lane16, h1w + qq * 4096); };
  auto checkG = [&](auto QQ) { constexpr int qq = QQ; static_for<4>([&](auto E) { pin(g[qq][E]); });
    revalidate<1024>(g[qq][0], g[qq][1], lane16, lane16, h1w + qq * 4096); revalidate<1024>(g[qq][2], g[qq][3], lane16, lane16, h1w + qq * 4096 + 2048); };
  auto loadG = [&](auto QQ) { static_for<4>([&](auto E) { loadG1(QQ, E); }); };
  auto mark = [&](auto MI) { constexpr int mi = MI;
    if constexpr (mi < 2) { const int rown = prb * 128 + pown * 8 + (tid & 7), kc = mi == 0 ? (tid >> 3) : 64 + (lane >> 3);
      st_empty(ws + WS_H2, (unsigned)(((rown >> 5) * 36 + (kc >> 1)) * 1024 + ((kc & 1) * 32 + (rown & 31)) * 16)); }
    else { const int n = tid + 512 * (mi - 2), n2 = n < 2304 ? n : n - 2304;
      st_empty(ws + WS_PB + (size_t)prb * 16 * 16 * 2304 + (size_t)pown * 2304, (unsigned)((n < 2304 ? 0 : (unsigned)(WS_PD - WS_PB)) + (n2 / 144) * 36864 + (n2 % 144) * 16)); } };
  {
    const int row = rt * 256 + (8 * (j & 3) + x) * 8 + (tid & 7), u0 = tid >> 3, u1 = 64 + (lane >> 3);
    u32x4 xv[2]; f32x4 sa[2][2], ca[2][2]; f32x2 mr;
    asm volatile("global_load_dwordx2 %0, %1, %2" : "=v"(mr) : "v"((unsigned)row * 8), "s"(ws + WS_STAT));
    gload16<0>(xv[0], (unsigned)(row * D_ + u0 * 8) * 2, ar.x); gload16<0>(xv[1], (unsigned)(row * D_ + u1 * 8) * 2, ar.x);
#pragma unroll
    for (int i = 0; i < 2; ++i) { const unsigned o = (i ? u1 : u0) * 32; gload16<0>(sa[i][0], o, ws + WS_MOD); gload16<16>(sa[i][1], o, ws + WS_MOD); gload16<0>(ca[i][0], o, ws + WS_MOD + 2304); gload16<16>(ca[i][1], o, ws + WS_MOD + 2304); }
    dmaW(0, 0); dmaW(0, 1); dmaW(1, 0); dmaW(1, 1); dmaW(2, 0); dmaW(2, 1); dmaK8(0);
    wait_vm<8>(); pin(mr);
    const float rq = mr[1] * QSCALE;
#pragma unroll
    for (int i = 0; i < 2; ++i) { pin(xv[i]); pin(sa[i][0]); pin(sa[i][1]); pin(ca[i][0]); pin(ca[i][1]);
      const f32x4 v0 = {bflo(xv[i][0]), bfhi(xv[i][0]), bflo(xv[i][1]), bfhi(xv[i][1])}, v1 = {bflo(xv[i][2]), bfhi(xv[i][2]), bflo(xv[i][3]), bfhi(xv[i][3])};
      const f32x4 h0 = (v0 - mr[0]) * rq * (ca[i][0] + 1.f) + sa[i][0] * QSCALE, h1v = (v1 - mr[0]) * rq * (ca[i][1] + 1.f) + sa[i][1] * QSCALE; const int u = i ? u1 : u0;
      st16c(ws + WS_H1, (unsigned)(((row >> 5) * 36 + (u >> 1)) * 1024 + ((u & 1) * 32 + (row & 31)) * 16), (u32x4){pk2(h0[0], h0[1]), pk2(h0[2], h0[3]), pk2(h1v[0], h1v[1]), pk2(h1v[2], h1v[3])}); }
  }
  ST(10);
#define H1_SLEEP 16
  __builtin_amdgcn_s_sleep(H1_SLEEP);
  for (unsigned spin = 0; spin < (1u << 15); ++spin) {
    loadG(integral_constant<int, 0>{}); wait_vm<0>();
    static_for<4>([&](auto E) { pin(g[0][E]); });
    if (!has_empty(g[0][0], g[0][1]) && !has_empty(g[0][2], g[0][3])) break;
    __builtin_amdgcn_s_sleep(2);
  }
  ST(11);
  loadG(integral_constant<int, 1>{}); loadG(integral_constant<int, 2>{});
  unsigned wb0 = 65536 + lane16;
  pin(wb0);
  f32x16 acc[4] = {};
  {
    constexpr int LA = Q_LA, NIT = 9 * 16;
    bf16x8 wf[LA + 1];
    auto rd = [&](auto I) { constexpr int i = I; constexpr int qq = i / 16, ks = (i % 16) / 4, db = i % 4;
      wf[i % (LA + 1)] = *(LAS bf16x8*)(lds + wb0 + (qq & 3) * 16384 + ks * 4096 + db * 1024); };
    SB(); bar();
    SB();
    ST(1);
    static_for<LA>([&](auto I) { rd(I); });
    SB();
    static_for<NIT>([&](auto I) { constexpr int i = I; constexpr int qq = i / 16, rr = i % 16, ks = rr / 4, db = rr % 4;
      if constexpr (rr == 0 && qq > 0) {
        constexpr int NW[9] = {8, 10, 10, 10, 10, 10, 10, 8, 8};
        vm_bar<NW[qq]>();
        checkG(integral_constant<int, qq>{});
        if constexpr (qq == 4) ST(2);
        SB();
      }
      if constexpr (i + LA < NIT) rd(integral_constant<int, i + LA>{});
      acc[db] = mfma32(wf[i % (LA + 1)], g[qq][ks], acc[db]);
      if constexpr (qq + 3 < 9) {
        if constexpr (rr == 1) dmaW(qq + 3, 0);
        if constexpr (rr == 3) dmaW(qq + 3, 1);
        if constexpr (rr >= 5 && rr <= 11 && (rr & 1)) loadG1(integral_constant<int, qq + 3>{}, integral_constant<int, (rr - 5) / 2>{});
      } else {
        if constexpr (rr == 1) dmaV8(qq - 6);
        if constexpr (rr == 7) dmaK8(qq - 5);
      }
      SB();
    });
  }
  ST(3);
  bf16x8 qf[8];
#pragma unroll
  for (int db = 0; db < 4; ++db)
#pragma unroll
    for (int s = 0; s < 2; ++s) { u32x4 w;
#pragma unroll
      for (int i = 0; i < 4; ++i) w[i] = pk2(acc[db][8 * s + 2 * i], acc[db][8 * s + 2 * i + 1]);
      qf[2 * db + s] = __builtin_bit_cast(bf16x8, w); }
  const unsigned kb0 = 256 * r + 16 * (h ^ (((r & 3) << 2) | ((r >> 2) & 3)));
  const unsigned vb0 = 65536 + 256 * (4 * h + q) + 16 * ((q << 2) | ((2 * blk + (p >> 1)) ^ h)) + 8 * (p & 1);
  unsigned ka[8], va[4][2];
#pragma unroll
  for (int s = 0; s < 8; ++s) ka[s] = kb0 ^ (s << 5);
#pragma unroll
  for (int db = 0; db < 4; ++db) { va[db][0] = vb0 ^ (db << 6); va[db][1] = vb0 ^ (db << 6) ^ 32; }
  f32x16 o[4] = {}; f32x16 S[2][2], negm; unsigned pw[16]; float m; f32x2 l2 = {0.f, 0.f};
  constexpr int LA = A_LA;
  bf16x8 fr[LA + 1];
  struct Map { int g[272]; int n; };
  constexpr Map MP = []() constexpr { Map mp{}; mp.n = 0; for (int G = 0; G < 9 * 32; ++G) { const int t = G / 32 - 1, i = G % 32; if (i < 16 ? (t + 1 <= 7) : (t >= 0 && t <= 7)) mp.g[mp.n++] = G; } return mp; }();
  constexpr int NIT = MP.n;
  auto rdA = [&](auto C) { constexpr int c = C; constexpr int G = MP.g[c], t = G / 32 - 1, i = G % 32;
    if constexpr (i < 16) { constexpr int s = i >> 1, kb = i & 1; fr[c % (LA + 1)] = *(LAS bf16x8*)(lds + ka[s] + ((t + 1) & 3) * 16384 + kb * 8192); }
    else { constexpr int ii = i - 16, kk = ii >> 2, db = ii & 3; constexpr int so = ((t + 1) & 3) * 16384 + kk * 4096;
      fr[c % (LA + 1)] = cat(tr4(lds + va[db][0] + so), tr4(lds + va[db][1] + so + 2048)); } };
  auto rowmax_finish = [&](float a) { auto rr = __builtin_amdgcn_permlane32_swap(__float_as_uint(a), __float_as_uint(a), false, false); return max2f(__uint_as_float(rr[0]), __uint_as_float(rr[1])); };
  float mxa, mxb;
  const unsigned gw = blockIdx.x * 8 + wid;
  auto touch = [&](auto TI) { constexpr int ti = TI; constexpr int m = ti >> 1, half = ti & 1;
    constexpr unsigned per = m == 0 ? 9 : 11, tot = m == 0 ? 36864 : 41472;
    unsigned line = (gw * 2 + half) * per + (lane < (int)per ? lane : 0); if (line >= tot) line = tot - 1;
    dma4(A_TAB + 2048 + wid * 256, line * 128, m == 0 ? ar.wo : (m == 1 ? ar.wg : (m == 2 ? ar.wu : ar.wd))); };
  auto background = [&](auto BI) { constexpr int bi = BI;
    if constexpr (bi < 16) { if constexpr ((bi & 3) < 2) touch(integral_constant<int, (bi >> 2) * 2 + (bi & 1)>{}); else mark(integral_constant<int, (bi >> 2) * 2 + (bi & 1)>{}); }
    else mark(integral_constant<int, bi - 8>{}); };
  SB(); vm_bar<8>(); SB();
  ST(4);
  static_for<LA>([&](auto C) { rdA(C); });
  SB();
  static_for<NIT>([&](auto C) { constexpr int c = C; constexpr int G = MP.g[c], t = G / 32 - 1, i = G % 32;
    constexpr bool firstX = (i == 0) || (t == 7 && i == 16);
    if constexpr (firstX && t >= 0) {
      constexpr int NW[8] = {4, 8, 12, 12, 12, 9, 3, 0};
      float a = rowmax_finish(max2f(mxa, mxb));
      if constexpr (t == 0) {
        m = a; negm = splat16(-a);
#pragma unroll
        for (int r2 = 0; r2 < 16; ++r2) { S[0][0][r2] -= a; S[0][1][r2] -= a; }
      } else if (__builtin_expect(__any(a > THR), 0)) {
        const float dl = fmaxf(a, 0.f), f = __builtin_amdgcn_exp2f(-dl);
        m += dl; negm = splat16(-m); l2 *= f;
#pragma unroll
        for (int r2 = 0; r2 < 16; ++r2) { S[t & 1][0][r2] -= dl; S[t & 1][1][r2] -= dl; }
#pragma unroll
        for (int d = 0; d < 4; ++d)
#pragma unroll
          for (int r2 = 0; r2 < 16; ++r2) o[d][r2] *= f;
      }
      SB(); vm_bar<NW[t]>(); SB();
      if constexpr (t == 7) { if (wid == 0 && lane == 0) __hip_atomic_fetch_add((unsigned*)(ws + WS_CNT) + (CNT_CLEAR + rt) * 64, 1u, __ATOMIC_RELAXED, __HIP_MEMORY_SCOPE_AGENT); }
      if constexpr (t == 3) ST(5);
      if constexpr (t == 4) ST(7);
      if constexpr (t == 5) ST(8);
      if constexpr (t == 7) proj_prefetch(ar.wo, 16, wid);
      if constexpr (t == 7) {
#pragma unroll
        for (int e = 0; e < 32; ++e) S[1][e >> 4][e & 15] = __builtin_amdgcn_exp2f(S[1][e >> 4][e & 15]);
#pragma unroll
        for (int e = 0; e < 16; ++e) pw[e] = pk2(S[1][e >> 3][(2 * e) & 15], S[1][e >> 3][((2 * e) & 15) + 1]);
        SB();
      }
    }
    if constexpr (c + LA < NIT) rdA(integral_constant<int, c + LA>{});
    if constexpr (i < 16) {
      constexpr int s = i >> 1, kb = i & 1, X = (t + 1) & 1;
      if constexpr (s == 0) { if constexpr (t < 0) S[X][kb] = mfma32(fr[c % (LA + 1)], qf[s], f32x16{}); else S[X][kb] = mfma32(fr[c % (LA + 1)], qf[s], negm); }
      else S[X][kb] = mfma32(fr[c % (LA + 1)], qf[s], S[X][kb]);
      if constexpr (t >= 0) {
        constexpr int e0 = 2 * i, e1 = 2 * i + 1, Y = t & 1;
        S[Y][e0 >> 4][e0 & 15] = __builtin_amdgcn_exp2f(S[Y][e0 >> 4][e0 & 15]);
        S[Y][e1 >> 4][e1 & 15] = __builtin_amdgcn_exp2f(S[Y][e1 >> 4][e1 & 15]);
        if constexpr (i >= 1) { constexpr int e = i - 1; pw[e] = pk2(S[Y][e >> 3][(2 * e) & 15], S[Y][e >> 3][((2 * e) & 15) + 1]); }
        if constexpr (i == 15) { SB(); pw[15] = pk2(S[Y][1][14], S[Y][1][15]); }
        if constexpr (t <= 3 && i == 6) dma16<0>((t & 3) * 16384 + wid * 2048, ksrc2[0], kb_ + (t + 4) * 16384);
        if constexpr (t <= 3 && i == 9) dma16<1024>((t & 3) * 16384 + wid * 2048, ksrc2[1], kb_ + (t + 4) * 16384);
        if constexpr (t <= 4 && i == 12) dma16<0>(65536 + (t & 3) * 16384 + wid * 2048, ksrc2[0], vb_ + (t + 3) * 16384);
        if constexpr (t <= 4 && i == 15) dma16<1024>(65536 + (t & 3) * 16384 + wid * 2048, ksrc2[1], vb_ + (t + 3) * 16384);
      }
      if constexpr (t < 0 && i == 15) {
        asm volatile("s_nop 11" : "+v"(S[0][0]), "+v"(S[0][1]));
        mxa = max3f(S[0][0][0], S[0][0][1], S[0][0][2]); mxb = max3f(S[0][1][0], S[0][1][1], S[0][1][2]);
#pragma unroll
        for (int r2 = 3; r2 < 15; r2 += 2) { mxa = max3f(mxa, S[0][0][r2], S[0][0][r2 + 1]); mxb = max3f(mxb, S[0][1][r2], S[0][1][r2 + 1]); }
        mxa = max3f(mxa, S[0][0][15], S[0][1][15]);
      }
    } else {
      constexpr int ii = i - 16, kk = ii >> 2, db = ii & 3, Y = t & 1, X = (t + 1) & 1;
      const u32x4 w = {pw[4 * kk], pw[4 * kk + 1], pw[4 * kk + 2], pw[4 * kk + 3]};
      o[db] = mfma32(fr[c % (LA + 1)], __builtin_bit_cast(bf16x8, w), o[db]);
      if constexpr (t <= 4 && (ii & 3) == 1 && 4 * t + (ii >> 2) < 19) background(integral_constant<int, 4 * t + (ii >> 2)>{});
      l2 += (f32x2){S[Y][ii >> 3][(2 * ii) & 15], S[Y][ii >> 3][((2 * ii) & 15) + 1]};
      if constexpr (t < 7) {
        if constexpr (ii == 4) { mxa = max3f(S[X][0][0], S[X][0][1], S[X][0][2]); mxb = max3f(S[X][0][3], S[X][0][4], S[X][0][5]); }
        if constexpr (ii == 5) { mxa = max3f(mxa, S[X][0][6], S[X][0][7]); mxb = max3f(mxb, S[X][0][8], S[X][0][9]); }
        if constexpr (ii == 6) { mxa = max3f(mxa, S[X][0][10], S[X][0][11]); mxb = max3f(mxb, S[X][0][12], S[X][0][13]); }
        if constexpr (ii == 7) { mxa = max3f(mxa, S[X][0][14], S[X][0][15]); mxb = max3f(mxb, S[X][1][0], S[X][1][1]); }
        if constexpr (ii == 8) { mxa = max3f(mxa, S[X][1][2], S[X][1][3]); mxb = max3f(mxb, S[X][1][4], S[X][1][5]); }
        if constexpr (ii == 9) { mxa = max3f(mxa, S[X][1][6], S[X][1][7]); mxb = max3f(mxb, S[X][1][8], S[X][1][9]); }
        if constexpr (ii == 10) { mxa = max3f(mxa, S[X][1][10], S[X][1][11]); mxb = max3f(mxb, S[X][1][12], S[X][1][13]); }
        if constexpr (ii == 11) { mxa = max3f(mxa, S[X][1][14], S[X][1][15]); }
      }
    }
    SB();
  });
  ST(12);
  float l = l2[0] + l2[1];
  { auto rr = __builtin_amdgcn_permlane32_swap(__float_as_uint(l), __float_as_uint(l), false, false); l = __uint_as_float(rr[0]) + __uint_as_float(rr[1]); }
  const float inv = __builtin_amdgcn_rcpf(l);
  char* ob = ws + WS_O + ((size_t)rb * 256 + head * 8) * 1024;
#pragma unroll
  for (int db = 0; db < 4; ++db)
#pragma unroll
    for (int s = 0; s < 2; ++s) { u32x4 w;
#pragma unroll
      for (int i = 0; i < 4; ++i) w[i] = pk2(o[db][8 * s + 2 * i] * inv, o[db][8 * s + 2 * i + 1] * inv);
      st16c(ob, lane16 + (2 * db + s) * 1024, w); }
  signal_done(ws, CNT_O + rt * 4 + (head >> 3), wid);
  ST_END(0);
}

#define P_LA 4
DI void ffn1_prefetch(const MainArgs& ar, int wid);
template <bool FINAL>
DI void phase_proj(const MainArgs& ar, LAS char* lds, const int wid DBG_PARAM) {
  PH_BEGIN
  char* const ws = ar.ws; const char* W = FINAL ? ar.wd : ar.wo; const unsigned short* xin = ar.x; unsigned short* out = ar.out;
  constexpr int NS16 = FINAL ? 288 : 256, NST = NS16 / 16;
  const char* act = ws + (FINAL ? WS_H : WS_O);
  const float* mod = (const float*)(ws + WS_MOD);
  char* part = ws + (FINAL ? WS_PD : WS_PB);
  const int lane = lane_id(), tid = wid * 64 + lane, rg = wid & 3, kh = wid >> 2;
  const int x = blockIdx.x & 7, j = blockIdx.x >> 3, ks = x >> 1, rb = 2 * (j >> 2) + (x & 1), ct = j & 3;
  const int own = ct * 4 + ks;
  const int g = lane >> 4, m16 = lane & 15, q4 = (lane & 15) >> 2, p = lane & 3;
  const int kb32 = (ks * 2 + kh) * NST;
  unsigned aoff[2];
#pragma unroll
  for (int a = 0; a < 2; ++a) { const int row = rb * 128 + rg * 32 + a * 16 + m16; aoff[a] = (unsigned)(row >> 5) * (NS16 * 1024) + (row & 31) * 16 + (g >> 1) * 1024 + (g & 1) * 512; }
  const char* actw = act + (size_t)kb32 * 2048;
  unsigned wsrc3[3];
#pragma unroll
  for (int e = 0; e < 3; ++e) { const int o = 64 * e + lane, row = o / 18, ch = o - row * 18; wsrc3[e] = row * 1152 + ch * 16; }
  const char* Ww = W + (size_t)(kb32 * 32 + rg * 8) * 1152 + ct * 288;
  const unsigned wdst = kh * (P_NSTG * P_STG) + rg * 2304;
  bf16x8 af[NST][2];
  auto issue_piece = [&](auto S, auto E) { constexpr int s = S, e = E;
    const char* wsrc = Ww + (size_t)s * 32 * 1152; const unsigned m = (s % P_NSTG) * P_STG + wdst;
    if constexpr (e == 0) dma16<0>(m, wsrc3[0], wsrc);
    if constexpr (e == 1) dma16<0>(m + 1024, wsrc3[1], wsrc);
    if constexpr (e == 2) { if (lane < 16) dma16<0>(m + 2048, wsrc3[2], wsrc); }
    if constexpr (e == 3) gload16c<0>(af[s][0], aoff[0], actw + s * 2048);
    if constexpr (e == 4) gload16c<0>(af[s][1], aoff[1], actw + s * 2048); };
  if (!FINAL && wid != 0) { ffn1_prefetch(ar, wid); if (wid == 1) ffn1_prefetch(ar, 0); }
  wait_count(ws, (FINAL ? CNT_H : CNT_O) + (rb >> 1) * 4 + ks, 8u, wid);
  static_for<3>([&](auto S) { issue_piece(S, integral_constant<int, 3>{}); issue_piece(S, integral_constant<int, 4>{}); });
  unsigned tb[P_NSTG];
#pragma unroll
  for (int i = 0; i < P_NSTG; ++i) tb[i] = kh * (P_NSTG * P_STG) + i * P_STG + 288 * (16 * (g >> 1) + 4 * (g & 1) + q4) + 8 * p;
  f32x4 acc[2][9] = {};
  constexpr int LA = P_LA, NIT = NST * 9;
  bf16x8 wf[LA + 1];
  auto rd = [&](auto I) { constexpr int i = I; constexpr int s = i / 9, c = i % 9;
    wf[i % (LA + 1)] = cat(tr4(lds + tb[s % P_NSTG] + 32 * c), tr4(lds + tb[s % P_NSTG] + 288 * 8 + 32 * c)); };
  constexpr int NPF = 0, NTB = FINAL ? 3 : 2;
  constexpr int TB = FINAL ? 81920 : 137600;
  SB(); vm_bar<4 + NPF>(); pin(af[0][0]); pin(af[0][1]);
#pragma unroll
  for (int e = 0; e < NTB; ++e) { const int n = NTB * wid + e;
    if (!FINAL) dma16<0>(TB + n * 1024, lane * 16, n < 7 ? (const char*)(mod + 1152) + n * 1024 : (const char*)xin + (size_t)(rb * 128 + own * 8) * 1152 + (n - 7) * 1024);
    else { const int n2 = n < 21 ? n : 0; dma16<0>(TB + n2 * 1024, lane * 16, n2 < 3 ? (const char*)(mod + 2880) + n2 * 1024 : ws + WS_X1R + (size_t)blockIdx.x * 576 * 32 + (n2 - 3) * 1024); } }
  SB();
  ST(1);
  static_for<LA>([&](auto I) { rd(I); });
  SB();
  static_for<NIT>([&](auto I) { constexpr int i = I; constexpr int s = i / 9, c = i % 9;
    if constexpr (c == 0 && s > 0) {
      constexpr int outst = s == 1 ? 2 + NPF + NTB + 5 : ((s + 2 < NST) ? 5 : 0);
      vm_bar<outst>(); pin(af[s][0]); pin(af[s][1]);
      if constexpr (s == 8) ST(2);
      SB();
    }
    if constexpr (i + LA < NIT) rd(integral_constant<int, i + LA>{});
    acc[0][c] = mfma16(wf[i % (LA + 1)], af[s][0], acc[0][c]);
    acc[1][c] = mfma16(wf[i % (LA + 1)], af[s][1], acc[1][c]);
    if constexpr (s + 3 < NST && c >= 1 && c <= 5) issue_piece(integral_constant<int, s + 3>{}, integral_constant<int, c - 1>{});
    SB();
  });
  ST(5);
  const int r8 = tid & 7, rown = rb * 128 + own * 8 + r8;
  int colA[2], colB[2];
  colA[0] = 144 * (tid >> 7) + 32 * ((tid >> 5) & 3) + 4 * ((tid >> 3) & 3); colB[0] = colA[0] + 16;
  colA[1] = 288 * ((tid >> 5) & 1) + 128 + 4 * ((tid >> 3) & 3); colB[1] = colA[1] + 144;
  unsigned marked;
  if (!FINAL) asm volatile("global_load_dword %0, %1, %2 sc1" : "=v"(marked) : "v"(0u), "s"(ws + WS_CNT + (size_t)(CNT_CLEAR + (rb >> 1)) * 256));
  {
    LAS char* exw = lds + (rg * 2 + kh) * 9216 + lane * 16; LAS char* exr = lds + (rg * 2 + (kh ^ 1)) * 9216 + lane * 16;
    bar();
    ST(3);
    f32x4 fin[9];
    if (kh) {
#pragma unroll
      for (int c = 0; c < 9; ++c) { *(LAS f32x4*)(exw + c * 1024) = acc[0][c]; fin[c] = acc[1][c]; }
    } else {
#pragma unroll
      for (int c = 0; c < 9; ++c) { *(LAS f32x4*)(exw + c * 1024) = acc[1][c]; fin[c] = acc[0][c]; }
    }
    asm volatile("s_waitcnt lgkmcnt(0)\n\ts_barrier" ::: "memory");
    ST(4);
    char* pw = part + (size_t)rb * 16 * 16 * 2304 + (size_t)own * 2304;
#pragma unroll
    for (int c = 0; c < 9; ++c) fin[c] += *(LAS f32x4*)(exr + c * 1024);
    const unsigned jo = (unsigned)(rg * 4 + kh * 2 + (m16 >> 3)) * 36864, lo = g * 8 + (m16 & 7);
    u32x4 cw[4];
#pragma unroll
    for (int cp = 0; cp < 4; ++cp) cw[cp] = (u32x4){pk2(fin[2 * cp][0], fin[2 * cp][1]), pk2(fin[2 * cp][2], fin[2 * cp][3]), pk2(fin[2 * cp + 1][0], fin[2 * cp + 1][1]), pk2(fin[2 * cp + 1][2], fin[2 * cp + 1][3])};
    const u32x2 w8 = {pk2(fin[8][0], fin[8][1]), pk2(fin[8][2], fin[8][3])};
#pragma unroll
    for (int cp = 0; cp < 4; ++cp) st16c(pw, jo + (cp * 32 + lo) * 16, cw[cp]);
    asm volatile("global_store_dwordx2 %0, %1, %2" STF "\n\ts_nop 1" ::"v"(jo + 2048 + lo * 8), "v"(w8), "s"(pw) : "memory");
  }
  ST(7);
  const char* pr = part + ((size_t)rb * 16 + own) * 16 * 2304;
  const bool two = wid == 0;
  u32x4 pv[4]; u32x2 pq[4][2];
  if (!FINAL) {
    asm volatile("s_waitcnt vmcnt(5)" : "+v"(marked) :: "memory");
    if (!__all(marked >= 32u)) {
      const unsigned* cnt = (const unsigned*)(ws + WS_CNT) + (CNT_CLEAR + (rb >> 1)) * 64;
      for (unsigned spin = 0; spin < (1u << 15); ++spin) { if (__all(__hip_atomic_load(cnt, __ATOMIC_RELAXED, __HIP_MEMORY_SCOPE_AGENT) >= 32u)) break; __builtin_amdgcn_s_sleep(1); }
    }
  }
#define POLLMODE 1
#define P_SLEEP 20
  if (wid == 7) {
    for (unsigned spin = 0; spin < (1u << 15); ++spin) {
      unsigned t; asm volatile("global_load_dword %0, %1, %2 sc1\n\ts_waitcnt vmcnt(0)" : "=&v"(t) : "v"((unsigned)((lane & 15) * 2304 + 2300)), "s"(pr) : "memory");
      if (__all(t != ~0u)) break;
      __builtin_amdgcn_s_sleep(1);
    } }
  bar();
  ST(8);
  for (unsigned spin = 0; spin < (1u << 15); ++spin) {
    const unsigned o0 = (unsigned)((tid >> 7) * 4 * 2304 + (tid & 127) * 16), o1 = (unsigned)(((tid >> 5) & 1) * 8 * 2304 + 2048 + (tid & 31) * 8);
#pragma unroll
    for (int k = 0; k < 4; ++k) gload16c<0>(pv[k], o0 + k * 2304, pr);
    if (two) {
#pragma unroll
      for (int k = 0; k < 4; ++k) {
        asm volatile("global_load_dwordx2 %0, %1, %2" LDF : "=v"(pq[k][0]) : "v"(o1 + k * 2304), "s"(pr));
        asm volatile("global_load_dwordx2 %0, %1, %2" LDF : "=v"(pq[k][1]) : "v"(o1 + (4 + k) * 2304), "s"(pr)); } }
    wait_vm<0>();
    bool full = true;
#pragma unroll
    for (int k = 0; k < 4; ++k) { pin(pv[k]); full &= pv[k][0] != ~0u && pv[k][1] != ~0u && pv[k][2] != ~0u && pv[k][3] != ~0u; }
    if (two) {
#pragma unroll
      for (int k = 0; k < 4; ++k) { pin(pq[k][0]); pin(pq[k][1]); full &= pq[k][0][0] != ~0u && pq[k][0][1] != ~0u && pq[k][1][0] != ~0u && pq[k][1][1] != ~0u; } }
    if (__all(full)) break;
    __builtin_amdgcn_s_sleep(4);
  }
  ST(9);
  f32x4 v[2][2], sc[2][2], sh[2][2];
#pragma unroll
  for (int i = 0; i < 2; ++i) if (i == 0 || two) {
#pragma unroll
    for (int hh = 0; hh < 2; ++hh) { const int col = hh ? colB[i] : colA[i]; f32x4 rs;
      const f32x4 gt = *(LAS f32x4*)(lds + TB + col * 4);
      if (FINAL) rs = *(LAS f32x4*)(lds + TB + 3072 + (i ? 512 + (tid & 63) : tid) * 32 + hh * 16);
      else { const u32x2 xr = *(LAS u32x2*)(lds + TB + 7168 + r8 * 1152 + col * 2); rs = (f32x4){bflo(xr[0]), bfhi(xr[0]), bflo(xr[1]), bfhi(xr[1])};
        sh[i][hh] = *(LAS f32x4*)(lds + TB + (576 + col) * 4); sc[i][hh] = *(LAS f32x4*)(lds + TB + (1152 + col) * 4); }
      f32x4 t = {0.f, 0.f, 0.f, 0.f};
#pragma unroll
      for (int k = 0; k < 4; ++k) { const unsigned w0 = i ? pq[k][hh][0] : pv[k][2 * hh], w1 = i ? pq[k][hh][1] : pv[k][2 * hh + 1]; t += (f32x4){bflo(w0), bfhi(w0), bflo(w1), bfhi(w1)}; }
      v[i][hh] = rs + gt * t; }
  }
  ST(10);
  if (FINAL) {
#pragma unroll
    for (int i = 0; i < 2; ++i) if (i == 0 || two) {
      *(u32x2*)(out + (size_t)rown * D_ + colA[i]) = (u32x2){pk2(v[i][0][0], v[i][0][1]), pk2(v[i][0][2], v[i][0][3])};
      *(u32x2*)(out + (size_t)rown * D_ + colB[i]) = (u32x2){pk2(v[i][1][0], v[i][1][1]), pk2(v[i][1][2], v[i][1][3])}; }
  } else {
    LAS float* st = (LAS float*)(lds + P_T);
    auto s8 = [](const f32x4& a, const f32x4& b) { return ((a[0] + a[1]) + (a[2] + a[3])) + ((b[0] + b[1]) + (b[2] + b[3])); };
    float s = s8(v[0][0], v[0][1]) + (two ? s8(v[1][0], v[1][1]) : 0.f);
    s = sum_x8_16_32(s);
    if (lane < 8) st[wid * 8 + lane] = s;
    asm volatile("s_waitcnt lgkmcnt(0)\n\ts_barrier" ::: "memory");
    float tot = 0.f;
#pragma unroll
    for (int w = 0; w < 8; ++w) tot += st[w * 8 + r8];
    const float mean = tot * (1.f / 576.f);
    f32x4 d[2][2];
#pragma unroll
    for (int i = 0; i < 2; ++i) { d[i][0] = v[i][0] - mean; d[i][1] = v[i][1] - mean; }
    float qq = s8(d[0][0] * d[0][0], d[0][1] * d[0][1]) + (two ? s8(d[1][0] * d[1][0], d[1][1] * d[1][1]) : 0.f);
    qq = sum_x8_16_32(qq);
    if (lane < 8) st[64 + wid * 8 + lane] = qq;
    asm volatile("s_waitcnt lgkmcnt(0)\n\ts_barrier" ::: "memory");
    float tq = 0.f;
#pragma unroll
    for (int w = 0; w < 8; ++w) tq += st[64 + w * 8 + r8];
    const float rstd = __builtin_amdgcn_rsqf(tq * (1.f / 576.f) + 1e-5f);
#pragma unroll
    for (int i = 0; i < 2; ++i) if (i == 0 || two) {
      const f32x4 h0 = d[i][0] * rstd * (sc[i][0] + 1.f) + sh[i][0], h1 = d[i][1] * rstd * (sc[i][1] + 1.f) + sh[i][1];
      const int kc = i ? 64 + (tid >> 3) : (tid >> 3);
      st16c(ws + WS_H2, (unsigned)(((rown >> 5) * 36 + (kc >> 1)) * 1024 + ((kc & 1) * 32 + (rown & 31)) * 16), (u32x4){pk2(h0[0], h0[1]), pk2(h0[2], h0[3]), pk2(h1[0], h1[1]), pk2(h1[2], h1[3])});
      char* xb = ws + WS_X1R + (size_t)blockIdx.x * 576 * 32 + (i ? 512 + (tid & 63) : tid) * 32;
      *(f32x4*)xb = v[i][0]; *(f32x4*)(xb + 16) = v[i][1]; }
  }
  ST_END(FINAL ? 3 : 1);
}

constexpr int C_STG = 9216 + 128 + 9216, C_UOFF = 9216 + 128, C_NSTG = 4;
DI unsigned geglu2(f32x2 g, f32x2 u) {
  const float c0 = -2.f * 0.7978845608028654f * 1.4426950408889634f, c1 = c0 * 0.044715f;
  const f32x2 z = g * (g * g * c1 + c0);
  const f32x2 d = (f32x2){__builtin_amdgcn_exp2f(z[0]), __builtin_amdgcn_exp2f(z[1])} + 1.f;
  const f32x2 o = (g * u) * (f32x2){__builtin_amdgcn_rcpf(d[0]), __builtin_amdgcn_rcpf(d[1])};
  return pk2(o[0], o[1]);
}
#define C_LA 6
DI void ffn1_prefetch(const MainArgs& ar, int wid) {
  const int lane = lane_id();
  const int x = blockIdx.x & 7, j = blockIdx.x >> 3, ct = 4 * x + (j & 3);
  unsigned wsrc3[3];
#pragma unroll
  for (int e = 0; e < 3; ++e) { const int o = 64 * e + lane, row = o / 18, ch = o - row * 18; wsrc3[e] = (16 * (row >> 2) + (row & 3)) * (FF * 2) + ch * 16; }
  const char* Ww = (wid < 4 ? ar.wg : ar.wu) + (size_t)(wid & 3) * 4 * (FF * 2) + ct * 288;
  const unsigned wdst = C_BASE + (wid < 4 ? 0 : C_UOFF) + (wid & 3) * 2304;
#pragma unroll
  for (int s = 0; s < 3; ++s) { const char* wsrc = Ww + (size_t)(32 * s) * (FF * 2); const unsigned m = s * C_STG + wdst;
    dma16<0>(m, wsrc3[0], wsrc); dma16<0>(m + 1024, wsrc3[1], wsrc); if (lane < 16) dma16<0>(m + 2048, wsrc3[2], wsrc); }
}
DI void phase_ffn1(const MainArgs& ar, LAS char* lds, const int wid DBG_PARAM) {
  PH_BEGIN
  char* const ws = ar.ws; const char* wg = ar.wg; const char* wu = ar.wu;
  const int lane = lane_id(), tid = wid * 64 + lane;
  const int x = blockIdx.x & 7, j = blockIdx.x >> 3, ct = 4 * x + (j & 3), rt = j >> 2;
  const int rb = rt * 8 + wid;
  const int h = lane >> 5, blk = (lane >> 4) & 1, q = (lane & 15) >> 2, p = lane & 3;
  unsigned wsrc3[3], wsrc3b[3];
#pragma unroll
  for (int e = 0; e < 3; ++e) { const int o = 64 * e + lane, row = o / 18, ch = o - row * 18;
    wsrc3[e] = (16 * (row >> 2) + (row & 3)) * (FF * 2) + ch * 16; wsrc3b[e] = (144 * (row >> 2) + (row & 3)) * (FF * 2) + ch * 16; }
  const char* Ww = (wid < 4 ? wg : wu) + (size_t)(wid & 3) * 4 * (FF * 2) + ct * 288;
  const unsigned wdst = C_BASE + (wid < 4 ? 0 : C_UOFF) + (wid & 3) * 2304;
  const char* hw = ws + WS_H2 + (size_t)rb * 36 * 1024;
  const unsigned lane16 = lane * 16;
  bf16x8 af[18][2];
  auto issue_piece = [&](auto S, auto E) { constexpr int s = S, e = E;
    constexpr int kbase = s < 16 ? 144 * (s >> 2) + 32 * (s & 3) : 288 * (s - 16) + 128;
    const char* wsrc = Ww + (size_t)kbase * (FF * 2); const unsigned m = (s % C_NSTG) * C_STG + wdst;
    if constexpr (e == 0) dma16<0>(m, s < 16 ? wsrc3[0] : wsrc3b[0], wsrc);
    if constexpr (e == 1) dma16<0>(m + 1024, s < 16 ? wsrc3[1] : wsrc3b[1], wsrc);
    if constexpr (e == 2) { if (lane < 16) dma16<0>(m + 2048, s < 16 ? wsrc3[2] : wsrc3b[2], wsrc); }
    if constexpr (e == 3) gload16c<0>(af[s][0], lane16, hw + s * 2048);
    if constexpr (e == 4) gload16c<1024>(af[s][1], lane16, hw + s * 2048); };
  static_for<3>([&](auto S) { issue_piece(S, integral_constant<int, 3>{}); issue_piece(S, integral_constant<int, 4>{}); });
  unsigned tb[C_NSTG];
#pragma unroll
  for (int i = 0; i < C_NSTG; ++i) { tb[i] = C_BASE + blk * C_UOFF + 288 * (8 * h + q) + 8 * p + i * C_STG; pin(tb[i]); }
  f32x16 acc[9] = {};
  constexpr int LA = C_LA, NIT = 18 * 18;
  bf16x8 wf[LA + 1];
  auto rd = [&](auto I) { constexpr int i = I; constexpr int s = i / 18, ks = (i % 18) / 9, f = i % 9;
    const bf16x4 lo = tr4(lds + tb[s % C_NSTG] + 288 * 16 * ks + 32 * f), hi = tr4(lds + tb[s % C_NSTG] + 288 * (16 * ks + 4) + 32 * f);
    wf[i % (LA + 1)] = cat(lo, hi); };
  SB(); vm_bar<4>(); pin(af[0][0]); pin(af[0][1]); revalidate<1024>(af[0][0], af[0][1], lane16, lane16, hw); SB();
  ST(1);
  static_for<LA>([&](auto I) { rd(I); });
  SB();
  static_for<NIT>([&](auto I) { constexpr int i = I; constexpr int s = i / 18, r = i % 18, ks = r / 9, f = r % 9;
    if constexpr (r == 0 && s > 0) {
      constexpr int outst = (s + 2 < 18) ? 5 : 0;
      vm_bar<outst>(); pin(af[s][0]); pin(af[s][1]);
      revalidate<1024>(af[s][0], af[s][1], lane16, lane16, hw + s * 2048);
      if constexpr (s == 4) ST(2);
      if constexpr (s == 8) ST(3);
      if constexpr (s == 12) ST(4);
      if constexpr (s == 16) ST(5);
      if constexpr (s == 17) proj_prefetch(ar.wd, 18, wid);
      SB();
    }
    if constexpr (i + LA < NIT) rd(integral_constant<int, i + LA>{});
    acc[f] = mfma32(wf[i % (LA + 1)], af[s][ks], acc[f]);
    if constexpr (s + 3 < 18 && (r % 3) == 1 && r / 3 < 5) issue_piece(integral_constant<int, s + 3>{}, integral_constant<int, r / 3>{});
    SB();
  });
  ST(6);
  char* ob = ws + WS_H + ((size_t)rb * 288 + ct * 9) * 1024;
#pragma unroll
  for (int f = 0; f < 9; ++f) { u32x4 w;
#pragma unroll
    for (int i = 0; i < 4; ++i) w[i] = geglu2((f32x2){acc[f][2 * i], acc[f][2 * i + 1]}, (f32x2){acc[f][8 + 2 * i], acc[f][8 + 2 * i + 1]});
    st16c(ob, lane16 + f * 1024, w); }
  signal_done(ws, CNT_H + rt * 4 + (ct >> 3), wid);
  ST_END(2);
}

DI unsigned long long rt_now() { unsigned long long t; asm volatile("s_memrealtime %0\n\ts_waitcnt lgkmcnt(0)" : "=s"(t) :: "memory"); return t; }
DI unsigned long long ck_now() { unsigned long long t; asm volatile("s_memtime %0\n\ts_waitcnt lgkmcnt(0)" : "=s"(t) :: "memory"); return t; }
__global__ __launch_bounds__(512, 2) void k_main(MainArgs ar DBG_PARAM) {
  unsigned long long r0 = rt_now(), c0 = ck_now();
  extern __shared__ __attribute__((aligned(16))) char lds_[];
  LAS char* lds = (LAS char*)lds_;
  const int wid = rfl(threadIdx.x >> 6);
  phase_attn(ar, lds, wid PH_DBG);
  unsigned long long r1 = rt_now(), c1 = ck_now();
  phase_proj<false>(ar, lds, wid PH_DBG);
  unsigned long long r2 = rt_now(), c2 = ck_now();
  phase_ffn1(ar, lds, wid PH_DBG);
  unsigned long long r3 = rt_now(), c3 = ck_now();
  phase_proj<true>(ar, lds, wid PH_DBG);
  unsigned long long r4 = rt_now(), c4 = ck_now();
  if (threadIdx.x == 0 && (blockIdx.x % 85) == 0) { const unsigned long long p0 = *(const unsigned long long*)(ar.ws + WS_STAT + 32768);
    printf("WG %d: pre start -> main start %.2f | A %.2f (%.3f GHz) | B %.2f (%.3f) | C %.2f (%.3f) | D %.2f (%.3f) | total %.2f us\n", blockIdx.x, (r0 - p0) / 100., (r1 - r0) / 100., (c1 - c0) / ((r1 - r0) * 10.), (r2 - r1) / 100., (c2 - c1) / ((r2 - r1) * 10.),
           (r3 - r2) / 100., (c3 - c2) / ((r3 - r2) * 10.), (r4 - r3) / 100., (c4 - c3) / ((r4 - r3) * 10.), (r4 - p0) / 100.); }
}

extern "C" void kernel_launch(void* const* d_in, const int* in_sizes, int n_in, void* d_out, int out_size, void* d_ws, size_t ws_size, hipStream_t stream) {
  const unsigned short* x = (const unsigned short*)d_in[0];
  const unsigned short* c = (const unsigned short*)d_in[9]; const unsigned short* wmod = (const unsigned short*)d_in[10];
  char* ws = (char*)d_ws;
  (void)hipFuncSetAttribute((const void*)k_pre, hipFuncAttributeMaxDynamicSharedMemorySize, PRE_LDS);
  (void)hipFuncSetAttribute((const void*)k_main, hipFuncAttributeMaxDynamicSharedMemorySize, MAIN_LDS);
  const char* kc = (const char*)d_in[1]; const char* vc = (const char*)d_in[2];
  const char* wq = (const char*)d_in[3]; const char* wo = (const char*)d_in[4];
  const char* wg = (const char*)d_in[6]; const char* wu = (const char*)d_in[7]; const char* wd = (const char*)d_in[8];
  k_pre<<<PRE_RQ + PRE_MOD + PRE_CLR, 512, PRE_LDS, stream>>>(c, wmod, x, wq, ws DBG_ARG);
  MainArgs ar{kc, vc, wg, wu, wo, wd, x, (unsigned short*)d_out, ws};
  k_main<<<256, 512, MAIN_LDS, stream>>>(ar DBG_ARG);
}
```

```cpp
#include <hip/hip_runtime.h>
#include <stdint.h>
#include <utility>

#define DI __device__ __forceinline__
#define LAS __attribute__((address_space(3)))
#define GAS __attribute__((address_space(1)))
typedef __bf16 bf16x8 __attribute__((ext_vector_type(8)));
typedef __bf16 bf16x4 __attribute__((ext_vector_type(4)));
typedef __bf16 bf16x2 __attribute__((ext_vector_type(2)));
typedef float f32x2 __attribute__((ext_vector_type(2)));
typedef float f32x4 __attribute__((ext_vector_type(4)));
typedef float f32x16 __attribute__((ext_vector_type(16)));
typedef unsigned u32x2 __attribute__((ext_vector_type(2)));
typedef unsigned u32x4 __attribute__((ext_vector_type(4)));
using std::integral_constant;

constexpr int M_ = 2048, D_ = 576, SK = 2048, WIN = 512, FF = 4608;

constexpr size_t WS_MOD = 0;
constexpr size_t WS_STAT = 16384;
constexpr size_t WS_H1 = WS_STAT + 149760 + 3840;
constexpr size_t WS_O = WS_H1 + (size_t)64 * 36 * 1024;
constexpr size_t WS_X1 = WS_O + (size_t)64 * 256 * 1024;
constexpr size_t WS_H2 = WS_X1 + (size_t)M_ * D_ * 4;
constexpr size_t WS_H = WS_H2 + (size_t)64 * 36 * 1024;
constexpr size_t WS_WQ = WS_H + (size_t)64 * 288 * 1024;
constexpr size_t WS_BP = WS_WQ + (size_t)32 * 36 * 4 * 1024;
constexpr size_t WS_CNT = WS_BP + (size_t)18 * 4096 * 4;
constexpr size_t WS_X1R = WS_CNT + 128 * 256;
constexpr size_t WS_PB = WS_X1R + (size_t)256 * 576 * 32;
constexpr size_t WS_PD = WS_PB + (size_t)16 * 16 * 16 * 2304;
constexpr size_t WS_END = WS_PD + (size_t)16 * 16 * 16 * 2304;

template <int... Is, class F> DI void static_for_impl(std::integer_sequence<int, Is...>, F&& f) { (f(std::integral_constant<int, Is>{}), ...); }
template <int N, class F> DI void static_for(F&& f) { static_for_impl(std::make_integer_sequence<int, N>{}, f); }

DI float bflo(unsigned w) { return __uint_as_float(w << 16); }
DI float bfhi(unsigned w) { return __uint_as_float(w & 0xffff0000u); }
DI unsigned pk2(float lo, float hi) { f32x2 v = {lo, hi}; bf16x2 b = __builtin_convertvector(v, bf16x2); return __builtin_bit_cast(unsigned, b); }
DI unsigned rfl(unsigned x) { return __builtin_amdgcn_readfirstlane(x); }
DI int lane_id() { int l; asm volatile("v_mbcnt_lo_u32_b32 %0, -1, 0\n\tv_mbcnt_hi_u32_b32 %0, -1, %0" : "=v"(l)); return l; }
DI void SB() { __builtin_amdgcn_sched_barrier(0); }

template <int IMM> DI void dma16(unsigned m0v, unsigned voff, const void* sbase) {
  asm volatile("s_mov_b32 m0, %0\n\ts_nop 0\n\tglobal_load_lds_dwordx4 %1, %2 offset:%3" ::"s"(m0v), "v"(voff), "s"(sbase), "i"(IMM) : "memory");
}
DI void dma4(unsigned m0v, unsigned voff, const void* sbase) {
  asm volatile("s_mov_b32 m0, %0\n\ts_nop 0\n\tglobal_load_lds_dword %1, %2" ::"s"(m0v), "v"(voff), "s"(sbase) : "memory");
}
template <int IMM, class T> DI void gload16(T& dst, unsigned voff, const void* sbase) {
  asm volatile("global_load_dwordx4 %0, %1, %2 offset:%3" : "=v"(dst) : "v"(voff), "s"(sbase), "i"(IMM));
}
#define LDF " sc1"
#define STF " sc1"
#define SC1L_S LDF
#define SC1S 1
template <int IMM, class T> DI void gload16c(T& dst, unsigned voff, const void* sbase) {
  asm volatile("global_load_dwordx4 %0, %1, %2 offset:%3" SC1L_S : "=v"(dst) : "v"(voff), "s"(sbase), "i"(IMM));
}
DI void st16c(char* base, unsigned voff, u32x4 v) {
  asm volatile("global_store_dwordx4 %0, %1, %2" STF "\n\ts_nop 1" ::"v"(voff), "v"(v), "s"(base) : "memory");
}
template <int N> DI void wait_vm() { asm volatile("s_waitcnt vmcnt(%0)" ::"i"(N) : "memory"); }
template <int N> DI void vm_bar() { asm volatile("s_waitcnt vmcnt(%0)\n\ts_barrier" ::"i"(N) : "memory"); }
template <int N> DI void vm_lgkm_bar() { asm volatile("s_waitcnt vmcnt(%0) lgkmcnt(0)\n\ts_barrier" ::"i"(N) : "memory"); }
DI void bar() { asm volatile("s_barrier" ::: "memory"); }
template <class T> DI void pin(T& a) { asm volatile("" : "+v"(a)); }
DI bf16x4 tr4(LAS char* p) { return __builtin_amdgcn_ds_read_tr16_b64_v4bf16((LAS bf16x4*)p); }
DI bf16x8 cat(bf16x4 a, bf16x4 b) { return __builtin_shufflevector(a, b, 0, 1, 2, 3, 4, 5, 6, 7); }
DI f32x16 mfma32(bf16x8 a, bf16x8 b, f32x16 c) { return __builtin_amdgcn_mfma_f32_32x32x16_bf16(a, b, c, 0, 0, 0); }
DI f32x4 mfma16(bf16x8 a, bf16x8 b, f32x4 c) { return __builtin_amdgcn_mfma_f32_16x16x32_bf16(a, b, c, 0, 0, 0); }
DI float max3f(float a, float b, float c) { float r; asm("v_max3_f32 %0, %1, %2, %3" : "=v"(r) : "v"(a), "v"(b), "v"(c)); return r; }
DI float max2f(float a, float b) { float r; asm("v_max_f32_e32 %0, %1, %2" : "=v"(r) : "v"(a), "v"(b)); return r; }
template <int CTRL> DI float dppf(float v) { return __builtin_bit_cast(float, __builtin_amdgcn_update_dpp(0, __builtin_bit_cast(int, v), CTRL, 0xf, 0xf, true)); }
DI float sum8(float v) { v += dppf<0xB1>(v); v += dppf<0x4E>(v); v += dppf<0x141>(v); return v; }
DI f32x16 splat16(float v) { f32x16 r; for (int i = 0; i < 16; ++i) r[i] = v; return r; }

#define ST_DECL
#define ST(i)
#define ST_END(kid)
#define DBG_PARAM
#define DBG_ARG
#define PH_BEGIN
#define PH_DBG

constexpr float QSCALE = 0.08838834764831845f * 1.4426950408889634f;
constexpr int PRE_RQ = 144, PRE_MOD = 54, PRE_CLR = 32;
constexpr int PRE_LDS = 32768 + 256;
DI float silu_f(float cv) { return cv * __builtin_amdgcn_rcpf(1.f + __builtin_amdgcn_exp2f(-1.4426950408889634f * cv)); }
DI float sum_x8_16_32(float v) {
  v += dppf<0x128>(v);
  { auto rr = __builtin_amdgcn_permlane16_swap(__float_as_uint(v), __float_as_uint(v), false, false); v = __uint_as_float(rr[0]) + __uint_as_float(rr[1]); }
  { auto rr = __builtin_amdgcn_permlane32_swap(__float_as_uint(v), __float_as_uint(v), false, false); v = __uint_as_float(rr[0]) + __uint_as_float(rr[1]); }
  return v;
}
__global__ __launch_bounds__(512) void k_pre(const unsigned short* __restrict__ c, const unsigned short* __restrict__ wmod, const unsigned short* __restrict__ xin, const char* __restrict__ wq, char* __restrict__ ws DBG_PARAM) {
  ST_DECL
  extern __shared__ __attribute__((aligned(16))) char lds_[];
  LAS char* lds = (LAS char*)lds_;
  const int tid = threadIdx.x, lane = tid & 63, wid = rfl(tid >> 6);
  int b = blockIdx.x;
  if (wid == 7 && b < 224) {
    unsigned long long pc; int d0; unsigned len;
    asm volatile("s_getpc_b64 %0\n.Lkp_here:\n\ts_mov_b32 %1, .Lkm_beg-.Lkp_here\n\ts_mov_b32 %2, .Lkm_fin-.Lkm_beg" : "=s"(pc), "=s"(d0), "=s"(len));
    const unsigned per = (len / 128 + 27) / 28, l0 = (b >> 3) * per + lane;
    unsigned off = (lane < (int)per && l0 * 128 < len) ? l0 * 128 : 0;
    dma4(32768, off, (const void*)(pc + (long long)d0));
  }
  if (b < PRE_MOD) {
    LAS float* sl = (LAS float*)lds; LAS float* red = (LAS float*)(lds + 2304);
    const int rg = tid >> 3, col0 = b * 64 + (tid & 7) * 8;
    u32x4 w[9]; unsigned cw;
    asm volatile("global_load_dword %0, %1, %2" : "=v"(cw) : "v"((unsigned)(tid < 288 ? tid : 0) * 4), "s"(c));
#pragma unroll
    for (int i = 0; i < 9; ++i) gload16<0>(w[i], (unsigned)((rg + 64 * i) * 3456 + col0) * 2, wmod);
    asm volatile("s_waitcnt vmcnt(9)" : "+v"(cw));
    if (tid < 288) *(LAS f32x2*)(sl + 2 * tid) = (f32x2){silu_f(bflo(cw)), silu_f(bfhi(cw))};
    asm volatile("s_waitcnt vmcnt(0) lgkmcnt(0)\n\ts_barrier" ::: "memory");
    float acc[8] = {};
#pragma unroll
    for (int i = 0; i < 9; ++i) { pin(w[i]); const float s = sl[rg + 64 * i];
#pragma unroll
      for (int j = 0; j < 4; ++j) { acc[2 * j] += s * bflo(w[i][j]); acc[2 * j + 1] += s * bfhi(w[i][j]); } }
#pragma unroll
    for (int j = 0; j < 8; ++j) acc[j] = sum_x8_16_32(acc[j]);
    if (lane < 8) { *(LAS f32x4*)(red + wid * 64 + lane * 8) = (f32x4){acc[0], acc[1], acc[2], acc[3]}; *(LAS f32x4*)(red + wid * 64 + lane * 8 + 4) = (f32x4){acc[4], acc[5], acc[6], acc[7]}; }
    asm volatile("s_waitcnt lgkmcnt(0)\n\ts_barrier" ::: "memory");
    if (tid < 64) { float t = 0.f;
#pragma unroll
      for (int w2 = 0; w2 < 8; ++w2) t += red[w2 * 64 + tid];
      ((float*)(ws + WS_MOD))[b * 64 + tid] = t; }
    ST_END(5);
    return;
  }
  b -= PRE_MOD;
  if (b < PRE_CLR) {
    const int cc = lane & 7, row = (b * 8 + wid) * 8 + (lane >> 3);
    u32x4 xv[9];
#pragma unroll
    for (int i = 0; i < 9; ++i) xv[i] = *(const u32x4*)(xin + (size_t)row * D_ + (cc + 8 * i) * 8);
    if (tid < 64) *(u32x4*)(ws + WS_CNT + (size_t)(b * 64 + tid) * 16) = (u32x4){0u, 0u, 0u, 0u};
#pragma unroll
    for (int i = 0; i < 9; ++i) st16c(ws + WS_H1, (unsigned)((b * 9 + i) * 8192 + tid * 16), (u32x4){~0u, ~0u, ~0u, ~0u});
    float sm = 0.f;
#pragma unroll
    for (int i = 0; i < 9; ++i)
#pragma unroll
      for (int j = 0; j < 4; ++j) sm += bflo(xv[i][j]) + bfhi(xv[i][j]);
    const float mean = sum8(sm) * (1.f / 576.f);
    float q = 0.f;
#pragma unroll
    for (int i = 0; i < 9; ++i)
#pragma unroll
      for (int j = 0; j < 4; ++j) { const float d0 = bflo(xv[i][j]) - mean, d1 = bfhi(xv[i][j]) - mean; q += d0 * d0 + d1 * d1; }
    const float rstd = __builtin_amdgcn_rsqf(sum8(q) * (1.f / 576.f) + 1e-5f);
    if (cc == 0) ((f32x2*)(ws + WS_STAT))[row] = (f32x2){mean, rstd};
    ST_END(6);
    return;
  }
  b -= PRE_CLR;
  {
    const int u = b * 8 + wid, s32 = u >> 6, cg = u & 63;
    { const int rr = lane >> 3, pp = lane & 7;
      const unsigned v = rr * 8192 + ((((pp >> 1) ^ ((rr >> 1) & 3)) << 1) + (pp & 1)) * 16;
      const char* src = wq + (size_t)s32 * 32 * 8192 + cg * 128; const unsigned m = wid * 4096;
      dma16<0>(m, v, src); dma16<0>(m + 1024, v, src + 8 * 8192); dma16<0>(m + 2048, v, src + 16 * 8192); dma16<0>(m + 3072, v, src + 24 * 8192); }
    wait_vm<0>();
    const int g = lane >> 4, q = (lane & 15) >> 2, p = lane & 3, h = lane >> 5, blk = g & 1, pc = ((p & 1) << 1) | (p >> 1);
    LAS char* scr = lds + wid * 4096;
#pragma unroll
    for (int dbl = 0; dbl < 2; ++dbl)
#pragma unroll
      for (int ks = 0; ks < 2; ++ks) { const int r0 = 16 * ks + 8 * h + q, r1 = r0 + 4, cx = 2 * dbl + blk;
        const bf16x8 w = cat(tr4(scr + 128 * r0 + 32 * (cx ^ ((r0 >> 1) & 3)) + 8 * pc), tr4(scr + 128 * r1 + 32 * (cx ^ ((r1 >> 1) & 3)) + 8 * pc));
        st16c(ws + WS_WQ, (unsigned)((((cg >> 1) * 36 + 2 * s32 + ks) * 4 + 2 * (cg & 1) + dbl) * 1024 + lane * 16), __builtin_bit_cast(u32x4, w)); }
    ST_END(4);
  }
}

constexpr float THR = 8.0f;
constexpr int A_TAB = 131072;
constexpr int A_LDS = 131072 + 2048;
#define A_LA 4
#define Q_LA 5
struct MainArgs { const char* kc; const char* vc; const char* wg; const char* wu; const char* wo; const char* wd; const unsigned short* x; unsigned short* out; char* ws; };
constexpr int P_STG = 9216, P_NSTG = 4, P_T = 73728;
constexpr int C_BASE = 81920;
constexpr int MAIN_LDS = 163840;
constexpr int CNT_CLEAR = 0;
constexpr int CNT_O = 8, CNT_H = 40;
DI void signal_done(char* ws, int idx, int wid) {
  vm_bar<0>();
  if (wid == 0 && lane_id() == 0) __hip_atomic_fetch_add((unsigned*)(ws + WS_CNT) + idx * 64, 1u, __ATOMIC_RELAXED, __HIP_MEMORY_SCOPE_AGENT);
}
DI void wait_count(const char* ws, int idx, unsigned target, int wid) {
  if (wid == 0) {
    const unsigned* cnt = (const unsigned*)(ws + WS_CNT) + idx * 64;
    for (unsigned spin = 0; spin < (1u << 15); ++spin) {
      if (rfl(__hip_atomic_load(cnt, __ATOMIC_RELAXED, __HIP_MEMORY_SCOPE_AGENT)) >= target) break;
      __builtin_amdgcn_s_sleep(1);
    }
  }
  bar();
}
DI void st_empty(char* base, unsigned voff) { st16c(base, voff, (u32x4){~0u, ~0u, ~0u, ~0u}); }
DI bool has_empty(const bf16x8& a, const bf16x8& b) {
  const u32x4 x = __builtin_bit_cast(u32x4, a), y = __builtin_bit_cast(u32x4, b);
  unsigned m = x[0] > x[1] ? x[0] : x[1]; m = m > x[2] ? m : x[2]; m = m > x[3] ? m : x[3]; m = m > y[0] ? m : y[0]; m = m > y[1] ? m : y[1]; m = m > y[2] ? m : y[2]; m = m > y[3] ? m : y[3];
  return __any(m == ~0u);
}
template <int IMM1> DI void revalidate(bf16x8& a, bf16x8& b, unsigned oa, unsigned ob, const char* base) {
  if (__builtin_expect(has_empty(a, b), 0)) {
    for (unsigned spin = 0; spin < (1u << 15); ++spin) {
      __builtin_amdgcn_s_sleep(3);
      asm volatile("global_load_dwordx4 %0, %2, %4 sc1\n\tglobal_load_dwordx4 %1, %3, %4 offset:%5 sc1\n\ts_waitcnt vmcnt(0)" : "+v"(a), "+v"(b) : "v"(oa), "v"(ob), "s"(base), "i"(IMM1) : "memory");
      if (!has_empty(a, b)) break;
    }
  }
}
DI void proj_prefetch(const char* W, int nst, int wid) {
  const int lane = lane_id();
  const int x = blockIdx.x & 7, j = blockIdx.x >> 3, ks = x >> 1, ct = j & 3, rg = wid & 3, kh = wid >> 2;
  unsigned wsrc3[3];
#pragma unroll
  for (int e = 0; e < 3; ++e) { const int o = 64 * e + lane, row = o / 18, ch = o - row * 18; wsrc3[e] = row * 1152 + ch * 16; }
  const char* Ww = W + (size_t)((ks * 2 + kh) * nst * 32 + rg * 8) * 1152 + ct * 288;
  const unsigned wdst = kh * (P_NSTG * P_STG) + rg * 2304;
#pragma unroll
  for (int s = 0; s < 3; ++s) { const char* wsrc = Ww + (size_t)s * 32 * 1152; const unsigned m = s * P_STG + wdst;
    dma16<0>(m, wsrc3[0], wsrc); dma16<0>(m + 1024, wsrc3[1], wsrc); if (lane < 16) dma16<0>(m + 2048, wsrc3[2], wsrc); }
}
DI void phase_attn(const MainArgs& ar, LAS char* lds, const int wid DBG_PARAM) {
  PH_BEGIN
  char* const ws = ar.ws;
  const int lane = lane_id(), tid = wid * 64 + lane;
  const int x = blockIdx.x & 7, j = blockIdx.x >> 3, head = 4 * x + (j & 3), rt = j >> 2, kvh = head >> 1;
  const int rb = rt * 8 + wid;
  const int prb = 2 * (j >> 2) + (x & 1), pown = (j & 3) * 4 + (x >> 1);
  const int h = lane >> 5, r = lane & 31, blk = (lane >> 4) & 1, q = (lane & 15) >> 2, p = lane & 3;
  const int l4 = lane >> 4, pos = lane & 15;
  unsigned ksrc2[2];
#pragma unroll
  for (int e = 0; e < 2; ++e) { const int n = 2 * wid + e, row = 4 * n + l4, ch = pos ^ ((l4 << 2) | (n & 3)); ksrc2[e] = row * 256 + ch * 16 - e * 1024; }
  const char* wq_h = ws + WS_WQ + (size_t)head * 36 * 4096 + wid * 2048;
  const char* kb_ = ar.kc + ((size_t)kvh * SK + (SK - WIN)) * 256;
  const char* vb_ = ar.vc + ((size_t)kvh * SK + (SK - WIN)) * 256;
  const char* h1w = ws + WS_H1 + (size_t)rb * 36 * 1024;
  const unsigned lane16 = lane * 16;
  auto dmaW = [&](int qq, int e) { const unsigned m = 65536 + (qq & 3) * 16384 + wid * 2048; if (e == 0) dma16<0>(m, lane16, wq_h + qq * 16384); else dma16<1024>(m, lane16, wq_h + qq * 16384); };
  auto dmaK8 = [&](int t) { const unsigned m = (t & 3) * 16384 + wid * 2048; const char* s = kb_ + t * 16384; dma16<0>(m, ksrc2[0], s); dma16<1024>(m, ksrc2[1], s); };
  auto dmaV8 = [&](int t) { const unsigned m = 65536 + ((t + 1) & 3) * 16384 + wid * 2048; const char* s = vb_ + t * 16384; dma16<0>(m, ksrc2[0], s); dma16<1024>(m, ksrc2[1], s); };
  bf16x8 g[9][4];
  auto loadG1 = [&](auto QQ, auto E) { constexpr int qq = QQ, e = E; gload16c<e * 1024>(g[qq][e], lane16, h1w + qq * 4096); };
  auto checkG = [&](auto QQ) { constexpr int qq = QQ; static_for<4>([&](auto E) { pin(g[qq][E]); });
    revalidate<1024>(g[qq][0], g[qq][1], lane16, lane16, h1w + qq * 4096); revalidate<1024>(g[qq][2], g[qq][3], lane16, lane16, h1w + qq * 4096 + 2048); };
  auto loadG = [&](auto QQ) { static_for<4>([&](auto E) { loadG1(QQ, E); }); };
  auto mark = [&](auto MI) { constexpr int mi = MI;
    if constexpr (mi < 2) { const int rown = prb * 128 + pown * 8 + (tid & 7), kc = mi == 0 ? (tid >> 3) : 64 + (lane >> 3);
      st_empty(ws + WS_H2, (unsigned)(((rown >> 5) * 36 + (kc >> 1)) * 1024 + ((kc & 1) * 32 + (rown & 31)) * 16)); }
    else { const int n = tid + 512 * (mi - 2), n2 = n < 2304 ? n : n - 2304;
      st_empty(ws + WS_PB + (size_t)prb * 16 * 16 * 2304 + (size_t)pown * 2304, (unsigned)((n < 2304 ? 0 : (unsigned)(WS_PD - WS_PB)) + (n2 / 144) * 36864 + (n2 % 144) * 16)); } };
  {
    const int row = rt * 256 + (8 * (j & 3) + x) * 8 + (tid & 7), u0 = tid >> 3, u1 = 64 + (lane >> 3);
    u32x4 xv[2]; f32x4 sa[2][2], ca[2][2]; f32x2 mr;
    asm volatile("global_load_dwordx2 %0, %1, %2" : "=v"(mr) : "v"((unsigned)row * 8), "s"(ws + WS_STAT));
    gload16<0>(xv[0], (unsigned)(row * D_ + u0 * 8) * 2, ar.x); gload16<0>(xv[1], (unsigned)(row * D_ + u1 * 8) * 2, ar.x);
#pragma unroll
    for (int i = 0; i < 2; ++i) { const unsigned o = (i ? u1 : u0) * 32; gload16<0>(sa[i][0], o, ws + WS_MOD); gload16<16>(sa[i][1], o, ws + WS_MOD); gload16<0>(ca[i][0], o, ws + WS_MOD + 2304); gload16<16>(ca[i][1], o, ws + WS_MOD + 2304); }
    dmaW(0, 0); dmaW(0, 1); dmaW(1, 0); dmaW(1, 1); dmaW(2, 0); dmaW(2, 1); dmaK8(0);
    wait_vm<8>(); pin(mr);
    const float rq = mr[1] * QSCALE;
#pragma unroll
    for (int i = 0; i < 2; ++i) { pin(xv[i]); pin(sa[i][0]); pin(sa[i][1]); pin(ca[i][0]); pin(ca[i][1]);
      const f32x4 v0 = {bflo(xv[i][0]), bfhi(xv[i][0]), bflo(xv[i][1]), bfhi(xv[i][1])}, v1 = {bflo(xv[i][2]), bfhi(xv[i][2]), bflo(xv[i][3]), bfhi(xv[i][3])};
      const f32x4 h0 = (v0 - mr[0]) * rq * (ca[i][0] + 1.f) + sa[i][0] * QSCALE, h1v = (v1 - mr[0]) * rq * (ca[i][1] + 1.f) + sa[i][1] * QSCALE; const int u = i ? u1 : u0;
      st16c(ws + WS_H1, (unsigned)(((row >> 5) * 36 + (u >> 1)) * 1024 + ((u & 1) * 32 + (row & 31)) * 16), (u32x4){pk2(h0[0], h0[1]), pk2(h0[2], h0[3]), pk2(h1v[0], h1v[1]), pk2(h1v[2], h1v[3])}); }
  }
  ST(10);
#define H1_SLEEP 16
  __builtin_amdgcn_s_sleep(H1_SLEEP);
  for (unsigned spin = 0; spin < (1u << 15); ++spin) {
    loadG(integral_constant<int, 0>{}); wait_vm<0>();
    static_for<4>([&](auto E) { pin(g[0][E]); });
    if (!has_empty(g[0][0], g[0][1]) && !has_empty(g[0][2], g[0][3])) break;
    __builtin_amdgcn_s_sleep(2);
  }
  ST(11);
  loadG(integral_constant<int, 1>{}); loadG(integral_constant<int, 2>{});
  unsigned wb0 = 65536 + lane16;
  pin(wb0);
  f32x16 acc[4] = {};
  {
    constexpr int LA = Q_LA, NIT = 9 * 16;
    bf16x8 wf[LA + 1];
    auto rd = [&](auto I) { constexpr int i = I; constexpr int qq = i / 16, ks = (i % 16) / 4, db = i % 4;
      wf[i % (LA + 1)] = *(LAS bf16x8*)(lds + wb0 + (qq & 3) * 16384 + ks * 4096 + db * 1024); };
    SB(); bar();
    SB();
    ST(1);
    static_for<LA>([&](auto I) { rd(I); });
    SB();
    static_for<NIT>([&](auto I) { constexpr int i = I; constexpr int qq = i / 16, rr = i % 16, ks = rr / 4, db = rr % 4;
      if constexpr (rr == 0 && qq > 0) {
        constexpr int NW[9] = {8, 10, 10, 10, 10, 10, 10, 8, 8};
        vm_bar<NW[qq]>();
        checkG(integral_constant<int, qq>{});
        if constexpr (qq == 4) ST(2);
        SB();
      }
      if constexpr (i + LA < NIT) rd(integral_constant<int, i + LA>{});
      acc[db] = mfma32(wf[i % (LA + 1)], g[qq][ks], acc[db]);
      if constexpr (qq + 3 < 9) {
        if constexpr (rr == 1) dmaW(qq + 3, 0);
        if constexpr (rr == 3) dmaW(qq + 3, 1);
        if constexpr (rr >= 5 && rr <= 11 && (rr & 1)) loadG1(integral_constant<int, qq + 3>{}, integral_constant<int, (rr - 5) / 2>{});
      } else {
        if constexpr (rr == 1) dmaV8(qq - 6);
        if constexpr (rr == 7) dmaK8(qq - 5);
      }
      SB();
    });
  }
  ST(3);
  bf16x8 qf[8];
#pragma unroll
  for (int db = 0; db < 4; ++db)
#pragma unroll
    for (int s = 0; s < 2; ++s) { u32x4 w;
#pragma unroll
      for (int i = 0; i < 4; ++i) w[i] = pk2(acc[db][8 * s + 2 * i], acc[db][8 * s + 2 * i + 1]);
      qf[2 * db + s] = __builtin_bit_cast(bf16x8, w); }
  const unsigned kb0 = 256 * r + 16 * (h ^ (((r & 3) << 2) | ((r >> 2) & 3)));
  const unsigned vb0 = 65536 + 256 * (4 * h + q) + 16 * ((q << 2) | ((2 * blk + (p >> 1)) ^ h)) + 8 * (p & 1);
  unsigned ka[8], va[4][2];
#pragma unroll
  for (int s = 0; s < 8; ++s) ka[s] = kb0 ^ (s << 5);
#pragma unroll
  for (int db = 0; db < 4; ++db) { va[db][0] = vb0 ^ (db << 6); va[db][1] = vb0 ^ (db << 6) ^ 32; }
  f32x16 o[4] = {}; f32x16 S[2][2], negm; unsigned pw[16]; float m; f32x2 l2 = {0.f, 0.f};
  constexpr int LA = A_LA;
  bf16x8 fr[LA + 1];
  struct Map { int g[272]; int n; };
  constexpr Map MP = []() constexpr { Map mp{}; mp.n = 0; for (int G = 0; G < 9 * 32; ++G) { const int t = G / 32 - 1, i = G % 32; if (i < 16 ? (t + 1 <= 7) : (t >= 0 && t <= 7)) mp.g[mp.n++] = G; } return mp; }();
  constexpr int NIT = MP.n;
  auto rdA = [&](auto C) { constexpr int c = C; constexpr int G = MP.g[c], t = G / 32 - 1, i = G % 32;
    if constexpr (i < 16) { constexpr int s = i >> 1, kb = i & 1; fr[c % (LA + 1)] = *(LAS bf16x8*)(lds + ka[s] + ((t + 1) & 3) * 16384 + kb * 8192); }
    else { constexpr int ii = i - 16, kk = ii >> 2, db = ii & 3; constexpr int so = ((t + 1) & 3) * 16384 + kk * 4096;
      fr[c % (LA + 1)] = cat(tr4(lds + va[db][0] + so), tr4(lds + va[db][1] + so + 2048)); } };
  auto rowmax_finish = [&](float a) { auto rr = __builtin_amdgcn_permlane32_swap(__float_as_uint(a), __float_as_uint(a), false, false); return max2f(__uint_as_float(rr[0]), __uint_as_float(rr[1])); };
  float mxa, mxb;
  const unsigned gw = blockIdx.x * 8 + wid;
  auto touch = [&](auto TI) { constexpr int ti = TI; constexpr int m = ti >> 1, half = ti & 1;
    constexpr unsigned per = m == 0 ? 9 : 11, tot = m == 0 ? 36864 : 41472;
    unsigned line = (gw * 2 + half) * per + (lane < (int)per ? lane : 0); if (line >= tot) line = tot - 1;
    dma4(A_TAB + 2048 + wid * 256, line * 128, m == 0 ? ar.wo : (m == 1 ? ar.wg : (m == 2 ? ar.wu : ar.wd))); };
  auto background = [&](auto BI) { constexpr int bi = BI;
    if constexpr (bi < 16) { if constexpr ((bi & 3) < 2) touch(integral_constant<int, (bi >> 2) * 2 + (bi & 1)>{}); else mark(integral_constant<int, (bi >> 2) * 2 + (bi & 1)>{}); }
    else mark(integral_constant<int, bi - 8>{}); };
  SB(); vm_bar<8>(); SB();
  ST(4);
  static_for<LA>([&](auto C) { rdA(C); });
  SB();
  static_for<NIT>([&](auto C) { constexpr int c = C; constexpr int G = MP.g[c], t = G / 32 - 1, i = G % 32;
    constexpr bool firstX = (i == 0) || (t == 7 && i == 16);
    if constexpr (firstX && t >= 0) {
      constexpr int NW[8] = {4, 8, 12, 12, 12, 9, 3, 0};
      float a = rowmax_finish(max2f(mxa, mxb));
      if constexpr (t == 0) {
        m = a; negm = splat16(-a);
#pragma unroll
        for (int r2 = 0; r2 < 16; ++r2) { S[0][0][r2] -= a; S[0][1][r2] -= a; }
      } else if (__builtin_expect(__any(a > THR), 0)) {
        const float dl = fmaxf(a, 0.f), f = __builtin_amdgcn_exp2f(-dl);
        m += dl; negm = splat16(-m); l2 *= f;
#pragma unroll
        for (int r2 = 0; r2 < 16; ++r2) { S[t & 1][0][r2] -= dl; S[t & 1][1][r2] -= dl; }
#pragma unroll
        for (int d = 0; d < 4; ++d)
#pragma unroll
          for (int r2 = 0; r2 < 16; ++r2) o[d][r2] *= f;
      }
      SB(); vm_bar<NW[t]>(); SB();
      if constexpr (t == 7) { if (wid == 0 && lane == 0) __hip_atomic_fetch_add((unsigned*)(ws + WS_CNT) + (CNT_CLEAR + rt) * 64, 1u, __ATOMIC_RELAXED, __HIP_MEMORY_SCOPE_AGENT); }
      if constexpr (t == 3) ST(5);
      if constexpr (t == 4) ST(7);
      if constexpr (t == 5) ST(8);
      if constexpr (t == 7) proj_prefetch(ar.wo, 16, wid);
      if constexpr (t == 7) {
#pragma unroll
        for (int e = 0; e < 32; ++e) S[1][e >> 4][e & 15] = __builtin_amdgcn_exp2f(S[1][e >> 4][e & 15]);
#pragma unroll
        for (int e = 0; e < 16; ++e) pw[e] = pk2(S[1][e >> 3][(2 * e) & 15], S[1][e >> 3][((2 * e) & 15) + 1]);
        SB();
      }
    }
    if constexpr (c + LA < NIT) rdA(integral_constant<int, c + LA>{});
    if constexpr (i < 16) {
      constexpr int s = i >> 1, kb = i & 1, X = (t + 1) & 1;
      if constexpr (s == 0) { if constexpr (t < 0) S[X][kb] = mfma32(fr[c % (LA + 1)], qf[s], f32x16{}); else S[X][kb] = mfma32(fr[c % (LA + 1)], qf[s], negm); }
      else S[X][kb] = mfma32(fr[c % (LA + 1)], qf[s], S[X][kb]);
      if constexpr (t >= 0) {
        constexpr int e0 = 2 * i, e1 = 2 * i + 1, Y = t & 1;
        S[Y][e0 >> 4][e0 & 15] = __builtin_amdgcn_exp2f(S[Y][e0 >> 4][e0 & 15]);
        S[Y][e1 >> 4][e1 & 15] = __builtin_amdgcn_exp2f(S[Y][e1 >> 4][e1 & 15]);
        if constexpr (i >= 1) { constexpr int e = i - 1; pw[e] = pk2(S[Y][e >> 3][(2 * e) & 15], S[Y][e >> 3][((2 * e) & 15) + 1]); }
        if constexpr (i == 15) { SB(); pw[15] = pk2(S[Y][1][14], S[Y][1][15]); }
        if constexpr (t <= 3 && i == 6) dma16<0>((t & 3) * 16384 + wid * 2048, ksrc2[0], kb_ + (t + 4) * 16384);
        if constexpr (t <= 3 && i == 9) dma16<1024>((t & 3) * 16384 + wid * 2048, ksrc2[1], kb_ + (t + 4) * 16384);
        if constexpr (t <= 4 && i == 12) dma16<0>(65536 + (t & 3) * 16384 + wid * 2048, ksrc2[0], vb_ + (t + 3) * 16384);
        if constexpr (t <= 4 && i == 15) dma16<1024>(65536 + (t & 3) * 16384 + wid * 2048, ksrc2[1], vb_ + (t + 3) * 16384);
      }
      if constexpr (t < 0 && i == 15) {
        asm volatile("s_nop 11" : "+v"(S[0][0]), "+v"(S[0][1]));
        mxa = max3f(S[0][0][0], S[0][0][1], S[0][0][2]); mxb = max3f(S[0][1][0], S[0][1][1], S[0][1][2]);
#pragma unroll
        for (int r2 = 3; r2 < 15; r2 += 2) { mxa = max3f(mxa, S[0][0][r2], S[0][0][r2 + 1]); mxb = max3f(mxb, S[0][1][r2], S[0][1][r2 + 1]); }
        mxa = max3f(mxa, S[0][0][15], S[0][1][15]);
      }
    } else {
      constexpr int ii = i - 16, kk = ii >> 2, db = ii & 3, Y = t & 1, X = (t + 1) & 1;
      const u32x4 w = {pw[4 * kk], pw[4 * kk + 1], pw[4 * kk + 2], pw[4 * kk + 3]};
      o[db] = mfma32(fr[c % (LA + 1)], __builtin_bit_cast(bf16x8, w), o[db]);
      if constexpr (t <= 4 && (ii & 3) == 1 && 4 * t + (ii >> 2) < 19) background(integral_constant<int, 4 * t + (ii >> 2)>{});
      l2 += (f32x2){S[Y][ii >> 3][(2 * ii) & 15], S[Y][ii >> 3][((2 * ii) & 15) + 1]};
      if constexpr (t < 7) {
        if constexpr (ii == 4) { mxa = max3f(S[X][0][0], S[X][0][1], S[X][0][2]); mxb = max3f(S[X][0][3], S[X][0][4], S[X][0][5]); }
        if constexpr (ii == 5) { mxa = max3f(mxa, S[X][0][6], S[X][0][7]); mxb = max3f(mxb, S[X][0][8], S[X][0][9]); }
        if constexpr (ii == 6) { mxa = max3f(mxa, S[X][0][10], S[X][0][11]); mxb = max3f(mxb, S[X][0][12], S[X][0][13]); }
        if constexpr (ii == 7) { mxa = max3f(mxa, S[X][0][14], S[X][0][15]); mxb = max3f(mxb, S[X][1][0], S[X][1][1]); }
        if constexpr (ii == 8) { mxa = max3f(mxa, S[X][1][2], S[X][1][3]); mxb = max3f(mxb, S[X][1][4], S[X][1][5]); }
        if constexpr (ii == 9) { mxa = max3f(mxa, S[X][1][6], S[X][1][7]); mxb = max3f(mxb, S[X][1][8], S[X][1][9]); }
        if constexpr (ii == 10) { mxa = max3f(mxa, S[X][1][10], S[X][1][11]); mxb = max3f(mxb, S[X][1][12], S[X][1][13]); }
        if constexpr (ii == 11) { mxa = max3f(mxa, S[X][1][14], S[X][1][15]); }
      }
    }
    SB();
  });
  ST(12);
  float l = l2[0] + l2[1];
  { auto rr = __builtin_amdgcn_permlane32_swap(__float_as_uint(l), __float_as_uint(l), false, false); l = __uint_as_float(rr[0]) + __uint_as_float(rr[1]); }
  const float inv = __builtin_amdgcn_rcpf(l);
  char* ob = ws + WS_O + ((size_t)rb * 256 + head * 8) * 1024;
#pragma unroll
  for (int db = 0; db < 4; ++db)
#pragma unroll
    for (int s = 0; s < 2; ++s) { u32x4 w;
#pragma unroll
      for (int i = 0; i < 4; ++i) w[i] = pk2(o[db][8 * s + 2 * i] * inv, o[db][8 * s + 2 * i + 1] * inv);
      st16c(ob, lane16 + (2 * db + s) * 1024, w); }
  signal_done(ws, CNT_O + rt * 4 + (head >> 3), wid);
  ST_END(0);
}

#define P_LA 4
DI void ffn1_prefetch(const MainArgs& ar, int wid);
template <bool FINAL>
DI void phase_proj(const MainArgs& ar, LAS char* lds, const int wid DBG_PARAM) {
  PH_BEGIN
  char* const ws = ar.ws; const char* W = FINAL ? ar.wd : ar.wo; const unsigned short* xin = ar.x; unsigned short* out = ar.out;
  constexpr int NS16 = FINAL ? 288 : 256, NST = NS16 / 16;
  const char* act = ws + (FINAL ? WS_H : WS_O);
  const float* mod = (const float*)(ws + WS_MOD);
  char* part = ws + (FINAL ? WS_PD : WS_PB);
  const int lane = lane_id(), tid = wid * 64 + lane, rg = wid & 3, kh = wid >> 2;
  const int x = blockIdx.x & 7, j = blockIdx.x >> 3, ks = x >> 1, rb = 2 * (j >> 2) + (x & 1), ct = j & 3;
  const int own = ct * 4 + ks;
  const int g = lane >> 4, m16 = lane & 15, q4 = (lane & 15) >> 2, p = lane & 3;
  const int kb32 = (ks * 2 + kh) * NST;
  unsigned aoff[2];
#pragma unroll
  for (int a = 0; a < 2; ++a) { const int row = rb * 128 + rg * 32 + a * 16 + m16; aoff[a] = (unsigned)(row >> 5) * (NS16 * 1024) + (row & 31) * 16 + (g >> 1) * 1024 + (g & 1) * 512; }
  const char* actw = act + (size_t)kb32 * 2048;
  unsigned wsrc3[3];
#pragma unroll
  for (int e = 0; e < 3; ++e) { const int o = 64 * e + lane, row = o / 18, ch = o - row * 18; wsrc3[e] = row * 1152 + ch * 16; }
  const char* Ww = W + (size_t)(kb32 * 32 + rg * 8) * 1152 + ct * 288;
  const unsigned wdst = kh * (P_NSTG * P_STG) + rg * 2304;
  bf16x8 af[NST][2];
  auto issue_piece = [&](auto S, auto E) { constexpr int s = S, e = E;
    const char* wsrc = Ww + (size_t)s * 32 * 1152; const unsigned m = (s % P_NSTG) * P_STG + wdst;
    if constexpr (e == 0) dma16<0>(m, wsrc3[0], wsrc);
    if constexpr (e == 1) dma16<0>(m + 1024, wsrc3[1], wsrc);
    if constexpr (e == 2) { if (lane < 16) dma16<0>(m + 2048, wsrc3[2], wsrc); }
    if constexpr (e == 3) gload16c<0>(af[s][0], aoff[0], actw + s * 2048);
    if constexpr (e == 4) gload16c<0>(af[s][1], aoff[1], actw + s * 2048); };
  if (!FINAL && wid != 0) { ffn1_prefetch(ar, wid); if (wid == 1) ffn1_prefetch(ar, 0); }
  wait_count(ws, (FINAL ? CNT_H : CNT_O) + (rb >> 1) * 4 + ks, 8u, wid);
  static_for<3>([&](auto S) { issue_piece(S, integral_constant<int, 3>{}); issue_piece(S, integral_constant<int, 4>{}); });
  unsigned tb[P_NSTG];
#pragma unroll
  for (int i = 0; i < P_NSTG; ++i) tb[i] = kh * (P_NSTG * P_STG) + i * P_STG + 288 * (16 * (g >> 1) + 4 * (g & 1) + q4) + 8 * p;
  f32x4 acc[2][9] = {};
  constexpr int LA = P_LA, NIT = NST * 9;
  bf16x8 wf[LA + 1];
  auto rd = [&](auto I) { constexpr int i = I; constexpr int s = i / 9, c = i % 9;
    wf[i % (LA + 1)] = cat(tr4(lds + tb[s % P_NSTG] + 32 * c), tr4(lds + tb[s % P_NSTG] + 288 * 8 + 32 * c)); };
  constexpr int NPF = 0, NTB = FINAL ? 3 : 2;
  constexpr int TB = FINAL ? 81920 : 137600;
  SB(); vm_bar<4 + NPF>(); pin(af[0][0]); pin(af[0][1]);
#pragma unroll
  for (int e = 0; e < NTB; ++e) { const int n = NTB * wid + e;
    if (!FINAL) dma16<0>(TB + n * 1024, lane * 16, n < 7 ? (const char*)(mod + 1152) + n * 1024 : (const char*)xin + (size_t)(rb * 128 + own * 8) * 1152 + (n - 7) * 1024);
    else { const int n2 = n < 21 ? n : 0; dma16<0>(TB + n2 * 1024, lane * 16, n2 < 3 ? (const char*)(mod + 2880) + n2 * 1024 : ws + WS_X1R + (size_t)blockIdx.x * 576 * 32 + (n2 - 3) * 1024); } }
  SB();
  ST(1);
  static_for<LA>([&](auto I) { rd(I); });
  SB();
  static_for<NIT>([&](auto I) { constexpr int i = I; constexpr int s = i / 9, c = i % 9;
    if constexpr (c == 0 && s > 0) {
      constexpr int outst = s == 1 ? 2 + NPF + NTB + 5 : ((s + 2 < NST) ? 5 : 0);
      vm_bar<outst>(); pin(af[s][0]); pin(af[s][1]);
      if constexpr (s == 8) ST(2);
      SB();
    }
    if constexpr (i + LA < NIT) rd(integral_constant<int, i + LA>{});
    acc[0][c] = mfma16(wf[i % (LA + 1)], af[s][0], acc[0][c]);
    acc[1][c] = mfma16(wf[i % (LA + 1)], af[s][1], acc[1][c]);
    if constexpr (s + 3 < NST && c >= 1 && c <= 5) issue_piece(integral_constant<int, s + 3>{}, integral_constant<int, c - 1>{});
    SB();
  });
  ST(5);
  const int r8 = tid & 7, rown = rb * 128 + own * 8 + r8;
  int colA[2], colB[2];
  colA[0] = 144 * (tid >> 7) + 32 * ((tid >> 5) & 3) + 4 * ((tid >> 3) & 3); colB[0] = colA[0] + 16;
  colA[1] = 288 * ((tid >> 5) & 1) + 128 + 4 * ((tid >> 3) & 3); colB[1] = colA[1] + 144;
  unsigned marked;
  if (!FINAL) asm volatile("global_load_dword %0, %1, %2 sc1" : "=v"(marked) : "v"(0u), "s"(ws + WS_CNT + (size_t)(CNT_CLEAR + (rb >> 1)) * 256));
  {
    LAS char* exw = lds + (rg * 2 + kh) * 9216 + lane * 16; LAS char* exr = lds + (rg * 2 + (kh ^ 1)) * 9216 + lane * 16;
    bar();
    ST(3);
    f32x4 fin[9];
    if (kh) {
#pragma unroll
      for (int c = 0; c < 9; ++c) { *(LAS f32x4*)(exw + c * 1024) = acc[0][c]; fin[c] = acc[1][c]; }
    } else {
#pragma unroll
      for (int c = 0; c < 9; ++c) { *(LAS f32x4*)(exw + c * 1024) = acc[1][c]; fin[c] = acc[0][c]; }
    }
    asm volatile("s_waitcnt lgkmcnt(0)\n\ts_barrier" ::: "memory");
    ST(4);
    char* pw = part + (size_t)rb * 16 * 16 * 2304 + (size_t)own * 2304;
#pragma unroll
    for (int c = 0; c < 9; ++c) fin[c] += *(LAS f32x4*)(exr + c * 1024);
    const unsigned jo = (unsigned)(rg * 4 + kh * 2 + (m16 >> 3)) * 36864, lo = g * 8 + (m16 & 7);
    u32x4 cw[4];
#pragma unroll
    for (int cp = 0; cp < 4; ++cp) cw[cp] = (u32x4){pk2(fin[2 * cp][0], fin[2 * cp][1]), pk2(fin[2 * cp][2], fin[2 * cp][3]), pk2(fin[2 * cp + 1][0], fin[2 * cp + 1][1]), pk2(fin[2 * cp + 1][2], fin[2 * cp + 1][3])};
    const u32x2 w8 = {pk2(fin[8][0], fin[8][1]), pk2(fin[8][2], fin[8][3])};
#pragma unroll
    for (int cp = 0; cp < 4; ++cp) st16c(pw, jo + (cp * 32 + lo) * 16, cw[cp]);
    asm volatile("global_store_dwordx2 %0, %1, %2" STF "\n\ts_nop 1" ::"v"(jo + 2048 + lo * 8), "v"(w8), "s"(pw) : "memory");
  }
  ST(7);
  const char* pr = part + ((size_t)rb * 16 + own) * 16 * 2304;
  const bool two = wid == 0;
  u32x4 pv[4]; u32x2 pq[4][2];
  if (!FINAL) {
    asm volatile("s_waitcnt vmcnt(5)" : "+v"(marked) :: "memory");
    if (!__all(marked >= 32u)) {
      const unsigned* cnt = (const unsigned*)(ws + WS_CNT) + (CNT_CLEAR + (rb >> 1)) * 64;
      for (unsigned spin = 0; spin < (1u << 15); ++spin) { if (__all(__hip_atomic_load(cnt, __ATOMIC_RELAXED, __HIP_MEMORY_SCOPE_AGENT) >= 32u)) break; __builtin_amdgcn_s_sleep(1); }
    }
  }
#define POLLMODE 1
#define P_SLEEP 20
  if (wid == 7) {
    for (unsigned spin = 0; spin < (1u << 15); ++spin) {
      unsigned t; asm volatile("global_load_dword %0, %1, %2 sc1\n\ts_waitcnt vmcnt(0)" : "=&v"(t) : "v"((unsigned)((lane & 15) * 2304 + 2300)), "s"(pr) : "memory");
      if (__all(t != ~0u)) break;
      __builtin_amdgcn_s_sleep(1);
    } }
  bar();
  ST(8);
  for (unsigned spin = 0; spin < (1u << 15); ++spin) {
    const unsigned o0 = (unsigned)((tid >> 7) * 4 * 2304 + (tid & 127) * 16), o1 = (unsigned)(((tid >> 5) & 1) * 8 * 2304 + 2048 + (tid & 31) * 8);
#pragma unroll
    for (int k = 0; k < 4; ++k) gload16c<0>(pv[k], o0 + k * 2304, pr);
    if (two) {
#pragma unroll
      for (int k = 0; k < 4; ++k) {
        asm volatile("global_load_dwordx2 %0, %1, %2" LDF : "=v"(pq[k][0]) : "v"(o1 + k * 2304), "s"(pr));
        asm volatile("global_load_dwordx2 %0, %1, %2" LDF : "=v"(pq[k][1]) : "v"(o1 + (4 + k) * 2304), "s"(pr)); } }
    wait_vm<0>();
    bool full = true;
#pragma unroll
    for (int k = 0; k < 4; ++k) { pin(pv[k]); full &= pv[k][0] != ~0u && pv[k][1] != ~0u && pv[k][2] != ~0u && pv[k][3] != ~0u; }
    if (two) {
#pragma unroll
      for (int k = 0; k < 4; ++k) { pin(pq[k][0]); pin(pq[k][1]); full &= pq[k][0][0] != ~0u && pq[k][0][1] != ~0u && pq[k][1][0] != ~0u && pq[k][1][1] != ~0u; } }
    if (__all(full)) break;
    __builtin_amdgcn_s_sleep(4);
  }
  ST(9);
  f32x4 v[2][2], sc[2][2], sh[2][2];
#pragma unroll
  for (int i = 0; i < 2; ++i) if (i == 0 || two) {
#pragma unroll
    for (int hh = 0; hh < 2; ++hh) { const int col = hh ? colB[i] : colA[i]; f32x4 rs;
      const f32x4 gt = *(LAS f32x4*)(lds + TB + col * 4);
      if (FINAL) rs = *(LAS f32x4*)(lds + TB + 3072 + (i ? 512 + (tid & 63) : tid) * 32 + hh * 16);
      else { const u32x2 xr = *(LAS u32x2*)(lds + TB + 7168 + r8 * 1152 + col * 2); rs = (f32x4){bflo(xr[0]), bfhi(xr[0]), bflo(xr[1]), bfhi(xr[1])};
        sh[i][hh] = *(LAS f32x4*)(lds + TB + (576 + col) * 4); sc[i][hh] = *(LAS f32x4*)(lds + TB + (1152 + col) * 4); }
      f32x4 t = {0.f, 0.f, 0.f, 0.f};
#pragma unroll
      for (int k = 0; k < 4; ++k) { const unsigned w0 = i ? pq[k][hh][0] : pv[k][2 * hh], w1 = i ? pq[k][hh][1] : pv[k][2 * hh + 1]; t += (f32x4){bflo(w0), bfhi(w0), bflo(w1), bfhi(w1)}; }
      v[i][hh] = rs + gt * t; }
  }
  ST(10);
  if (FINAL) {
#pragma unroll
    for (int i = 0; i < 2; ++i) if (i == 0 || two) {
      *(u32x2*)(out + (size_t)rown * D_ + colA[i]) = (u32x2){pk2(v[i][0][0], v[i][0][1]), pk2(v[i][0][2], v[i][0][3])};
      *(u32x2*)(out + (size_t)rown * D_ + colB[i]) = (u32x2){pk2(v[i][1][0], v[i][1][1]), pk2(v[i][1][2], v[i][1][3])}; }
  } else {
    LAS float* st = (LAS float*)(lds + P_T);
    auto s8 = [](const f32x4& a, const f32x4& b) { return ((a[0] + a[1]) + (a[2] + a[3])) + ((b[0] + b[1]) + (b[2] + b[3])); };
    float s = s8(v[0][0], v[0][1]) + (two ? s8(v[1][0], v[1][1]) : 0.f);
    s = sum_x8_16_32(s);
    if (lane < 8) st[wid * 8 + lane] = s;
    asm volatile("s_waitcnt lgkmcnt(0)\n\ts_barrier" ::: "memory");
    float tot = 0.f;
#pragma unroll
    for (int w = 0; w < 8; ++w) tot += st[w * 8 + r8];
    const float mean = tot * (1.f / 576.f);
    f32x4 d[2][2];
#pragma unroll
    for (int i = 0; i < 2; ++i) { d[i][0] = v[i][0] - mean; d[i][1] = v[i][1] - mean; }
    float qq = s8(d[0][0] * d[0][0], d[0][1] * d[0][1]) + (two ? s8(d[1][0] * d[1][0], d[1][1] * d[1][1]) : 0.f);
    qq = sum_x8_16_32(qq);
    if (lane < 8) st[64 + wid * 8 + lane] = qq;
    asm volatile("s_waitcnt lgkmcnt(0)\n\ts_barrier" ::: "memory");
    float tq = 0.f;
#pragma unroll
    for (int w = 0; w < 8; ++w) tq += st[64 + w * 8 + r8];
    const float rstd = __builtin_amdgcn_rsqf(tq * (1.f / 576.f) + 1e-5f);
#pragma unroll
    for (int i = 0; i < 2; ++i) if (i == 0 || two) {
      const f32x4 h0 = d[i][0] * rstd * (sc[i][0] + 1.f) + sh[i][0], h1 = d[i][1] * rstd * (sc[i][1] + 1.f) + sh[i][1];
      const int kc = i ? 64 + (tid >> 3) : (tid >> 3);
      st16c(ws + WS_H2, (unsigned)(((rown >> 5) * 36 + (kc >> 1)) * 1024 + ((kc & 1) * 32 + (rown & 31)) * 16), (u32x4){pk2(h0[0], h0[1]), pk2(h0[2], h0[3]), pk2(h1[0], h1[1]), pk2(h1[2], h1[3])});
      char* xb = ws + WS_X1R + (size_t)blockIdx.x * 576 * 32 + (i ? 512 + (tid & 63) : tid) * 32;
      *(f32x4*)xb = v[i][0]; *(f32x4*)(xb + 16) = v[i][1]; }
  }
  ST_END(FINAL ? 3 : 1);
}

constexpr int C_STG = 9216 + 128 + 9216, C_UOFF = 9216 + 128, C_NSTG = 4;
DI unsigned geglu2(f32x2 g, f32x2 u) {
  const float c0 = -2.f * 0.7978845608028654f * 1.4426950408889634f, c1 = c0 * 0.044715f;
  const f32x2 z = g * (g * g * c1 + c0);
  const f32x2 d = (f32x2){__builtin_amdgcn_exp2f(z[0]), __builtin_amdgcn_exp2f(z[1])} + 1.f;
  const f32x2 o = (g * u) * (f32x2){__builtin_amdgcn_rcpf(d[0]), __builtin_amdgcn_rcpf(d[1])};
  return pk2(o[0], o[1]);
}
#define C_LA 6
DI void ffn1_prefetch(const MainArgs& ar, int wid) {
  const int lane = lane_id();
  const int x = blockIdx.x & 7, j = blockIdx.x >> 3, ct = 4 * x + (j & 3);
  unsigned wsrc3[3];
#pragma unroll
  for (int e = 0; e < 3; ++e) { const int o = 64 * e + lane, row = o / 18, ch = o - row * 18; wsrc3[e] = (16 * (row >> 2) + (row & 3)) * (FF * 2) + ch * 16; }
  const char* Ww = (wid < 4 ? ar.wg : ar.wu) + (size_t)(wid & 3) * 4 * (FF * 2) + ct * 288;
  const unsigned wdst = C_BASE + (wid < 4 ? 0 : C_UOFF) + (wid & 3) * 2304;
#pragma unroll
  for (int s = 0; s < 3; ++s) { const char* wsrc = Ww + (size_t)(32 * s) * (FF * 2); const unsigned m = s * C_STG + wdst;
    dma16<0>(m, wsrc3[0], wsrc); dma16<0>(m + 1024, wsrc3[1], wsrc); if (lane < 16) dma16<0>(m + 2048, wsrc3[2], wsrc); }
}
DI void phase_ffn1(const MainArgs& ar, LAS char* lds, const int wid DBG_PARAM) {
  PH_BEGIN
  char* const ws = ar.ws; const char* wg = ar.wg; const char* wu = ar.wu;
  const int lane = lane_id(), tid = wid * 64 + lane;
  const int x = blockIdx.x & 7, j = blockIdx.x >> 3, ct = 4 * x + (j & 3), rt = j >> 2;
  const int rb = rt * 8 + wid;
  const int h = lane >> 5, blk = (lane >> 4) & 1, q = (lane & 15) >> 2, p = lane & 3;
  unsigned wsrc3[3], wsrc3b[3];
#pragma unroll
  for (int e = 0; e < 3; ++e) { const int o = 64 * e + lane, row = o / 18, ch = o - row * 18;
    wsrc3[e] = (16 * (row >> 2) + (row & 3)) * (FF * 2) + ch * 16; wsrc3b[e] = (144 * (row >> 2) + (row & 3)) * (FF * 2) + ch * 16; }
  const char* Ww = (wid < 4 ? wg : wu) + (size_t)(wid & 3) * 4 * (FF * 2) + ct * 288;
  const unsigned wdst = C_BASE + (wid < 4 ? 0 : C_UOFF) + (wid & 3) * 2304;
  const char* hw = ws + WS_H2 + (size_t)rb * 36 * 1024;
  const unsigned lane16 = lane * 16;
  bf16x8 af[18][2];
  auto issue_piece = [&](auto S, auto E) { constexpr int s = S, e = E;
    constexpr int kbase = s < 16 ? 144 * (s >> 2) + 32 * (s & 3) : 288 * (s - 16) + 128;
    const char* wsrc = Ww + (size_t)kbase * (FF * 2); const unsigned m = (s % C_NSTG) * C_STG + wdst;
    if constexpr (e == 0) dma16<0>(m, s < 16 ? wsrc3[0] : wsrc3b[0], wsrc);
    if constexpr (e == 1) dma16<0>(m + 1024, s < 16 ? wsrc3[1] : wsrc3b[1], wsrc);
    if constexpr (e == 2) { if (lane < 16) dma16<0>(m + 2048, s < 16 ? wsrc3[2] : wsrc3b[2], wsrc); }
    if constexpr (e == 3) gload16c<0>(af[s][0], lane16, hw + s * 2048);
    if constexpr (e == 4) gload16c<1024>(af[s][1], lane16, hw + s * 2048); };
  static_for<3>([&](auto S) { issue_piece(S, integral_constant<int, 3>{}); issue_piece(S, integral_constant<int, 4>{}); });
  unsigned tb[C_NSTG];
#pragma unroll
  for (int i = 0; i < C_NSTG; ++i) { tb[i] = C_BASE + blk * C_UOFF + 288 * (8 * h + q) + 8 * p + i * C_STG; pin(tb[i]); }
  f32x16 acc[9] = {};
  constexpr int LA = C_LA, NIT = 18 * 18;
  bf16x8 wf[LA + 1];
  auto rd = [&](auto I) { constexpr int i = I; constexpr int s = i / 18, ks = (i % 18) / 9, f = i % 9;
    const bf16x4 lo = tr4(lds + tb[s % C_NSTG] + 288 * 16 * ks + 32 * f), hi = tr4(lds + tb[s % C_NSTG] + 288 * (16 * ks + 4) + 32 * f);
    wf[i % (LA + 1)] = cat(lo, hi); };
  SB(); vm_bar<4>(); pin(af[0][0]); pin(af[0][1]); revalidate<1024>(af[0][0], af[0][1], lane16, lane16, hw); SB();
  ST(1);
  static_for<LA>([&](auto I) { rd(I); });
  SB();
  static_for<NIT>([&](auto I) { constexpr int i = I; constexpr int s = i / 18, r = i % 18, ks = r / 9, f = r % 9;
    if constexpr (r == 0 && s > 0) {
      constexpr int outst = (s + 2 < 18) ? 5 : 0;
      vm_bar<outst>(); pin(af[s][0]); pin(af[s][1]);
      revalidate<1024>(af[s][0], af[s][1], lane16, lane16, hw + s * 2048);
      if constexpr (s == 4) ST(2);
      if constexpr (s == 8) ST(3);
      if constexpr (s == 12) ST(4);
      if constexpr (s == 16) ST(5);
      if constexpr (s == 17) proj_prefetch(ar.wd, 18, wid);
      SB();
    }
    if constexpr (i + LA < NIT) rd(integral_constant<int, i + LA>{});
    acc[f] = mfma32(wf[i % (LA + 1)], af[s][ks], acc[f]);
    if constexpr (s + 3 < 18 && (r % 3) == 1 && r / 3 < 5) issue_piece(integral_constant<int, s + 3>{}, integral_constant<int, r / 3>{});
    SB();
  });
  ST(6);
  char* ob = ws + WS_H + ((size_t)rb * 288 + ct * 9) * 1024;
#pragma unroll
  for (int f = 0; f < 9; ++f) { u32x4 w;
#pragma unroll
    for (int i = 0; i < 4; ++i) w[i] = geglu2((f32x2){acc[f][2 * i], acc[f][2 * i + 1]}, (f32x2){acc[f][8 + 2 * i], acc[f][8 + 2 * i + 1]});
    st16c(ob, lane16 + f * 1024, w); }
  signal_done(ws, CNT_H + rt * 4 + (ct >> 3), wid);
  ST_END(2);
}

__global__ __launch_bounds__(512, 2) void k_main(MainArgs ar DBG_PARAM) {
  extern __shared__ __attribute__((aligned(16))) char lds_[];
  LAS char* lds = (LAS char*)lds_;
  const int wid = rfl(threadIdx.x >> 6);
  if (wid == 7) {
    unsigned long long pc; unsigned len;
    asm volatile("s_getpc_b64 %0\n.Lkm_beg:\n\ts_mov_b32 %1, .Lkm_fin-.Lkm_beg" : "=s"(pc), "=s"(len));
    const unsigned per = (len / 128 + 31) / 32, l0 = (blockIdx.x >> 3) * per + lane_id();
    unsigned off = (lane_id() < (int)per && l0 * 128 < len) ? l0 * 128 : 0;
    dma4(A_TAB + 2048 + 7 * 256, off, (const void*)pc);
  }
  phase_attn(ar, lds, wid PH_DBG);
  phase_proj<false>(ar, lds, wid PH_DBG);
  phase_ffn1(ar, lds, wid PH_DBG);
  phase_proj<true>(ar, lds, wid PH_DBG);
  asm volatile(".Lkm_fin:");
}

extern "C" void kernel_launch(void* const* d_in, const int* in_sizes, int n_in, void* d_out, int out_size, void* d_ws, size_t ws_size, hipStream_t stream) {
  const unsigned short* x = (const unsigned short*)d_in[0];
  const unsigned short* c = (const unsigned short*)d_in[9]; const unsigned short* wmod = (const unsigned short*)d_in[10];
  char* ws = (char*)d_ws;
  (void)hipFuncSetAttribute((const void*)k_pre, hipFuncAttributeMaxDynamicSharedMemorySize, PRE_LDS);
  (void)hipFuncSetAttribute((const void*)k_main, hipFuncAttributeMaxDynamicSharedMemorySize, MAIN_LDS);
  const char* kc = (const char*)d_in[1]; const char* vc = (const char*)d_in[2];
  const char* wq = (const char*)d_in[3]; const char* wo = (const char*)d_in[4];
  const char* wg = (const char*)d_in[6]; const char* wu = (const char*)d_in[7]; const char* wd = (const char*)d_in[8];
  k_pre<<<PRE_RQ + PRE_MOD + PRE_CLR, 512, PRE_LDS, stream>>>(c, wmod, x, wq, ws DBG_ARG);
  MainArgs ar{kc, vc, wg, wu, wo, wd, x, (unsigned short*)d_out, ws};
  k_main<<<256, 512, MAIN_LDS, stream>>>(ar DBG_ARG);
}
```

```cpp
#include <hip/hip_runtime.h>
#include <stdint.h>
#include <utility>

#define DI __device__ __forceinline__
#define LAS __attribute__((address_space(3)))
#define GAS __attribute__((address_space(1)))
typedef __bf16 bf16x8 __attribute__((ext_vector_type(8)));
typedef __bf16 bf16x4 __attribute__((ext_vector_type(4)));
typedef __bf16 bf16x2 __attribute__((ext_vector_type(2)));
typedef float f32x2 __attribute__((ext_vector_type(2)));
typedef float f32x4 __attribute__((ext_vector_type(4)));
typedef float f32x16 __attribute__((ext_vector_type(16)));
typedef unsigned u32x2 __attribute__((ext_vector_type(2)));
typedef unsigned u32x4 __attribute__((ext_vector_type(4)));
using std::integral_constant;

constexpr int M_ = 2048, D_ = 576, SK = 2048, WIN = 512, FF = 4608;

constexpr size_t WS_MOD = 0;
constexpr size_t WS_STAT = 16384;
constexpr size_t WS_H1 = WS_STAT + 149760 + 3840;
constexpr size_t WS_O = WS_H1 + (size_t)64 * 36 * 1024;
constexpr size_t WS_X1 = WS_O + (size_t)64 * 256 * 1024;
constexpr size_t WS_H2 = WS_X1 + (size_t)M_ * D_ * 4;
constexpr size_t WS_H = WS_H2 + (size_t)64 * 36 * 1024;
constexpr size_t WS_WQ = WS_H + (size_t)64 * 288 * 1024;
constexpr size_t WS_BP = WS_WQ + (size_t)32 * 36 * 4 * 1024;
constexpr size_t WS_CNT = WS_BP + (size_t)18 * 4096 * 4;
constexpr size_t WS_X1R = WS_CNT + 128 * 256;
constexpr size_t WS_PB = WS_X1R + (size_t)256 * 576 * 32;
constexpr size_t WS_PD = WS_PB + (size_t)16 * 16 * 16 * 2304;
constexpr size_t WS_END = WS_PD + (size_t)16 * 16 * 16 * 2304;

template <int... Is, class F> DI void static_for_impl(std::integer_sequence<int, Is...>, F&& f) { (f(std::integral_constant<int, Is>{}), ...); }
template <int N, class F> DI void static_for(F&& f) { static_for_impl(std::make_integer_sequence<int, N>{}, f); }

DI float bflo(unsigned w) { return __uint_as_float(w << 16); }
DI float bfhi(unsigned w) { return __uint_as_float(w & 0xffff0000u); }
DI unsigned pk2(float lo, float hi) { f32x2 v = {lo, hi}; bf16x2 b = __builtin_convertvector(v, bf16x2); return __builtin_bit_cast(unsigned, b); }
DI unsigned rfl(unsigned x) { return __builtin_amdgcn_readfirstlane(x); }
DI int lane_id() { int l; asm volatile("v_mbcnt_lo_u32_b32 %0, -1, 0\n\tv_mbcnt_hi_u32_b32 %0, -1, %0" : "=v"(l)); return l; }
DI void SB() { __builtin_amdgcn_sched_barrier(0); }

template <int IMM> DI void dma16(unsigned m0v, unsigned voff, const void* sbase) {
  asm volatile("s_mov_b32 m0, %0\n\ts_nop 0\n\tglobal_load_lds_dwordx4 %1, %2 offset:%3" ::"s"(m0v), "v"(voff), "s"(sbase), "i"(IMM) : "memory");
}
DI void dma4(unsigned m0v, unsigned voff, const void* sbase) {
  asm volatile("s_mov_b32 m0, %0\n\ts_nop 0\n\tglobal_load_lds_dword %1, %2" ::"s"(m0v), "v"(voff), "s"(sbase) : "memory");
}
template <int IMM, class T> DI void gload16(T& dst, unsigned voff, const void* sbase) {
  asm volatile("global_load_dwordx4 %0, %1, %2 offset:%3" : "=v"(dst) : "v"(voff), "s"(sbase), "i"(IMM));
}
#define LDF " sc1"
#define STF " sc1"
#define SC1L_S LDF
#define SC1S 1
template <int IMM, class T> DI void gload16c(T& dst, unsigned voff, const void* sbase) {
  asm volatile("global_load_dwordx4 %0, %1, %2 offset:%3" SC1L_S : "=v"(dst) : "v"(voff), "s"(sbase), "i"(IMM));
}
DI void st16c(char* base, unsigned voff, u32x4 v) {
  asm volatile("global_store_dwordx4 %0, %1, %2" STF "\n\ts_nop 1" ::"v"(voff), "v"(v), "s"(base) : "memory");
}
template <int N> DI void wait_vm() { asm volatile("s_waitcnt vmcnt(%0)" ::"i"(N) : "memory"); }
template <int N> DI void vm_bar() { asm volatile("s_waitcnt vmcnt(%0)\n\ts_barrier" ::"i"(N) : "memory"); }
template <int N> DI void vm_lgkm_bar() { asm volatile("s_waitcnt vmcnt(%0) lgkmcnt(0)\n\ts_barrier" ::"i"(N) : "memory"); }
DI void bar() { asm volatile("s_barrier" ::: "memory"); }
template <class T> DI void pin(T& a) { asm volatile("" : "+v"(a)); }
DI bf16x4 tr4(LAS char* p) { return __builtin_amdgcn_ds_read_tr16_b64_v4bf16((LAS bf16x4*)p); }
DI bf16x8 cat(bf16x4 a, bf16x4 b) { return __builtin_shufflevector(a, b, 0, 1, 2, 3, 4, 5, 6, 7); }
DI f32x16 mfma32(bf16x8 a, bf16x8 b, f32x16 c) { return __builtin_amdgcn_mfma_f32_32x32x16_bf16(a, b, c, 0, 0, 0); }
DI f32x4 mfma16(bf16x8 a, bf16x8 b, f32x4 c) { return __builtin_amdgcn_mfma_f32_16x16x32_bf16(a, b, c, 0, 0, 0); }
DI float max3f(float a, float b, float c) { float r; asm("v_max3_f32 %0, %1, %2, %3" : "=v"(r) : "v"(a), "v"(b), "v"(c)); return r; }
DI float max2f(float a, float b) { float r; asm("v_max_f32_e32 %0, %1, %2" : "=v"(r) : "v"(a), "v"(b)); return r; }
template <int CTRL> DI float dppf(float v) { return __builtin_bit_cast(float, __builtin_amdgcn_update_dpp(0, __builtin_bit_cast(int, v), CTRL, 0xf, 0xf, true)); }
DI float sum8(float v) { v += dppf<0xB1>(v); v += dppf<0x4E>(v); v += dppf<0x141>(v); return v; }
DI f32x16 splat16(float v) { f32x16 r; for (int i = 0; i < 16; ++i) r[i] = v; return r; }

#define ST_DECL
#define ST(i)
#define ST_END(kid)
#define DBG_PARAM
#define DBG_ARG
#define PH_BEGIN
#define PH_DBG

constexpr float QSCALE = 0.08838834764831845f * 1.4426950408889634f;
constexpr int PRE_RQ = 144, PRE_MOD = 54, PRE_CLR = 32;
constexpr int PRE_LDS = 32768;
DI float silu_f(float cv) { return cv * __builtin_amdgcn_rcpf(1.f + __builtin_amdgcn_exp2f(-1.4426950408889634f * cv)); }
DI float sum_x8_16_32(float v) {
  v += dppf<0x128>(v);
  { auto rr = __builtin_amdgcn_permlane16_swap(__float_as_uint(v), __float_as_uint(v), false, false); v = __uint_as_float(rr[0]) + __uint_as_float(rr[1]); }
  { auto rr = __builtin_amdgcn_permlane32_swap(__float_as_uint(v), __float_as_uint(v), false, false); v = __uint_as_float(rr[0]) + __uint_as_float(rr[1]); }
  return v;
}
__global__ __launch_bounds__(512) void k_pre(const unsigned short* __restrict__ c, const unsigned short* __restrict__ wmod, const unsigned short* __restrict__ xin, const char* __restrict__ wq, char* __restrict__ ws DBG_PARAM) {
  ST_DECL
  extern __shared__ __attribute__((aligned(16))) char lds_[];
  LAS char* lds = (LAS char*)lds_;
  const int tid = threadIdx.x, lane = tid & 63, wid = rfl(tid >> 6);
  int b = blockIdx.x;
  if (b < PRE_MOD) {
    LAS float* sl = (LAS float*)lds; LAS float* red = (LAS float*)(lds + 2304);
    const int rg = tid >> 3, col0 = b * 64 + (tid & 7) * 8;
    u32x4 w[9]; unsigned cw;
    asm volatile("global_load_dword %0, %1, %2" : "=v"(cw) : "v"((unsigned)(tid < 288 ? tid : 0) * 4), "s"(c));
#pragma unroll
    for (int i = 0; i < 9; ++i) gload16<0>(w[i], (unsigned)((rg + 64 * i) * 3456 + col0) * 2, wmod);
    asm volatile("s_waitcnt vmcnt(9)" : "+v"(cw));
    if (tid < 288) *(LAS f32x2*)(sl + 2 * tid) = (f32x2){silu_f(bflo(cw)), silu_f(bfhi(cw))};
    asm volatile("s_waitcnt vmcnt(0) lgkmcnt(0)\n\ts_barrier" ::: "memory");
    float acc[8] = {};
#pragma unroll
    for (int i = 0; i < 9; ++i) { pin(w[i]); const float s = sl[rg + 64 * i];
#pragma unroll
      for (int j = 0; j < 4; ++j) { acc[2 * j] += s * bflo(w[i][j]); acc[2 * j + 1] += s * bfhi(w[i][j]); } }
#pragma unroll
    for (int j = 0; j < 8; ++j) acc[j] = sum_x8_16_32(acc[j]);
    if (lane < 8) { *(LAS f32x4*)(red + wid * 64 + lane * 8) = (f32x4){acc[0], acc[1], acc[2], acc[3]}; *(LAS f32x4*)(red + wid * 64 + lane * 8 + 4) = (f32x4){acc[4], acc[5], acc[6], acc[7]}; }
    asm volatile("s_waitcnt lgkmcnt(0)\n\ts_barrier" ::: "memory");
    if (tid < 64) { float t = 0.f;
#pragma unroll
      for (int w2 = 0; w2 < 8; ++w2) t += red[w2 * 64 + tid];
      ((float*)(ws + WS_MOD))[b * 64 + tid] = t; }
    ST_END(5);
    return;
  }
  b -= PRE_MOD;
  if (b < PRE_CLR) {
    const int cc = lane & 7, row = (b * 8 + wid) * 8 + (lane >> 3);
    u32x4 xv[9];
#pragma unroll
    for (int i = 0; i < 9; ++i) xv[i] = *(const u32x4*)(xin + (size_t)row * D_ + (cc + 8 * i) * 8);
    if (tid < 64) *(u32x4*)(ws + WS_CNT + (size_t)(b * 64 + tid) * 16) = (u32x4){0u, 0u, 0u, 0u};
#pragma unroll
    for (int i = 0; i < 9; ++i) st16c(ws + WS_H1, (unsigned)((b * 9 + i) * 8192 + tid * 16), (u32x4){~0u, ~0u, ~0u, ~0u});
    float sm = 0.f;
#pragma unroll
    for (int i = 0; i < 9; ++i)
#pragma unroll
      for (int j = 0; j < 4; ++j) sm += bflo(xv[i][j]) + bfhi(xv[i][j]);
    const float mean = sum8(sm) * (1.f / 576.f);
    float q = 0.f;
#pragma unroll
    for (int i = 0; i < 9; ++i)
#pragma unroll
      for (int j = 0; j < 4; ++j) { const float d0 = bflo(xv[i][j]) - mean, d1 = bfhi(xv[i][j]) - mean; q += d0 * d0 + d1 * d1; }
    const float rstd = __builtin_amdgcn_rsqf(sum8(q) * (1.f / 576.f) + 1e-5f);
    if (cc == 0) ((f32x2*)(ws + WS_STAT))[row] = (f32x2){mean, rstd};
    ST_END(6);
    return;
  }
  b -= PRE_CLR;
  {
    const int u = b * 8 + wid, s32 = u >> 6, cg = u & 63;
    { const int rr = lane >> 3, pp = lane & 7;
      const unsigned v = rr * 8192 + ((((pp >> 1) ^ ((rr >> 1) & 3)) << 1) + (pp & 1)) * 16;
      const char* src = wq + (size_t)s32 * 32 * 8192 + cg * 128; const unsigned m = wid * 4096;
      dma16<0>(m, v, src); dma16<0>(m + 1024, v, src + 8 * 8192); dma16<0>(m + 2048, v, src + 16 * 8192); dma16<0>(m + 3072, v, src + 24 * 8192); }
    wait_vm<0>();
    const int g = lane >> 4, q = (lane & 15) >> 2, p = lane & 3, h = lane >> 5, blk = g & 1, pc = ((p & 1) << 1) | (p >> 1);
    LAS char* scr = lds + wid * 4096;
#pragma unroll
    for (int dbl = 0; dbl < 2; ++dbl)
#pragma unroll
      for (int ks = 0; ks < 2; ++ks) { const int r0 = 16 * ks + 8 * h + q, r1 = r0 + 4, cx = 2 * dbl + blk;
        const bf16x8 w = cat(tr4(scr + 128 * r0 + 32 * (cx ^ ((r0 >> 1) & 3)) + 8 * pc), tr4(scr + 128 * r1 + 32 * (cx ^ ((r1 >> 1) & 3)) + 8 * pc));
        st16c(ws + WS_WQ, (unsigned)((((cg >> 1) * 36 + 2 * s32 + ks) * 4 + 2 * (cg & 1) + dbl) * 1024 + lane * 16), __builtin_bit_cast(u32x4, w)); }
    ST_END(4);
  }
}

constexpr float THR = 8.0f;
constexpr int A_TAB = 131072;
constexpr int A_LDS = 131072 + 2048;
#define A_LA 4
#define Q_LA 5
struct MainArgs { const char* kc; const char* vc; const char* wg; const char* wu; const char* wo; const char* wd; const unsigned short* x; unsigned short* out; char* ws; };
constexpr int P_STG = 9216, P_NSTG = 4, P_T = 73728;
constexpr int C_BASE = 81920;
constexpr int MAIN_LDS = 163840;
constexpr int CNT_CLEAR = 0;
constexpr int CNT_O = 8, CNT_H = 40;
DI void signal_done(char* ws, int idx, int wid) {
  vm_bar<0>();
  if (wid == 0 && lane_id() == 0) __hip_atomic_fetch_add((unsigned*)(ws + WS_CNT) + idx * 64, 1u, __ATOMIC_RELAXED, __HIP_MEMORY_SCOPE_AGENT);
}
DI void wait_count(const char* ws, int idx, unsigned target, int wid) {
  if (wid == 0) {
    const unsigned* cnt = (const unsigned*)(ws + WS_CNT) + idx * 64;
    for (unsigned spin = 0; spin < (1u << 15); ++spin) {
      if (rfl(__hip_atomic_load(cnt, __ATOMIC_RELAXED, __HIP_MEMORY_SCOPE_AGENT)) >= target) break;
      __builtin_amdgcn_s_sleep(1);
    }
  }
  bar();
}
DI void st_empty(char* base, unsigned voff) { st16c(base, voff, (u32x4){~0u, ~0u, ~0u, ~0u}); }
DI bool has_empty(const bf16x8& a, const bf16x8& b) {
  const u32x4 x = __builtin_bit_cast(u32x4, a), y = __builtin_bit_cast(u32x4, b);
  unsigned m = x[0] > x[1] ? x[0] : x[1]; m = m > x[2] ? m : x[2]; m = m > x[3] ? m : x[3]; m = m > y[0] ? m : y[0]; m = m > y[1] ? m : y[1]; m = m > y[2] ? m : y[2]; m = m > y[3] ? m : y[3];
  return __any(m == ~0u);
}
template <int IMM1> DI void revalidate(bf16x8& a, bf16x8& b, unsigned oa, unsigned ob, const char* base) {
  if (__builtin_expect(has_empty(a, b), 0)) {
    for (unsigned spin = 0; spin < (1u << 15); ++spin) {
      __builtin_amdgcn_s_sleep(3);
      asm volatile("global_load_dwordx4 %0, %2, %4 sc1\n\tglobal_load_dwordx4 %1, %3, %4 offset:%5 sc1\n\ts_waitcnt vmcnt(0)" : "+v"(a), "+v"(b) : "v"(oa), "v"(ob), "s"(base), "i"(IMM1) : "memory");
      if (!has_empty(a, b)) break;
    }
  }
}
DI void proj_prefetch(const char* W, int nst, int wid) {
  const int lane = lane_id();
  const int x = blockIdx.x & 7, j = blockIdx.x >> 3, ks = x >> 1, ct = j & 3, rg = wid & 3, kh = wid >> 2;
  unsigned wsrc3[3];
#pragma unroll
  for (int e = 0; e < 3; ++e) { const int o = 64 * e + lane, row = o / 18, ch = o - row * 18; wsrc3[e] = row * 1152 + ch * 16; }
  const char* Ww = W + (size_t)((ks * 2 + kh) * nst * 32 + rg * 8) * 1152 + ct * 288;
  const unsigned wdst = kh * (P_NSTG * P_STG) + rg * 2304;
#pragma unroll
  for (int s = 0; s < 3; ++s) { const char* wsrc = Ww + (size_t)s * 32 * 1152; const unsigned m = s * P_STG + wdst;
    dma16<0>(m, wsrc3[0], wsrc); dma16<0>(m + 1024, wsrc3[1], wsrc); if (lane < 16) dma16<0>(m + 2048, wsrc3[2], wsrc); }
}
DI void phase_attn(const MainArgs& ar, LAS char* lds, const int wid DBG_PARAM) {
  PH_BEGIN
  char* const ws = ar.ws;
  const int lane = lane_id(), tid = wid * 64 + lane;
  const int x = blockIdx.x & 7, j = blockIdx.x >> 3, head = 4 * x + (j & 3), rt = j >> 2, kvh = head >> 1;
  const int rb = rt * 8 + wid;
  const int prb = 2 * (j >> 2) + (x & 1), pown = (j & 3) * 4 + (x >> 1);
  const int h = lane >> 5, r = lane & 31, blk = (lane >> 4) & 1, q = (lane & 15) >> 2, p = lane & 3;
  const int l4 = lane >> 4, pos = lane & 15;
  unsigned ksrc2[2];
#pragma unroll
  for (int e = 0; e < 2; ++e) { const int n = 2 * wid + e, row = 4 * n + l4, ch = pos ^ ((l4 << 2) | (n & 3)); ksrc2[e] = row * 256 + ch * 16 - e * 1024; }
  const char* wq_h = ws + WS_WQ + (size_t)head * 36 * 4096 + wid * 2048;
  const char* kb_ = ar.kc + ((size_t)kvh * SK + (SK - WIN)) * 256;
  const char* vb_ = ar.vc + ((size_t)kvh * SK + (SK - WIN)) * 256;
  const char* h1w = ws + WS_H1 + (size_t)rb * 36 * 1024;
  const unsigned lane16 = lane * 16;
  auto dmaW = [&](int qq, int e) { const unsigned m = 65536 + (qq & 3) * 16384 + wid * 2048; if (e == 0) dma16<0>(m, lane16, wq_h + qq * 16384); else dma16<1024>(m, lane16, wq_h + qq * 16384); };
  auto dmaK8 = [&](int t) { const unsigned m = (t & 3) * 16384 + wid * 2048; const char* s = kb_ + t * 16384; dma16<0>(m, ksrc2[0], s); dma16<1024>(m, ksrc2[1], s); };
  auto dmaV8 = [&](int t) { const unsigned m = 65536 + ((t + 1) & 3) * 16384 + wid * 2048; const char* s = vb_ + t * 16384; dma16<0>(m, ksrc2[0], s); dma16<1024>(m, ksrc2[1], s); };
  bf16x8 g[9][4];
  auto loadG1 = [&](auto QQ, auto E) { constexpr int qq = QQ, e = E; gload16c<e * 1024>(g[qq][e], lane16, h1w + qq * 4096); };
  auto checkG = [&](auto QQ) { constexpr int qq = QQ; static_for<4>([&](auto E) { pin(g[qq][E]); });
    revalidate<1024>(g[qq][0], g[qq][1], lane16, lane16, h1w + qq * 4096); revalidate<1024>(g[qq][2], g[qq][3], lane16, lane16, h1w + qq * 4096 + 2048); };
  auto loadG = [&](auto QQ) { static_for<4>([&](auto E) { loadG1(QQ, E); }); };
  auto mark = [&](auto MI) { constexpr int mi = MI;
    if constexpr (mi < 2) { const int rown = prb * 128 + pown * 8 + (tid & 7), kc = mi == 0 ? (tid >> 3) : 64 + (lane >> 3);
      st_empty(ws + WS_H2, (unsigned)(((rown >> 5) * 36 + (kc >> 1)) * 1024 + ((kc & 1) * 32 + (rown & 31)) * 16)); }
    else { const int n = tid + 512 * (mi - 2), n2 = n < 2304 ? n : n - 2304;
      st_empty(ws + WS_PB + (size_t)prb * 16 * 16 * 2304 + (size_t)pown * 2304, (unsigned)((n < 2304 ? 0 : (unsigned)(WS_PD - WS_PB)) + (n2 / 144) * 36864 + (n2 % 144) * 16)); } };
  {
    const int row = rt * 256 + (8 * (j & 3) + x) * 8 + (tid & 7), u0 = tid >> 3, u1 = 64 + (lane >> 3);
    u32x4 xv[2]; f32x4 sa[2][2], ca[2][2]; f32x2 mr;
    asm volatile("global_load_dwordx2 %0, %1, %2" : "=v"(mr) : "v"((unsigned)row * 8), "s"(ws + WS_STAT));
    gload16<0>(xv[0], (unsigned)(row * D_ + u0 * 8) * 2, ar.x); gload16<0>(xv[1], (unsigned)(row * D_ + u1 * 8) * 2, ar.x);
#pragma unroll
    for (int i = 0; i < 2; ++i) { const unsigned o = (i ? u1 : u0) * 32; gload16<0>(sa[i][0], o, ws + WS_MOD); gload16<16>(sa[i][1], o, ws + WS_MOD); gload16<0>(ca[i][0], o, ws + WS_MOD + 2304); gload16<16>(ca[i][1], o, ws + WS_MOD + 2304); }
    dmaW(0, 0); dmaW(0, 1); dmaW(1, 0); dmaW(1, 1); dmaW(2, 0); dmaW(2, 1); dmaK8(0);
    wait_vm<8>(); pin(mr);
    const float rq = mr[1] * QSCALE;
#pragma unroll
    for (int i = 0; i < 2; ++i) { pin(xv[i]); pin(sa[i][0]); pin(sa[i][1]); pin(ca[i][0]); pin(ca[i][1]);
      const f32x4 v0 = {bflo(xv[i][0]), bfhi(xv[i][0]), bflo(xv[i][1]), bfhi(xv[i][1])}, v1 = {bflo(xv[i][2]), bfhi(xv[i][2]), bflo(xv[i][3]), bfhi(xv[i][3])};
      const f32x4 h0 = (v0 - mr[0]) * rq * (ca[i][0] + 1.f) + sa[i][0] * QSCALE, h1v = (v1 - mr[0]) * rq * (ca[i][1] + 1.f) + sa[i][1] * QSCALE; const int u = i ? u1 : u0;
      st16c(ws + WS_H1, (unsigned)(((row >> 5) * 36 + (u >> 1)) * 1024 + ((u & 1) * 32 + (row & 31)) * 16), (u32x4){pk2(h0[0], h0[1]), pk2(h0[2], h0[3]), pk2(h1v[0], h1v[1]), pk2(h1v[2], h1v[3])}); }
  }
  ST(10);
#define H1_SLEEP 16
  __builtin_amdgcn_s_sleep(H1_SLEEP);
  for (unsigned spin = 0; spin < (1u << 15); ++spin) {
    loadG(integral_constant<int, 0>{}); wait_vm<0>();
    static_for<4>([&](auto E) { pin(g[0][E]); });
    if (!has_empty(g[0][0], g[0][1]) && !has_empty(g[0][2], g[0][3])) break;
    __builtin_amdgcn_s_sleep(2);
  }
  ST(11);
  loadG(integral_constant<int, 1>{}); loadG(integral_constant<int, 2>{});
  unsigned wb0 = 65536 + lane16;
  pin(wb0);
  f32x16 acc[4] = {};
  {
    constexpr int LA = Q_LA, NIT = 9 * 16;
    bf16x8 wf[LA + 1];
    auto rd = [&](auto I) { constexpr int i = I; constexpr int qq = i / 16, ks = (i % 16) / 4, db = i % 4;
      wf[i % (LA + 1)] = *(LAS bf16x8*)(lds + wb0 + (qq & 3) * 16384 + ks * 4096 + db * 1024); };
    SB(); bar();
    SB();
    ST(1);
    static_for<LA>([&](auto I) { rd(I); });
    SB();
    static_for<NIT>([&](auto I) { constexpr int i = I; constexpr int qq = i / 16, rr = i % 16, ks = rr / 4, db = rr % 4;
      if constexpr (rr == 0 && qq > 0) {
        constexpr int NW[9] = {8, 10, 10, 10, 10, 10, 10, 8, 8};
        vm_bar<NW[qq]>();
        checkG(integral_constant<int, qq>{});
        if constexpr (qq == 4) ST(2);
        SB();
      }
      if constexpr (i + LA < NIT) rd(integral_constant<int, i + LA>{});
      acc[db] = mfma32(wf[i % (LA + 1)], g[qq][ks], acc[db]);
      if constexpr (qq + 3 < 9) {
        if constexpr (rr == 1) dmaW(qq + 3, 0);
        if constexpr (rr == 3) dmaW(qq + 3, 1);
        if constexpr (rr >= 5 && rr <= 11 && (rr & 1)) loadG1(integral_constant<int, qq + 3>{}, integral_constant<int, (rr - 5) / 2>{});
      } else {
        if constexpr (rr == 1) dmaV8(qq - 6);
        if constexpr (rr == 7) dmaK8(qq - 5);
      }
      SB();
    });
  }
  ST(3);
  bf16x8 qf[8];
#pragma unroll
  for (int db = 0; db < 4; ++db)
#pragma unroll
    for (int s = 0; s < 2; ++s) { u32x4 w;
#pragma unroll
      for (int i = 0; i < 4; ++i) w[i] = pk2(acc[db][8 * s + 2 * i], acc[db][8 * s + 2 * i + 1]);
      qf[2 * db + s] = __builtin_bit_cast(bf16x8, w); }
  const unsigned kb0 = 256 * r + 16 * (h ^ (((r & 3) << 2) | ((r >> 2) & 3)));
  const unsigned vb0 = 65536 + 256 * (4 * h + q) + 16 * ((q << 2) | ((2 * blk + (p >> 1)) ^ h)) + 8 * (p & 1);
  unsigned ka[8], va[4][2];
#pragma unroll
  for (int s = 0; s < 8; ++s) ka[s] = kb0 ^ (s << 5);
#pragma unroll
  for (int db = 0; db < 4; ++db) { va[db][0] = vb0 ^ (db << 6); va[db][1] = vb0 ^ (db << 6) ^ 32; }
  f32x16 o[4] = {}; f32x16 S[2][2], negm; unsigned pw[16]; float m; f32x2 l2 = {0.f, 0.f};
  constexpr int LA = A_LA;
  bf16x8 fr[LA + 1];
  struct Map { int g[272]; int n; };
  constexpr Map MP = []() constexpr { Map mp{}; mp.n = 0; for (int G = 0; G < 9 * 32; ++G) { const int t = G / 32 - 1, i = G % 32; if (i < 16 ? (t + 1 <= 7) : (t >= 0 && t <= 7)) mp.g[mp.n++] = G; } return mp; }();
  constexpr int NIT = MP.n;
  auto rdA = [&](auto C) { constexpr int c = C; constexpr int G = MP.g[c], t = G / 32 - 1, i = G % 32;
    if constexpr (i < 16) { constexpr int s = i >> 1, kb = i & 1; fr[c % (LA + 1)] = *(LAS bf16x8*)(lds + ka[s] + ((t + 1) & 3) * 16384 + kb * 8192); }
    else { constexpr int ii = i - 16, kk = ii >> 2, db = ii & 3; constexpr int so = ((t + 1) & 3) * 16384 + kk * 4096;
      fr[c % (LA + 1)] = cat(tr4(lds + va[db][0] + so), tr4(lds + va[db][1] + so + 2048)); } };
  auto rowmax_finish = [&](float a) { auto rr = __builtin_amdgcn_permlane32_swap(__float_as_uint(a), __float_as_uint(a), false, false); return max2f(__uint_as_float(rr[0]), __uint_as_float(rr[1])); };
  float mxa, mxb;
  const unsigned gw = blockIdx.x * 8 + wid;
  auto touch = [&](auto TI) { constexpr int ti = TI; constexpr int m = ti >> 1, half = ti & 1;
    constexpr unsigned per = m == 0 ? 9 : 11, tot = m == 0 ? 36864 : 41472;
    unsigned line = (gw * 2 + half) * per + (lane < (int)per ? lane : 0); if (line >= tot) line = tot - 1;
    dma4(A_TAB + 2048 + wid * 256, line * 128, m == 0 ? ar.wo : (m == 1 ? ar.wg : (m == 2 ? ar.wu : ar.wd))); };
  auto background = [&](auto BI) { constexpr int bi = BI;
    if constexpr (bi < 16) { if constexpr ((bi & 3) < 2) touch(integral_constant<int, (bi >> 2) * 2 + (bi & 1)>{}); else mark(integral_constant<int, (bi >> 2) * 2 + (bi & 1)>{}); }
    else mark(integral_constant<int, bi - 8>{}); };
  SB(); vm_bar<8>(); SB();
  ST(4);
  static_for<LA>([&](auto C) { rdA(C); });
  SB();
  static_for<NIT>([&](auto C) { constexpr int c = C; constexpr int G = MP.g[c], t = G / 32 - 1, i = G % 32;
    constexpr bool firstX = (i == 0) || (t == 7 && i == 16);
    if constexpr (firstX && t >= 0) {
      constexpr int NW[8] = {4, 8, 12, 12, 12, 9, 3, 0};
      float a = rowmax_finish(max2f(mxa, mxb));
      if constexpr (t == 0) {
        m = a; negm = splat16(-a);
#pragma unroll
        for (int r2 = 0; r2 < 16; ++r2) { S[0][0][r2] -= a; S[0][1][r2] -= a; }
      } else if (__builtin_expect(__any(a > THR), 0)) {
        const float dl = fmaxf(a, 0.f), f = __builtin_amdgcn_exp2f(-dl);
        m += dl; negm = splat16(-m); l2 *= f;
#pragma unroll
        for (int r2 = 0; r2 < 16; ++r2) { S[t & 1][0][r2] -= dl; S[t & 1][1][r2] -= dl; }
#pragma unroll
        for (int d = 0; d < 4; ++d)
#pragma unroll
          for (int r2 = 0; r2 < 16; ++r2) o[d][r2] *= f;
      }
      SB(); vm_bar<NW[t]>(); SB();
      if constexpr (t == 7) { if (wid == 0 && lane == 0) __hip_atomic_fetch_add((unsigned*)(ws + WS_CNT) + (CNT_CLEAR + rt) * 64, 1u, __ATOMIC_RELAXED, __HIP_MEMORY_SCOPE_AGENT); }
      if constexpr (t == 3) ST(5);
      if constexpr (t == 4) ST(7);
      if constexpr (t == 5) ST(8);
      if constexpr (t == 7) proj_prefetch(ar.wo, 16, wid);
      if constexpr (t == 7) {
#pragma unroll
        for (int e = 0; e < 32; ++e) S[1][e >> 4][e & 15] = __builtin_amdgcn_exp2f(S[1][e >> 4][e & 15]);
#pragma unroll
        for (int e = 0; e < 16; ++e) pw[e] = pk2(S[1][e >> 3][(2 * e) & 15], S[1][e >> 3][((2 * e) & 15) + 1]);
        SB();
      }
    }
    if constexpr (c + LA < NIT) rdA(integral_constant<int, c + LA>{});
    if constexpr (i < 16) {
      constexpr int s = i >> 1, kb = i & 1, X = (t + 1) & 1;
      if constexpr (s == 0) { if constexpr (t < 0) S[X][kb] = mfma32(fr[c % (LA + 1)], qf[s], f32x16{}); else S[X][kb] = mfma32(fr[c % (LA + 1)], qf[s], negm); }
      else S[X][kb] = mfma32(fr[c % (LA + 1)], qf[s], S[X][kb]);
      if constexpr (t >= 0) {
        constexpr int e0 = 2 * i, e1 = 2 * i + 1, Y = t & 1;
        S[Y][e0 >> 4][e0 & 15] = __builtin_amdgcn_exp2f(S[Y][e0 >> 4][e0 & 15]);
        S[Y][e1 >> 4][e1 & 15] = __builtin_amdgcn_exp2f(S[Y][e1 >> 4][e1 & 15]);
        if constexpr (i >= 1) { constexpr int e = i - 1; pw[e] = pk2(S[Y][e >> 3][(2 * e) & 15], S[Y][e >> 3][((2 * e) & 15) + 1]); }
        if constexpr (i == 15) { SB(); pw[15] = pk2(S[Y][1][14], S[Y][1][15]); }
        if constexpr (t <= 3 && i == 6) dma16<0>((t & 3) * 16384 + wid * 2048, ksrc2[0], kb_ + (t + 4) * 16384);
        if constexpr (t <= 3 && i == 9) dma16<1024>((t & 3) * 16384 + wid * 2048, ksrc2[1], kb_ + (t + 4) * 16384);
        if constexpr (t <= 4 && i == 12) dma16<0>(65536 + (t & 3) * 16384 + wid * 2048, ksrc2[0], vb_ + (t + 3) * 16384);
        if constexpr (t <= 4 && i == 15) dma16<1024>(65536 + (t & 3) * 16384 + wid * 2048, ksrc2[1], vb_ + (t + 3) * 16384);
      }
      if constexpr (t < 0 && i == 15) {
        asm volatile("s_nop 11" : "+v"(S[0][0]), "+v"(S[0][1]));
        mxa = max3f(S[0][0][0], S[0][0][1], S[0][0][2]); mxb = max3f(S[0][1][0], S[0][1][1], S[0][1][2]);
#pragma unroll
        for (int r2 = 3; r2 < 15; r2 += 2) { mxa = max3f(mxa, S[0][0][r2], S[0][0][r2 + 1]); mxb = max3f(mxb, S[0][1][r2], S[0][1][r2 + 1]); }
        mxa = max3f(mxa, S[0][0][15], S[0][1][15]);
      }
    } else {
      constexpr int ii = i - 16, kk = ii >> 2, db = ii & 3, Y = t & 1, X = (t + 1) & 1;
      const u32x4 w = {pw[4 * kk], pw[4 * kk + 1], pw[4 * kk + 2], pw[4 * kk + 3]};
      o[db] = mfma32(fr[c % (LA + 1)], __builtin_bit_cast(bf16x8, w), o[db]);
      if constexpr (t <= 4 && (ii & 3) == 1 && 4 * t + (ii >> 2) < 19) background(integral_constant<int, 4 * t + (ii >> 2)>{});
      l2 += (f32x2){S[Y][ii >> 3][(2 * ii) & 15], S[Y][ii >> 3][((2 * ii) & 15) + 1]};
      if constexpr (t < 7) {
        if constexpr (ii == 4) { mxa = max3f(S[X][0][0], S[X][0][1], S[X][0][2]); mxb = max3f(S[X][0][3], S[X][0][4], S[X][0][5]); }
        if constexpr (ii == 5) { mxa = max3f(mxa, S[X][0][6], S[X][0][7]); mxb = max3f(mxb, S[X][0][8], S[X][0][9]); }
        if constexpr (ii == 6) { mxa = max3f(mxa, S[X][0][10], S[X][0][11]); mxb = max3f(mxb, S[X][0][12], S[X][0][13]); }
        if constexpr (ii == 7) { mxa = max3f(mxa, S[X][0][14], S[X][0][15]); mxb = max3f(mxb, S[X][1][0], S[X][1][1]); }
        if constexpr (ii == 8) { mxa = max3f(mxa, S[X][1][2], S[X][1][3]); mxb = max3f(mxb, S[X][1][4], S[X][1][5]); }
        if constexpr (ii == 9) { mxa = max3f(mxa, S[X][1][6], S[X][1][7]); mxb = max3f(mxb, S[X][1][8], S[X][1][9]); }
        if constexpr (ii == 10) { mxa = max3f(mxa, S[X][1][10], S[X][1][11]); mxb = max3f(mxb, S[X][1][12], S[X][1][13]); }
        if constexpr (ii == 11) { mxa = max3f(mxa, S[X][1][14], S[X][1][15]); }
      }
    }
    SB();
  });
  ST(12);
  float l = l2[0] + l2[1];
  { auto rr = __builtin_amdgcn_permlane32_swap(__float_as_uint(l), __float_as_uint(l), false, false); l = __uint_as_float(rr[0]) + __uint_as_float(rr[1]); }
  const float inv = __builtin_amdgcn_rcpf(l);
  char* ob = ws + WS_O + ((size_t)rb * 256 + head * 8) * 1024;
#pragma unroll
  for (int db = 0; db < 4; ++db)
#pragma unroll
    for (int s = 0; s < 2; ++s) { u32x4 w;
#pragma unroll
      for (int i = 0; i < 4; ++i) w[i] = pk2(o[db][8 * s + 2 * i] * inv, o[db][8 * s + 2 * i + 1] * inv);
      st16c(ob, lane16 + (2 * db + s) * 1024, w); }
  signal_done(ws, CNT_O + rt * 4 + (head >> 3), wid);
  ST_END(0);
}

#define P_LA 4
DI void ffn1_prefetch(const MainArgs& ar, int wid);
template <bool FINAL>
DI void phase_proj(const MainArgs& ar, LAS char* lds, const int wid DBG_PARAM) {
  PH_BEGIN
  char* const ws = ar.ws; const char* W = FINAL ? ar.wd : ar.wo; const unsigned short* xin = ar.x; unsigned short* out = ar.out;
  constexpr int NS16 = FINAL ? 288 : 256, NST = NS16 / 16;
  const char* act = ws + (FINAL ? WS_H : WS_O);
  const float* mod = (const float*)(ws + WS_MOD);
  char* part = ws + (FINAL ? WS_PD : WS_PB);
  const int lane = lane_id(), tid = wid * 64 + lane, rg = wid & 3, kh = wid >> 2;
  const int x = blockIdx.x & 7, j = blockIdx.x >> 3, ks = x >> 1, rb = 2 * (j >> 2) + (x & 1), ct = j & 3;
  const int own = ct * 4 + ks;
  const int g = lane >> 4, m16 = lane & 15, q4 = (lane & 15) >> 2, p = lane & 3;
  const int kb32 = (ks * 2 + kh) * NST;
  unsigned aoff[2];
#pragma unroll
  for (int a = 0; a < 2; ++a) { const int row = rb * 128 + rg * 32 + a * 16 + m16; aoff[a] = (unsigned)(row >> 5) * (NS16 * 1024) + (row & 31) * 16 + (g >> 1) * 1024 + (g & 1) * 512; }
  const char* actw = act + (size_t)kb32 * 2048;
  unsigned wsrc3[3];
#pragma unroll
  for (int e = 0; e < 3; ++e) { const int o = 64 * e + lane, row = o / 18, ch = o - row * 18; wsrc3[e] = row * 1152 + ch * 16; }
  const char* Ww = W + (size_t)(kb32 * 32 + rg * 8) * 1152 + ct * 288;
  const unsigned wdst = kh * (P_NSTG * P_STG) + rg * 2304;
  bf16x8 af[NST][2];
  auto issue_piece = [&](auto S, auto E) { constexpr int s = S, e = E;
    const char* wsrc = Ww + (size_t)s * 32 * 1152; const unsigned m = (s % P_NSTG) * P_STG + wdst;
    if constexpr (e == 0) dma16<0>(m, wsrc3[0], wsrc);
    if constexpr (e == 1) dma16<0>(m + 1024, wsrc3[1], wsrc);
    if constexpr (e == 2) { if (lane < 16) dma16<0>(m + 2048, wsrc3[2], wsrc); }
    if constexpr (e == 3) gload16c<0>(af[s][0], aoff[0], actw + s * 2048);
    if constexpr (e == 4) gload16c<0>(af[s][1], aoff[1], actw + s * 2048); };
  if (!FINAL && wid != 0) { ffn1_prefetch(ar, wid); if (wid == 1) ffn1_prefetch(ar, 0); }
  wait_count(ws, (FINAL ? CNT_H : CNT_O) + (rb >> 1) * 4 + ks, 8u, wid);
  static_for<3>([&](auto S) { issue_piece(S, integral_constant<int, 3>{}); issue_piece(S, integral_constant<int, 4>{}); });
  unsigned tb[P_NSTG];
#pragma unroll
  for (int i = 0; i < P_NSTG; ++i) tb[i] = kh * (P_NSTG * P_STG) + i * P_STG + 288 * (16 * (g >> 1) + 4 * (g & 1) + q4) + 8 * p;
  f32x4 acc[2][9] = {};
  constexpr int LA = P_LA, NIT = NST * 9;
  bf16x8 wf[LA + 1];
  auto rd = [&](auto I) { constexpr int i = I; constexpr int s = i / 9, c = i % 9;
    wf[i % (LA + 1)] = cat(tr4(lds + tb[s % P_NSTG] + 32 * c), tr4(lds + tb[s % P_NSTG] + 288 * 8 + 32 * c)); };
  constexpr int NPF = 0, NTB = FINAL ? 3 : 2;
  constexpr int TB = FINAL ? 81920 : 137600;
  SB(); vm_bar<4 + NPF>(); pin(af[0][0]); pin(af[0][1]);
#pragma unroll
  for (int e = 0; e < NTB; ++e) { const int n = NTB * wid + e;
    if (!FINAL) dma16<0>(TB + n * 1024, lane * 16, n < 7 ? (const char*)(mod + 1152) + n * 1024 : (const char*)xin + (size_t)(rb * 128 + own * 8) * 1152 + (n - 7) * 1024);
    else { const int n2 = n < 21 ? n : 0; dma16<0>(TB + n2 * 1024, lane * 16, n2 < 3 ? (const char*)(mod + 2880) + n2 * 1024 : ws + WS_X1R + (size_t)blockIdx.x * 576 * 32 + (n2 - 3) * 1024); } }
  SB();
  ST(1);
  static_for<LA>([&](auto I) { rd(I); });
  SB();
  static_for<NIT>([&](auto I) { constexpr int i = I; constexpr int s = i / 9, c = i % 9;
    if constexpr (c == 0 && s > 0) {
      constexpr int outst = s == 1 ? 2 + NPF + NTB + 5 : ((s + 2 < NST) ? 5 : 0);
      vm_bar<outst>(); pin(af[s][0]); pin(af[s][1]);
      if constexpr (s == 8) ST(2);
      SB();
    }
    if constexpr (i + LA < NIT) rd(integral_constant<int, i + LA>{});
    acc[0][c] = mfma16(wf[i % (LA + 1)], af[s][0], acc[0][c]);
    acc[1][c] = mfma16(wf[i % (LA + 1)], af[s][1], acc[1][c]);
    if constexpr (s + 3 < NST && c >= 1 && c <= 5) issue_piece(integral_constant<int, s + 3>{}, integral_constant<int, c - 1>{});
    SB();
  });
  ST(5);
  const int r8 = tid & 7, rown = rb * 128 + own * 8 + r8;
  int colA[2], colB[2];
  colA[0] = 144 * (tid >> 7) + 32 * ((tid >> 5) & 3) + 4 * ((tid >> 3) & 3); colB[0] = colA[0] + 16;
  colA[1] = 288 * ((tid >> 5) & 1) + 128 + 4 * ((tid >> 3) & 3); colB[1] = colA[1] + 144;
  unsigned marked;
  if (!FINAL) asm volatile("global_load_dword %0, %1, %2 sc1" : "=v"(marked) : "v"(0u), "s"(ws + WS_CNT + (size_t)(CNT_CLEAR + (rb >> 1)) * 256));
  {
    LAS char* exw = lds + (rg * 2 + kh) * 9216 + lane * 16; LAS char* exr = lds + (rg * 2 + (kh ^ 1)) * 9216 + lane * 16;
    bar();
    ST(3);
    f32x4 fin[9];
    if (kh) {
#pragma unroll
      for (int c = 0; c < 9; ++c) { *(LAS f32x4*)(exw + c * 1024) = acc[0][c]; fin[c] = acc[1][c]; }
    } else {
#pragma unroll
      for (int c = 0; c < 9; ++c) { *(LAS f32x4*)(exw + c * 1024) = acc[1][c]; fin[c] = acc[0][c]; }
    }
    asm volatile("s_waitcnt lgkmcnt(0)\n\ts_barrier" ::: "memory");
    ST(4);
    char* pw = part + (size_t)rb * 16 * 16 * 2304 + (size_t)own * 2304;
#pragma unroll
    for (int c = 0; c < 9; ++c) fin[c] += *(LAS f32x4*)(exr + c * 1024);
    const unsigned jo = (unsigned)(rg * 4 + kh * 2 + (m16 >> 3)) * 36864, lo = g * 8 + (m16 & 7);
    u32x4 cw[4];
#pragma unroll
    for (int cp = 0; cp < 4; ++cp) cw[cp] = (u32x4){pk2(fin[2 * cp][0], fin[2 * cp][1]), pk2(fin[2 * cp][2], fin[2 * cp][3]), pk2(fin[2 * cp + 1][0], fin[2 * cp + 1][1]), pk2(fin[2 * cp + 1][2], fin[2 * cp + 1][3])};
    const u32x2 w8 = {pk2(fin[8][0], fin[8][1]), pk2(fin[8][2], fin[8][3])};
#pragma unroll
    for (int cp = 0; cp < 4; ++cp) st16c(pw, jo + (cp * 32 + lo) * 16, cw[cp]);
    asm volatile("global_store_dwordx2 %0, %1, %2" STF "\n\ts_nop 1" ::"v"(jo + 2048 + lo * 8), "v"(w8), "s"(pw) : "memory");
  }
  ST(7);
  const char* pr = part + ((size_t)rb * 16 + own) * 16 * 2304;
  const bool two = wid == 0;
  u32x4 pv[4]; u32x2 pq[4][2];
  if (!FINAL) {
    asm volatile("s_waitcnt vmcnt(5)" : "+v"(marked) :: "memory");
    if (!__all(marked >= 32u)) {
      const unsigned* cnt = (const unsigned*)(ws + WS_CNT) + (CNT_CLEAR + (rb >> 1)) * 64;
      for (unsigned spin = 0; spin < (1u << 15); ++spin) { if (__all(__hip_atomic_load(cnt, __ATOMIC_RELAXED, __HIP_MEMORY_SCOPE_AGENT) >= 32u)) break; __builtin_amdgcn_s_sleep(1); }
    }
  }
#define POLLMODE 1
#define P_SLEEP 20
  if (wid == 7) {
    for (unsigned spin = 0; spin < (1u << 15); ++spin) {
      unsigned t; asm volatile("global_load_dword %0, %1, %2 sc1\n\ts_waitcnt vmcnt(0)" : "=&v"(t) : "v"((unsigned)((lane & 15) * 2304 + 2300)), "s"(pr) : "memory");
      if (__all(t != ~0u)) break;
      __builtin_amdgcn_s_sleep(1);
    } }
  bar();
  ST(8);
  for (unsigned spin = 0; spin < (1u << 15); ++spin) {
    const unsigned o0 = (unsigned)((tid >> 7) * 4 * 2304 + (tid & 127) * 16), o1 = (unsigned)(((tid >> 5) & 1) * 8 * 2304 + 2048 + (tid & 31) * 8);
#pragma unroll
    for (int k = 0; k < 4; ++k) gload16c<0>(pv[k], o0 + k * 2304, pr);
    if (two) {
#pragma unroll
      for (int k = 0; k < 4; ++k) {
        asm volatile("global_load_dwordx2 %0, %1, %2" LDF : "=v"(pq[k][0]) : "v"(o1 + k * 2304), "s"(pr));
        asm volatile("global_load_dwordx2 %0, %1, %2" LDF : "=v"(pq[k][1]) : "v"(o1 + (4 + k) * 2304), "s"(pr)); } }
    wait_vm<0>();
    bool full = true;
#pragma unroll
    for (int k = 0; k < 4; ++k) { pin(pv[k]); full &= pv[k][0] != ~0u && pv[k][1] != ~0u && pv[k][2] != ~0u && pv[k][3] != ~0u; }
    if (two) {
#pragma unroll
      for (int k = 0; k < 4; ++k) { pin(pq[k][0]); pin(pq[k][1]); full &= pq[k][0][0] != ~0u && pq[k][0][1] != ~0u && pq[k][1][0] != ~0u && pq[k][1][1] != ~0u; } }
    if (__all(full)) break;
    __builtin_amdgcn_s_sleep(4);
  }
  ST(9);
  f32x4 v[2][2], sc[2][2], sh[2][2];
#pragma unroll
  for (int i = 0; i < 2; ++i) if (i == 0 || two) {
#pragma unroll
    for (int hh = 0; hh < 2; ++hh) { const int col = hh ? colB[i] : colA[i]; f32x4 rs;
      const f32x4 gt = *(LAS f32x4*)(lds + TB + col * 4);
      if (FINAL) rs = *(LAS f32x4*)(lds + TB + 3072 + (i ? 512 + (tid & 63) : tid) * 32 + hh * 16);
      else { const u32x2 xr = *(LAS u32x2*)(lds + TB + 7168 + r8 * 1152 + col * 2); rs = (f32x4){bflo(xr[0]), bfhi(xr[0]), bflo(xr[1]), bfhi(xr[1])};
        sh[i][hh] = *(LAS f32x4*)(lds + TB + (576 + col) * 4); sc[i][hh] = *(LAS f32x4*)(lds + TB + (1152 + col) * 4); }
      f32x4 t = {0.f, 0.f, 0.f, 0.f};
#pragma unroll
      for (int k = 0; k < 4; ++k) { const unsigned w0 = i ? pq[k][hh][0] : pv[k][2 * hh], w1 = i ? pq[k][hh][1] : pv[k][2 * hh + 1]; t += (f32x4){bflo(w0), bfhi(w0), bflo(w1), bfhi(w1)}; }
      v[i][hh] = rs + gt * t; }
  }
  ST(10);
  if (FINAL) {
#pragma unroll
    for (int i = 0; i < 2; ++i) if (i == 0 || two) {
      *(u32x2*)(out + (size_t)rown * D_ + colA[i]) = (u32x2){pk2(v[i][0][0], v[i][0][1]), pk2(v[i][0][2], v[i][0][3])};
      *(u32x2*)(out + (size_t)rown * D_ + colB[i]) = (u32x2){pk2(v[i][1][0], v[i][1][1]), pk2(v[i][1][2], v[i][1][3])}; }
  } else {
    LAS float* st = (LAS float*)(lds + P_T);
    auto s8 = [](const f32x4& a, const f32x4& b) { return ((a[0] + a[1]) + (a[2] + a[3])) + ((b[0] + b[1]) + (b[2] + b[3])); };
    float s = s8(v[0][0], v[0][1]) + (two ? s8(v[1][0], v[1][1]) : 0.f);
    s = sum_x8_16_32(s);
    if (lane < 8) st[wid * 8 + lane] = s;
    asm volatile("s_waitcnt lgkmcnt(0)\n\ts_barrier" ::: "memory");
    float tot = 0.f;
#pragma unroll
    for (int w = 0; w < 8; ++w) tot += st[w * 8 + r8];
    const float mean = tot * (1.f / 576.f);
    f32x4 d[2][2];
#pragma unroll
    for (int i = 0; i < 2; ++i) { d[i][0] = v[i][0] - mean; d[i][1] = v[i][1] - mean; }
    float qq = s8(d[0][0] * d[0][0], d[0][1] * d[0][1]) + (two ? s8(d[1][0] * d[1][0], d[1][1] * d[1][1]) : 0.f);
    qq = sum_x8_16_32(qq);
    if (lane < 8) st[64 + wid * 8 + lane] = qq;
    asm volatile("s_waitcnt lgkmcnt(0)\n\ts_barrier" ::: "memory");
    float tq = 0.f;
#pragma unroll
    for (int w = 0; w < 8; ++w) tq += st[64 + w * 8 + r8];
    const float rstd = __builtin_amdgcn_rsqf(tq * (1.f / 576.f) + 1e-5f);
#pragma unroll
    for (int i = 0; i < 2; ++i) if (i == 0 || two) {
      const f32x4 h0 = d[i][0] * rstd * (sc[i][0] + 1.f) + sh[i][0], h1 = d[i][1] * rstd * (sc[i][1] + 1.f) + sh[i][1];
      const int kc = i ? 64 + (tid >> 3) : (tid >> 3);
      st16c(ws + WS_H2, (unsigned)(((rown >> 5) * 36 + (kc >> 1)) * 1024 + ((kc & 1) * 32 + (rown & 31)) * 16), (u32x4){pk2(h0[0], h0[1]), pk2(h0[2], h0[3]), pk2(h1[0], h1[1]), pk2(h1[2], h1[3])});
      char* xb = ws + WS_X1R + (size_t)blockIdx.x * 576 * 32 + (i ? 512 + (tid & 63) : tid) * 32;
      *(f32x4*)xb = v[i][0]; *(f32x4*)(xb + 16) = v[i][1]; }
  }
  ST_END(FINAL ? 3 : 1);
}

constexpr int C_STG = 9216 + 128 + 9216, C_UOFF = 9216 + 128, C_NSTG = 4;
DI unsigned geglu2(f32x2 g, f32x2 u) {
  const float c0 = -2.f * 0.7978845608028654f * 1.4426950408889634f, c1 = c0 * 0.044715f;
  const f32x2 z = g * (g * g * c1 + c0);
  const f32x2 d = (f32x2){__builtin_amdgcn_exp2f(z[0]), __builtin_amdgcn_exp2f(z[1])} + 1.f;
  const f32x2 o = (g * u) * (f32x2){__builtin_amdgcn_rcpf(d[0]), __builtin_amdgcn_rcpf(d[1])};
  return pk2(o[0], o[1]);
}
#define C_LA 6
DI void ffn1_prefetch(const MainArgs& ar, int wid) {
  const int lane = lane_id();
  const int x = blockIdx.x & 7, j = blockIdx.x >> 3, ct = 4 * x + (j & 3);
  unsigned wsrc3[3];
#pragma unroll
  for (int e = 0; e < 3; ++e) { const int o = 64 * e + lane, row = o / 18, ch = o - row * 18; wsrc3[e] = (16 * (row >> 2) + (row & 3)) * (FF * 2) + ch * 16; }
  const char* Ww = (wid < 4 ? ar.wg : ar.wu) + (size_t)(wid & 3) * 4 * (FF * 2) + ct * 288;
  const unsigned wdst = C_BASE + (wid < 4 ? 0 : C_UOFF) + (wid & 3) * 2304;
#pragma unroll
  for (int s = 0; s < 3; ++s) { const char* wsrc = Ww + (size_t)(32 * s) * (FF * 2); const unsigned m = s * C_STG + wdst;
    dma16<0>(m, wsrc3[0], wsrc); dma16<0>(m + 1024, wsrc3[1], wsrc); if (lane < 16) dma16<0>(m + 2048, wsrc3[2], wsrc); }
}
DI void phase_ffn1(const MainArgs& ar, LAS char* lds, const int wid DBG_PARAM) {
  PH_BEGIN
  char* const ws = ar.ws; const char* wg = ar.wg; const char* wu = ar.wu;
  const int lane = lane_id(), tid = wid * 64 + lane;
  const int x = blockIdx.x & 7, j = blockIdx.x >> 3, ct = 4 * x + (j & 3), rt = j >> 2;
  const int rb = rt * 8 + wid;
  const int h = lane >> 5, blk = (lane >> 4) & 1, q = (lane & 15) >> 2, p = lane & 3;
  unsigned wsrc3[3], wsrc3b[3];
#pragma unroll
  for (int e = 0; e < 3; ++e) { const int o = 64 * e + lane, row = o / 18, ch = o - row * 18;
    wsrc3[e] = (16 * (row >> 2) + (row & 3)) * (FF * 2) + ch * 16; wsrc3b[e] = (144 * (row >> 2) + (row & 3)) * (FF * 2) + ch * 16; }
  const char* Ww = (wid < 4 ? wg : wu) + (size_t)(wid & 3) * 4 * (FF * 2) + ct * 288;
  const unsigned wdst = C_BASE + (wid < 4 ? 0 : C_UOFF) + (wid & 3) * 2304;
  const char* hw = ws + WS_H2 + (size_t)rb * 36 * 1024;
  const unsigned lane16 = lane * 16;
  bf16x8 af[18][2];
  auto issue_piece = [&](auto S, auto E) { constexpr int s = S, e = E;
    constexpr int kbase = s < 16 ? 144 * (s >> 2) + 32 * (s & 3) : 288 * (s - 16) + 128;
    const char* wsrc = Ww + (size_t)kbase * (FF * 2); const unsigned m = (s % C_NSTG) * C_STG + wdst;
    if constexpr (e == 0) dma16<0>(m, s < 16 ? wsrc3[0] : wsrc3b[0], wsrc);
    if constexpr (e == 1) dma16<0>(m + 1024, s < 16 ? wsrc3[1] : wsrc3b[1], wsrc);
    if constexpr (e == 2) { if (lane < 16) dma16<0>(m + 2048, s < 16 ? wsrc3[2] : wsrc3b[2], wsrc); }
    if constexpr (e == 3) gload16c<0>(af[s][0], lane16, hw + s * 2048);
    if constexpr (e == 4) gload16c<1024>(af[s][1], lane16, hw + s * 2048); };
  static_for<3>([&](auto S) { issue_piece(S, integral_constant<int, 3>{}); issue_piece(S, integral_constant<int, 4>{}); });
  unsigned tb[C_NSTG];
#pragma unroll
  for (int i = 0; i < C_NSTG; ++i) { tb[i] = C_BASE + blk * C_UOFF + 288 * (8 * h + q) + 8 * p + i * C_STG; pin(tb[i]); }
  f32x16 acc[9] = {};
  constexpr int LA = C_LA, NIT = 18 * 18;
  bf16x8 wf[LA + 1];
  auto rd = [&](auto I) { constexpr int i = I; constexpr int s = i / 18, ks = (i % 18) / 9, f = i % 9;
    const bf16x4 lo = tr4(lds + tb[s % C_NSTG] + 288 * 16 * ks + 32 * f), hi = tr4(lds + tb[s % C_NSTG] + 288 * (16 * ks + 4) + 32 * f);
    wf[i % (LA + 1)] = cat(lo, hi); };
  SB(); vm_bar<4>(); pin(af[0][0]); pin(af[0][1]); revalidate<1024>(af[0][0], af[0][1], lane16, lane16, hw); SB();
  ST(1);
  static_for<LA>([&](auto I) { rd(I); });
  SB();
  static_for<NIT>([&](auto I) { constexpr int i = I; constexpr int s = i / 18, r = i % 18, ks = r / 9, f = r % 9;
    if constexpr (r == 0 && s > 0) {
      constexpr int outst = (s + 2 < 18) ? 5 : 0;
      vm_bar<outst>(); pin(af[s][0]); pin(af[s][1]);
      revalidate<1024>(af[s][0], af[s][1], lane16, lane16, hw + s * 2048);
      if constexpr (s == 4) ST(2);
      if constexpr (s == 8) ST(3);
      if constexpr (s == 12) ST(4);
      if constexpr (s == 16) ST(5);
      if constexpr (s == 17) proj_prefetch(ar.wd, 18, wid);
      SB();
    }
    if constexpr (i + LA < NIT) rd(integral_constant<int, i + LA>{});
    acc[f] = mfma32(wf[i % (LA + 1)], af[s][ks], acc[f]);
    if constexpr (s + 3 < 18 && (r % 3) == 1 && r / 3 < 5) issue_piece(integral_constant<int, s + 3>{}, integral_constant<int, r / 3>{});
    SB();
  });
  ST(6);
  char* ob = ws + WS_H + ((size_t)rb * 288 + ct * 9) * 1024;
#pragma unroll
  for (int f = 0; f < 9; ++f) { u32x4 w;
#pragma unroll
    for (int i = 0; i < 4; ++i) w[i] = geglu2((f32x2){acc[f][2 * i], acc[f][2 * i + 1]}, (f32x2){acc[f][8 + 2 * i], acc[f][8 + 2 * i + 1]});
    st16c(ob, lane16 + f * 1024, w); }
  signal_done(ws, CNT_H + rt * 4 + (ct >> 3), wid);
  ST_END(2);
}

__global__ __launch_bounds__(512, 2) void k_main(MainArgs ar DBG_PARAM) {
  extern __shared__ __attribute__((aligned(16))) char lds_[];
  LAS char* lds = (LAS char*)lds_;
  const int wid = rfl(threadIdx.x >> 6);
  if (wid == 7) {
    unsigned long long pc; unsigned len;
    asm volatile("s_getpc_b64 %0\n.Lkm_beg:\n\ts_mov_b32 %1, .Lkm_fin-.Lkm_beg" : "=s"(pc), "=s"(len));
    const unsigned per = (len / 128 + 31) / 32, l0 = (blockIdx.x >> 3) * per + lane_id();
    unsigned off = (lane_id() < (int)per && l0 * 128 < len) ? l0 * 128 : 0;
    dma4(A_TAB + 2048 + 7 * 256, off, (const void*)pc);
  }
  phase_attn(ar, lds, wid PH_DBG);
  phase_proj<false>(ar, lds, wid PH_DBG);
  phase_ffn1(ar, lds, wid PH_DBG);
  phase_proj<true>(ar, lds, wid PH_DBG);
  asm volatile(".Lkm_fin:");
}

extern "C" void kernel_launch(void* const* d_in, const int* in_sizes, int n_in, void* d_out, int out_size, void* d_ws, size_t ws_size, hipStream_t stream) {
  const unsigned short* x = (const unsigned short*)d_in[0];
  const unsigned short* c = (const unsigned short*)d_in[9]; const unsigned short* wmod = (const unsigned short*)d_in[10];
  char* ws = (char*)d_ws;
  (void)hipFuncSetAttribute((const void*)k_pre, hipFuncAttributeMaxDynamicSharedMemorySize, PRE_LDS);
  (void)hipFuncSetAttribute((const void*)k_main, hipFuncAttributeMaxDynamicSharedMemorySize, MAIN_LDS);
  const char* kc = (const char*)d_in[1]; const char* vc = (const char*)d_in[2];
  const char* wq = (const char*)d_in[3]; const char* wo = (const char*)d_in[4];
  const char* wg = (const char*)d_in[6]; const char* wu = (const char*)d_in[7]; const char* wd = (const char*)d_in[8];
  k_pre<<<PRE_RQ + PRE_MOD + PRE_CLR, 512, PRE_LDS, stream>>>(c, wmod, x, wq, ws DBG_ARG);
  MainArgs ar{kc, vc, wg, wu, wo, wd, x, (unsigned short*)d_out, ws};
  k_main<<<256, 512, MAIN_LDS, stream>>>(ar DBG_ARG);
}
```

```cpp
#include <hip/hip_runtime.h>
#include <stdint.h>
#include <utility>

#define DI __device__ __forceinline__
#define LAS __attribute__((address_space(3)))
#define GAS __attribute__((address_space(1)))
typedef __bf16 bf16x8 __attribute__((ext_vector_type(8)));
typedef __bf16 bf16x4 __attribute__((ext_vector_type(4)));
typedef __bf16 bf16x2 __attribute__((ext_vector_type(2)));
typedef float f32x2 __attribute__((ext_vector_type(2)));
typedef float f32x4 __attribute__((ext_vector_type(4)));
typedef float f32x16 __attribute__((ext_vector_type(16)));
typedef unsigned u32x2 __attribute__((ext_vector_type(2)));
typedef unsigned u32x4 __attribute__((ext_vector_type(4)));
using std::integral_constant;

constexpr int M_ = 2048, D_ = 576, SK = 2048, WIN = 512, FF = 4608;

constexpr size_t WS_MOD = 0;
constexpr size_t WS_STAT = 16384;
constexpr size_t WS_H1 = WS_STAT + 149760 + 3840;
constexpr size_t WS_O = WS_H1 + (size_t)64 * 36 * 1024;
constexpr size_t WS_X1 = WS_O + (size_t)64 * 256 * 1024;
constexpr size_t WS_H2 = WS_X1 + (size_t)M_ * D_ * 4;
constexpr size_t WS_H = WS_H2 + (size_t)64 * 36 * 1024;
constexpr size_t WS_WQ = WS_H + (size_t)64 * 288 * 1024;
constexpr size_t WS_BP = WS_WQ + (size_t)32 * 36 * 4 * 1024;
constexpr size_t WS_CNT = WS_BP + (size_t)18 * 4096 * 4;
constexpr size_t WS_X1R = WS_CNT + 128 * 256;
constexpr size_t WS_PB = WS_X1R + (size_t)256 * 576 * 32;
constexpr size_t WS_PD = WS_PB + (size_t)16 * 16 * 16 * 2304;
constexpr size_t WS_END = WS_PD + (size_t)16 * 16 * 16 * 2304;

template <int... Is, class F> DI void static_for_impl(std::integer_sequence<int, Is...>, F&& f) { (f(std::integral_constant<int, Is>{}), ...); }
template <int N, class F> DI void static_for(F&& f) { static_for_impl(std::make_integer_sequence<int, N>{}, f); }

DI float bflo(unsigned w) { return __uint_as_float(w << 16); }
DI float bfhi(unsigned w) { return __uint_as_float(w & 0xffff0000u); }
DI unsigned pk2(float lo, float hi) { f32x2 v = {lo, hi}; bf16x2 b = __builtin_convertvector(v, bf16x2); return __builtin_bit_cast(unsigned, b); }
DI unsigned rfl(unsigned x) { return __builtin_amdgcn_readfirstlane(x); }
DI int lane_id() { int l; asm volatile("v_mbcnt_lo_u32_b32 %0, -1, 0\n\tv_mbcnt_hi_u32_b32 %0, -1, %0" : "=v"(l)); return l; }
DI void SB() { __builtin_amdgcn_sched_barrier(0); }

template <int IMM> DI void dma16(unsigned m0v, unsigned voff, const void* sbase) {
  asm volatile("s_mov_b32 m0, %0\n\ts_nop 0\n\tglobal_load_lds_dwordx4 %1, %2 offset:%3" ::"s"(m0v), "v"(voff), "s"(sbase), "i"(IMM) : "memory");
}
DI void dma4(unsigned m0v, unsigned voff, const void* sbase) {
  asm volatile("s_mov_b32 m0, %0\n\ts_nop 0\n\tglobal_load_lds_dword %1, %2" ::"s"(m0v), "v"(voff), "s"(sbase) : "memory");
}
template <int IMM, class T> DI void gload16(T& dst, unsigned voff, const void* sbase) {
  asm volatile("global_load_dwordx4 %0, %1, %2 offset:%3" : "=v"(dst) : "v"(voff), "s"(sbase), "i"(IMM));
}
#define LDF " sc1"
#define STF " sc1"
#define SC1L_S LDF
#define SC1S 1
template <int IMM, class T> DI void gload16c(T& dst, unsigned voff, const void* sbase) {
  asm volatile("global_load_dwordx4 %0, %1, %2 offset:%3" SC1L_S : "=v"(dst) : "v"(voff), "s"(sbase), "i"(IMM));
}
DI void st16c(char* base, unsigned voff, u32x4 v) {
  asm volatile("global_store_dwordx4 %0, %1, %2" STF "\n\ts_nop 1" ::"v"(voff), "v"(v), "s"(base) : "memory");
}
template <int N> DI void wait_vm() { asm volatile("s_waitcnt vmcnt(%0)" ::"i"(N) : "memory"); }
template <int N> DI void vm_bar() { asm volatile("s_waitcnt vmcnt(%0)\n\ts_barrier" ::"i"(N) : "memory"); }
template <int N> DI void vm_lgkm_bar() { asm volatile("s_waitcnt vmcnt(%0) lgkmcnt(0)\n\ts_barrier" ::"i"(N) : "memory"); }
DI void bar() { asm volatile("s_barrier" ::: "memory"); }
template <class T> DI void pin(T& a) { asm volatile("" : "+v"(a)); }
DI bf16x4 tr4(LAS char* p) { return __builtin_amdgcn_ds_read_tr16_b64_v4bf16((LAS bf16x4*)p); }
DI bf16x8 cat(bf16x4 a, bf16x4 b) { return __builtin_shufflevector(a, b, 0, 1, 2, 3, 4, 5, 6, 7); }
DI f32x16 mfma32(bf16x8 a, bf16x8 b, f32x16 c) { return __builtin_amdgcn_mfma_f32_32x32x16_bf16(a, b, c, 0, 0, 0); }
DI f32x4 mfma16(bf16x8 a, bf16x8 b, f32x4 c) { return __builtin_amdgcn_mfma_f32_16x16x32_bf16(a, b, c, 0, 0, 0); }
DI float max3f(float a, float b, float c) { float r; asm("v_max3_f32 %0, %1, %2, %3" : "=v"(r) : "v"(a), "v"(b), "v"(c)); return r; }
DI float max2f(float a, float b) { float r; asm("v_max_f32_e32 %0, %1, %2" : "=v"(r) : "v"(a), "v"(b)); return r; }
template <int CTRL> DI float dppf(float v) { return __builtin_bit_cast(float, __builtin_amdgcn_update_dpp(0, __builtin_bit_cast(int, v), CTRL, 0xf, 0xf, true)); }
DI float sum8(float v) { v += dppf<0xB1>(v); v += dppf<0x4E>(v); v += dppf<0x141>(v); return v; }
DI f32x16 splat16(float v) { f32x16 r; for (int i = 0; i < 16; ++i) r[i] = v; return r; }

#define ST_DECL
#define ST(i)
#define ST_END(kid)
#define DBG_PARAM
#define DBG_ARG
#define PH_BEGIN
#define PH_DBG

constexpr float QSCALE = 0.08838834764831845f * 1.4426950408889634f;
constexpr int PRE_RQ = 144, PRE_MOD = 108, PRE_CLR = 64;
constexpr int PRE_LDS = 32768;
DI float silu_f(float cv) { return cv * __builtin_amdgcn_rcpf(1.f + __builtin_amdgcn_exp2f(-1.4426950408889634f * cv)); }
DI float sum_x8_16_32(float v) {
  v += dppf<0x128>(v);
  { auto rr = __builtin_amdgcn_permlane16_swap(__float_as_uint(v), __float_as_uint(v), false, false); v = __uint_as_float(rr[0]) + __uint_as_float(rr[1]); }
  { auto rr = __builtin_amdgcn_permlane32_swap(__float_as_uint(v), __float_as_uint(v), false, false); v = __uint_as_float(rr[0]) + __uint_as_float(rr[1]); }
  return v;
}
__global__ __launch_bounds__(512) void k_pre(const unsigned short* __restrict__ c, const unsigned short* __restrict__ wmod, const unsigned short* __restrict__ xin, const char* __restrict__ wq, char* __restrict__ ws DBG_PARAM) {
  ST_DECL
  extern __shared__ __attribute__((aligned(16))) char lds_[];
  LAS char* lds = (LAS char*)lds_;
  const int tid = threadIdx.x, lane = tid & 63, wid = rfl(tid >> 6);
  int b = blockIdx.x;
  if (b < PRE_MOD) {
    LAS float* sl = (LAS float*)lds; LAS float* red = (LAS float*)(lds + 2304);
    const int rg = tid >> 2, col0 = b * 32 + (tid & 3) * 8;
    u32x4 w[5]; unsigned cw;
    asm volatile("global_load_dword %0, %1, %2" : "=v"(cw) : "v"((unsigned)(tid < 288 ? tid : 0) * 4), "s"(c));
#pragma unroll
    for (int i = 0; i < 5; ++i) { const int row = rg + 128 * i < 576 ? rg + 128 * i : rg; gload16<0>(w[i], (unsigned)(row * 3456 + col0) * 2, wmod); }
    asm volatile("s_waitcnt vmcnt(5)" : "+v"(cw));
    if (tid < 288) *(LAS f32x2*)(sl + 2 * tid) = (f32x2){silu_f(bflo(cw)), silu_f(bfhi(cw))};
    asm volatile("s_waitcnt vmcnt(0) lgkmcnt(0)\n\ts_barrier" ::: "memory");
    float acc[8] = {};
#pragma unroll
    for (int i = 0; i < 5; ++i) { pin(w[i]); const float s = rg + 128 * i < 576 ? sl[rg + 128 * i] : 0.f;
#pragma unroll
      for (int j = 0; j < 4; ++j) { acc[2 * j] += s * bflo(w[i][j]); acc[2 * j + 1] += s * bfhi(w[i][j]); } }
#pragma unroll
    for (int j = 0; j < 8; ++j) { float v = acc[j]; v += dppf<0x124>(v); acc[j] = sum_x8_16_32(v); }
    if (lane < 4) { *(LAS f32x4*)(red + wid * 32 + lane * 8) = (f32x4){acc[0], acc[1], acc[2], acc[3]}; *(LAS f32x4*)(red + wid * 32 + lane * 8 + 4) = (f32x4){acc[4], acc[5], acc[6], acc[7]}; }
    asm volatile("s_waitcnt lgkmcnt(0)\n\ts_barrier" ::: "memory");
    if (tid < 32) { float t = 0.f;
#pragma unroll
      for (int w2 = 0; w2 < 8; ++w2) t += red[w2 * 32 + tid];
      ((float*)(ws + WS_MOD))[b * 32 + tid] = t; }
    ST_END(5);
    return;
  }
  b -= PRE_MOD;
  if (b < PRE_CLR) {
    const int cc = lane & 15, row = (b * 8 + wid) * 4 + (lane >> 4);
    u32x4 xv[5];
#pragma unroll
    for (int i = 0; i < 5; ++i) xv[i] = *(const u32x4*)(xin + (size_t)row * D_ + (cc + 16 * i < 72 ? cc + 16 * i : cc) * 8);
    if (tid < 32) *(u32x4*)(ws + WS_CNT + (size_t)(b * 32 + tid) * 16) = (u32x4){0u, 0u, 0u, 0u};
#pragma unroll
    for (int i = 0; i < 5; ++i) { const unsigned o = (unsigned)(b * 36864 + i * 8192 + tid * 16); if (i < 4 || tid < 256) st16c(ws + WS_H1, o, (u32x4){~0u, ~0u, ~0u, ~0u}); }
    const bool last = cc < 8;
    float sm = 0.f;
#pragma unroll
    for (int i = 0; i < 5; ++i)
#pragma unroll
      for (int j = 0; j < 4; ++j) { const float t = bflo(xv[i][j]) + bfhi(xv[i][j]); sm += (i < 4 || last) ? t : 0.f; }
    sm = sum8(sm); sm += dppf<0x128>(sm);
    const float mean = sm * (1.f / 576.f);
    float q = 0.f;
#pragma unroll
    for (int i = 0; i < 5; ++i)
#pragma unroll
      for (int j = 0; j < 4; ++j) { const float d0 = bflo(xv[i][j]) - mean, d1 = bfhi(xv[i][j]) - mean; q += (i < 4 || last) ? d0 * d0 + d1 * d1 : 0.f; }
    q = sum8(q); q += dppf<0x128>(q);
    const float rstd = __builtin_amdgcn_rsqf(q * (1.f / 576.f) + 1e-5f);
    if (cc == 0) ((f32x2*)(ws + WS_STAT))[row] = (f32x2){mean, rstd};
    ST_END(6);
    return;
  }
  b -= PRE_CLR;
  {
    const int u = b * 8 + wid, s32 = u >> 6, cg = u & 63;
    { const int rr = lane >> 3, pp = lane & 7;
      const unsigned v = rr * 8192 + ((((pp >> 1) ^ ((rr >> 1) & 3)) << 1) + (pp & 1)) * 16;
      const char* src = wq + (size_t)s32 * 32 * 8192 + cg * 128; const unsigned m = wid * 4096;
      dma16<0>(m, v, src); dma16<0>(m + 1024, v, src + 8 * 8192); dma16<0>(m + 2048, v, src + 16 * 8192); dma16<0>(m + 3072, v, src + 24 * 8192); }
    wait_vm<0>();
    const int g = lane >> 4, q = (lane & 15) >> 2, p = lane & 3, h = lane >> 5, blk = g & 1, pc = ((p & 1) << 1) | (p >> 1);
    LAS char* scr = lds + wid * 4096;
#pragma unroll
    for (int dbl = 0; dbl < 2; ++dbl)
#pragma unroll
      for (int ks = 0; ks < 2; ++ks) { const int r0 = 16 * ks + 8 * h + q, r1 = r0 + 4, cx = 2 * dbl + blk;
        const bf16x8 w = cat(tr4(scr + 128 * r0 + 32 * (cx ^ ((r0 >> 1) & 3)) + 8 * pc), tr4(scr + 128 * r1 + 32 * (cx ^ ((r1 >> 1) & 3)) + 8 * pc));
        st16c(ws + WS_WQ, (unsigned)((((cg >> 1) * 36 + 2 * s32 + ks) * 4 + 2 * (cg & 1) + dbl) * 1024 + lane * 16), __builtin_bit_cast(u32x4, w)); }
    ST_END(4);
  }
}

constexpr float THR = 8.0f;
constexpr int A_TAB = 131072;
constexpr int A_LDS = 131072 + 2048;
#define A_LA 4
#define Q_LA 5
struct MainArgs { const char* kc; const char* vc; const char* wg; const char* wu; const char* wo; const char* wd; const unsigned short* x; unsigned short* out; char* ws; };
constexpr int P_STG = 9216, P_NSTG = 4, P_T = 73728;
constexpr int C_BASE = 81920;
constexpr int MAIN_LDS = 163840;
constexpr int CNT_CLEAR = 0;
constexpr int CNT_O = 8, CNT_H = 40;
DI void signal_done(char* ws, int idx, int wid) {
  vm_bar<0>();
  if (wid == 0 && lane_id() == 0) __hip_atomic_fetch_add((unsigned*)(ws + WS_CNT) + idx * 64, 1u, __ATOMIC_RELAXED, __HIP_MEMORY_SCOPE_AGENT);
}
DI void wait_count(const char* ws, int idx, unsigned target, int wid) {
  if (wid == 0) {
    const unsigned* cnt = (const unsigned*)(ws + WS_CNT) + idx * 64;
    for (unsigned spin = 0; spin < (1u << 15); ++spin) {
      if (rfl(__hip_atomic_load(cnt, __ATOMIC_RELAXED, __HIP_MEMORY_SCOPE_AGENT)) >= target) break;
      __builtin_amdgcn_s_sleep(1);
    }
  }
  bar();
}
DI void st_empty(char* base, unsigned voff) { st16c(base, voff, (u32x4){~0u, ~0u, ~0u, ~0u}); }
DI bool has_empty(const bf16x8& a, const bf16x8& b) {
  const u32x4 x = __builtin_bit_cast(u32x4, a), y = __builtin_bit_cast(u32x4, b);
  unsigned m = x[0] > x[1] ? x[0] : x[1]; m = m > x[2] ? m : x[2]; m = m > x[3] ? m : x[3]; m = m > y[0] ? m : y[0]; m = m > y[1] ? m : y[1]; m = m > y[2] ? m : y[2]; m = m > y[3] ? m : y[3];
  return __any(m == ~0u);
}
template <int IMM1> DI void revalidate(bf16x8& a, bf16x8& b, unsigned oa, unsigned ob, const char* base) {
  if (__builtin_expect(has_empty(a, b), 0)) {
    for (unsigned spin = 0; spin < (1u << 15); ++spin) {
      __builtin_amdgcn_s_sleep(3);
      asm volatile("global_load_dwordx4 %0, %2, %4 sc1\n\tglobal_load_dwordx4 %1, %3, %4 offset:%5 sc1\n\ts_waitcnt vmcnt(0)" : "+v"(a), "+v"(b) : "v"(oa), "v"(ob), "s"(base), "i"(IMM1) : "memory");
      if (!has_empty(a, b)) break;
    }
  }
}
DI void proj_prefetch(const char* W, int nst, int wid) {
  const int lane = lane_id();
  const int x = blockIdx.x & 7, j = blockIdx.x >> 3, ks = x >> 1, ct = j & 3, rg = wid & 3, kh = wid >> 2;
  unsigned wsrc3[3];
#pragma unroll
  for (int e = 0; e < 3; ++e) { const int o = 64 * e + lane, row = o / 18, ch = o - row * 18; wsrc3[e] = row * 1152 + ch * 16; }
  const char* Ww = W + (size_t)((ks * 2 + kh) * nst * 32 + rg * 8) * 1152 + ct * 288;
  const unsigned wdst = kh * (P_NSTG * P_STG) + rg * 2304;
#pragma unroll
  for (int s = 0; s < 3; ++s) { const char* wsrc = Ww + (size_t)s * 32 * 1152; const unsigned m = s * P_STG + wdst;
    dma16<0>(m, wsrc3[0], wsrc); dma16<0>(m + 1024, wsrc3[1], wsrc); if (lane < 16) dma16<0>(m + 2048, wsrc3[2], wsrc); }
}
DI void phase_attn(const MainArgs& ar, LAS char* lds, const int wid DBG_PARAM) {
  PH_BEGIN
  char* const ws = ar.ws;
  const int lane = lane_id(), tid = wid * 64 + lane;
  const int x = blockIdx.x & 7, j = blockIdx.x >> 3, head = 4 * x + (j & 3), rt = j >> 2, kvh = head >> 1;
  const int rb = rt * 8 + wid;
  const int prb = 2 * (j >> 2) + (x & 1), pown = (j & 3) * 4 + (x >> 1);
  const int h = lane >> 5, r = lane & 31, blk = (lane >> 4) & 1, q = (lane & 15) >> 2, p = lane & 3;
  const int l4 = lane >> 4, pos = lane & 15;
  unsigned ksrc2[2];
#pragma unroll
  for (int e = 0; e < 2; ++e) { const int n = 2 * wid + e, row = 4 * n + l4, ch = pos ^ ((l4 << 2) | (n & 3)); ksrc2[e] = row * 256 + ch * 16 - e * 1024; }
  const char* wq_h = ws + WS_WQ + (size_t)head * 36 * 4096 + wid * 2048;
  const char* kb_ = ar.kc + ((size_t)kvh * SK + (SK - WIN)) * 256;
  const char* vb_ = ar.vc + ((size_t)kvh * SK + (SK - WIN)) * 256;
  const char* h1w = ws + WS_H1 + (size_t)rb * 36 * 1024;
  const unsigned lane16 = lane * 16;
  auto dmaW = [&](int qq, int e) { const unsigned m = 65536 + (qq & 3) * 16384 + wid * 2048; if (e == 0) dma16<0>(m, lane16, wq_h + qq * 16384); else dma16<1024>(m, lane16, wq_h + qq * 16384); };
  auto dmaK8 = [&](int t) { const unsigned m = (t & 3) * 16384 + wid * 2048; const char* s = kb_ + t * 16384; dma16<0>(m, ksrc2[0], s); dma16<1024>(m, ksrc2[1], s); };
  auto dmaV8 = [&](int t) { const unsigned m = 65536 + ((t + 1) & 3) * 16384 + wid * 2048; const char* s = vb_ + t * 16384; dma16<0>(m, ksrc2[0], s); dma16<1024>(m, ksrc2[1], s); };
  bf16x8 g[9][4];
  auto loadG1 = [&](auto QQ, auto E) { constexpr int qq = QQ, e = E; gload16c<e * 1024>(g[qq][e], lane16, h1w + qq * 4096); };
  auto checkG = [&](auto QQ) { constexpr int qq = QQ; static_for<4>([&](auto E) { pin(g[qq][E]); });
    revalidate<1024>(g[qq][0], g[qq][1], lane16, lane16, h1w + qq * 4096); revalidate<1024>(g[qq][2], g[qq][3], lane16, lane16, h1w + qq * 4096 + 2048); };
  auto loadG = [&](auto QQ) { static_for<4>([&](auto E) { loadG1(QQ, E); }); };
  auto mark = [&](auto MI) { constexpr int mi = MI;
    if constexpr (mi < 2) { const int rown = prb * 128 + pown * 8 + (tid & 7), kc = mi == 0 ? (tid >> 3) : 64 + (lane >> 3);
      st_empty(ws + WS_H2, (unsigned)(((rown >> 5) * 36 + (kc >> 1)) * 1024 + ((kc & 1) * 32 + (rown & 31)) * 16)); }
    else { const int n = tid + 512 * (mi - 2);
      st_empty(ws + WS_PB + (size_t)(prb * 16 + pown) * 36864, (unsigned)(n * 16 + (n < 2304 ? 0 : (unsigned)(WS_PD - WS_PB) - 36864u))); } };
  {
    const int row = rt * 256 + (8 * (j & 3) + x) * 8 + (tid & 7), u0 = tid >> 3, u1 = 64 + (lane >> 3);
    u32x4 xv[2]; f32x4 sa[2][2], ca[2][2]; f32x2 mr;
    asm volatile("global_load_dwordx2 %0, %1, %2" : "=v"(mr) : "v"((unsigned)row * 8), "s"(ws + WS_STAT));
    gload16<0>(xv[0], (unsigned)(row * D_ + u0 * 8) * 2, ar.x); gload16<0>(xv[1], (unsigned)(row * D_ + u1 * 8) * 2, ar.x);
#pragma unroll
    for (int i = 0; i < 2; ++i) { const unsigned o = (i ? u1 : u0) * 32; gload16<0>(sa[i][0], o, ws + WS_MOD); gload16<16>(sa[i][1], o, ws + WS_MOD); gload16<0>(ca[i][0], o, ws + WS_MOD + 2304); gload16<16>(ca[i][1], o, ws + WS_MOD + 2304); }
    dmaW(0, 0); dmaW(0, 1); dmaW(1, 0); dmaW(1, 1); dmaW(2, 0); dmaW(2, 1); dmaK8(0);
    wait_vm<8>(); pin(mr);
    const float rq = mr[1] * QSCALE;
#pragma unroll
    for (int i = 0; i < 2; ++i) { pin(xv[i]); pin(sa[i][0]); pin(sa[i][1]); pin(ca[i][0]); pin(ca[i][1]);
      const f32x4 v0 = {bflo(xv[i][0]), bfhi(xv[i][0]), bflo(xv[i][1]), bfhi(xv[i][1])}, v1 = {bflo(xv[i][2]), bfhi(xv[i][2]), bflo(xv[i][3]), bfhi(xv[i][3])};
      const f32x4 h0 = (v0 - mr[0]) * rq * (ca[i][0] + 1.f) + sa[i][0] * QSCALE, h1v = (v1 - mr[0]) * rq * (ca[i][1] + 1.f) + sa[i][1] * QSCALE; const int u = i ? u1 : u0;
      st16c(ws + WS_H1, (unsigned)(((row >> 5) * 36 + (u >> 1)) * 1024 + ((u & 1) * 32 + (row & 31)) * 16), (u32x4){pk2(h0[0], h0[1]), pk2(h0[2], h0[3]), pk2(h1v[0], h1v[1]), pk2(h1v[2], h1v[3])}); }
  }
  ST(10);
#define H1_SLEEP 16
  __builtin_amdgcn_s_sleep(H1_SLEEP);
  for (unsigned spin = 0; spin < (1u << 15); ++spin) {
    loadG(integral_constant<int, 0>{}); wait_vm<0>();
    static_for<4>([&](auto E) { pin(g[0][E]); });
    if (!has_empty(g[0][0], g[0][1]) && !has_empty(g[0][2], g[0][3])) break;
    __builtin_amdgcn_s_sleep(2);
  }
  ST(11);
  loadG(integral_constant<int, 1>{}); loadG(integral_constant<int, 2>{});
  unsigned wb0 = 65536 + lane16;
  pin(wb0);
  f32x16 acc[4] = {};
  {
    constexpr int LA = Q_LA, NIT = 9 * 16;
    bf16x8 wf[LA + 1];
    auto rd = [&](auto I) { constexpr int i = I; constexpr int qq = i / 16, ks = (i % 16) / 4, db = i % 4;
      wf[i % (LA + 1)] = *(LAS bf16x8*)(lds + wb0 + (qq & 3) * 16384 + ks * 4096 + db * 1024); };
    SB(); bar();
    SB();
    ST(1);
    static_for<LA>([&](auto I) { rd(I); });
    SB();
    static_for<NIT>([&](auto I) { constexpr int i = I; constexpr int qq = i / 16, rr = i % 16, ks = rr / 4, db = rr % 4;
      if constexpr (rr == 0 && qq > 0) {
        constexpr int NW[9] = {8, 10, 10, 10, 10, 10, 10, 8, 8};
        vm_bar<NW[qq]>();
        checkG(integral_constant<int, qq>{});
        if constexpr (qq == 4) ST(2);
        SB();
      }
      if constexpr (i + LA < NIT) rd(integral_constant<int, i + LA>{});
      acc[db] = mfma32(wf[i % (LA + 1)], g[qq][ks], acc[db]);
      if constexpr (qq + 3 < 9) {
        if constexpr (rr == 1) dmaW(qq + 3, 0);
        if constexpr (rr == 3) dmaW(qq + 3, 1);
        if constexpr (rr >= 5 && rr <= 11 && (rr & 1)) loadG1(integral_constant<int, qq + 3>{}, integral_constant<int, (rr - 5) / 2>{});
      } else {
        if constexpr (rr == 1) dmaV8(qq - 6);
        if constexpr (rr == 7) dmaK8(qq - 5);
      }
      SB();
    });
  }
  ST(3);
  bf16x8 qf[8];
#pragma unroll
  for (int db = 0; db < 4; ++db)
#pragma unroll
    for (int s = 0; s < 2; ++s) { u32x4 w;
#pragma unroll
      for (int i = 0; i < 4; ++i) w[i] = pk2(acc[db][8 * s + 2 * i], acc[db][8 * s + 2 * i + 1]);
      qf[2 * db + s] = __builtin_bit_cast(bf16x8, w); }
  const unsigned kb0 = 256 * r + 16 * (h ^ (((r & 3) << 2) | ((r >> 2) & 3)));
  const unsigned vb0 = 65536 + 256 * (4 * h + q) + 16 * ((q << 2) | ((2 * blk + (p >> 1)) ^ h)) + 8 * (p & 1);
  unsigned ka[8], va[4][2];
#pragma unroll
  for (int s = 0; s < 8; ++s) ka[s] = kb0 ^ (s << 5);
#pragma unroll
  for (int db = 0; db < 4; ++db) { va[db][0] = vb0 ^ (db << 6); va[db][1] = vb0 ^ (db << 6) ^ 32; }
  f32x16 o[4] = {}; f32x16 S[2][2], negm; unsigned pw[16]; float m; f32x2 l2 = {0.f, 0.f};
  constexpr int LA = A_LA;
  bf16x8 fr[LA + 1];
  struct Map { int g[272]; int n; };
  constexpr Map MP = []() constexpr { Map mp{}; mp.n = 0; for (int G = 0; G < 9 * 32; ++G) { const int t = G / 32 - 1, i = G % 32; if (i < 16 ? (t + 1 <= 7) : (t >= 0 && t <= 7)) mp.g[mp.n++] = G; } return mp; }();
  constexpr int NIT = MP.n;
  auto rdA = [&](auto C) { constexpr int c = C; constexpr int G = MP.g[c], t = G / 32 - 1, i = G % 32;
    if constexpr (i < 16) { constexpr int s = i >> 1, kb = i & 1; fr[c % (LA + 1)] = *(LAS bf16x8*)(lds + ka[s] + ((t + 1) & 3) * 16384 + kb * 8192); }
    else { constexpr int ii = i - 16, kk = ii >> 2, db = ii & 3; constexpr int so = ((t + 1) & 3) * 16384 + kk * 4096;
      fr[c % (LA + 1)] = cat(tr4(lds + va[db][0] + so), tr4(lds + va[db][1] + so + 2048)); } };
  auto rowmax_finish = [&](float a) { auto rr = __builtin_amdgcn_permlane32_swap(__float_as_uint(a), __float_as_uint(a), false, false); return max2f(__uint_as_float(rr[0]), __uint_as_float(rr[1])); };
  float mxa, mxb;
  const unsigned gw = blockIdx.x * 8 + wid;
  auto touch = [&](auto TI) { constexpr int ti = TI; constexpr int m = ti >> 1, half = ti & 1;
    constexpr unsigned per = m == 0 ? 9 : 11, tot = m == 0 ? 36864 : 41472;
    unsigned line = (gw * 2 + half) * per + (lane < (int)per ? lane : 0); if (line >= tot) line = tot - 1;
    dma4(A_TAB + 2048 + wid * 256, line * 128, m == 0 ? ar.wo : (m == 1 ? ar.wg : (m == 2 ? ar.wu : ar.wd))); };
  auto background = [&](auto BI) { constexpr int bi = BI;
    if constexpr (bi < 16) { if constexpr ((bi & 3) < 2) touch(integral_constant<int, (bi >> 2) * 2 + (bi & 1)>{}); else mark(integral_constant<int, (bi >> 2) * 2 + (bi & 1)>{}); }
    else mark(integral_constant<int, bi - 8>{}); };
  SB(); vm_bar<8>(); SB();
  ST(4);
  static_for<LA>([&](auto C) { rdA(C); });
  SB();
  static_for<NIT>([&](auto C) { constexpr int c = C; constexpr int G = MP.g[c], t = G / 32 - 1, i = G % 32;
    constexpr bool firstX = (i == 0) || (t == 7 && i == 16);
    if constexpr (firstX && t >= 0) {
      constexpr int NW[8] = {4, 8, 12, 12, 12, 9, 3, 0};
      float a = rowmax_finish(max2f(mxa, mxb));
      if constexpr (t == 0) {
        m = a; negm = splat16(-a);
#pragma unroll
        for (int r2 = 0; r2 < 16; ++r2) { S[0][0][r2] -= a; S[0][1][r2] -= a; }
      } else if (__builtin_expect(__any(a > THR), 0)) {
        const float dl = fmaxf(a, 0.f), f = __builtin_amdgcn_exp2f(-dl);
        m += dl; negm = splat16(-m); l2 *= f;
#pragma unroll
        for (int r2 = 0; r2 < 16; ++r2) { S[t & 1][0][r2] -= dl; S[t & 1][1][r2] -= dl; }
#pragma unroll
        for (int d = 0; d < 4; ++d)
#pragma unroll
          for (int r2 = 0; r2 < 16; ++r2) o[d][r2] *= f;
      }
      SB(); vm_bar<NW[t]>(); SB();
      if constexpr (t == 7) { if (wid == 0 && lane == 0) __hip_atomic_fetch_add((unsigned*)(ws + WS_CNT) + (CNT_CLEAR + rt) * 64, 1u, __ATOMIC_RELAXED, __HIP_MEMORY_SCOPE_AGENT); }
      if constexpr (t == 3) ST(5);
      if constexpr (t == 4) ST(7);
      if constexpr (t == 5) ST(8);
      if constexpr (t == 7) proj_prefetch(ar.wo, 16, wid);
      if constexpr (t == 7) {
#pragma unroll
        for (int e = 0; e < 32; ++e) S[1][e >> 4][e & 15] = __builtin_amdgcn_exp2f(S[1][e >> 4][e & 15]);
#pragma unroll
        for (int e = 0; e < 16; ++e) pw[e] = pk2(S[1][e >> 3][(2 * e) & 15], S[1][e >> 3][((2 * e) & 15) + 1]);
        SB();
      }
    }
    if constexpr (c + LA < NIT) rdA(integral_constant<int, c + LA>{});
    if constexpr (i < 16) {
      constexpr int s = i >> 1, kb = i & 1, X = (t + 1) & 1;
      if constexpr (s == 0) { if constexpr (t < 0) S[X][kb] = mfma32(fr[c % (LA + 1)], qf[s], f32x16{}); else S[X][kb] = mfma32(fr[c % (LA + 1)], qf[s], negm); }
      else S[X][kb] = mfma32(fr[c % (LA + 1)], qf[s], S[X][kb]);
      if constexpr (t >= 0) {
        constexpr int e0 = 2 * i, e1 = 2 * i + 1, Y = t & 1;
        S[Y][e0 >> 4][e0 & 15] = __builtin_amdgcn_exp2f(S[Y][e0 >> 4][e0 & 15]);
        S[Y][e1 >> 4][e1 & 15] = __builtin_amdgcn_exp2f(S[Y][e1 >> 4][e1 & 15]);
        if constexpr (i >= 1) { constexpr int e = i - 1; pw[e] = pk2(S[Y][e >> 3][(2 * e) & 15], S[Y][e >> 3][((2 * e) & 15) + 1]); }
        if constexpr (i == 15) { SB(); pw[15] = pk2(S[Y][1][14], S[Y][1][15]); }
        if constexpr (t <= 3 && i == 6) dma16<0>((t & 3) * 16384 + wid * 2048, ksrc2[0], kb_ + (t + 4) * 16384);
        if constexpr (t <= 3 && i == 9) dma16<1024>((t & 3) * 16384 + wid * 2048, ksrc2[1], kb_ + (t + 4) * 16384);
        if constexpr (t <= 4 && i == 12) dma16<0>(65536 + (t & 3) * 16384 + wid * 2048, ksrc2[0], vb_ + (t + 3) * 16384);
        if constexpr (t <= 4 && i == 15) dma16<1024>(65536 + (t & 3) * 16384 + wid * 2048, ksrc2[1], vb_ + (t + 3) * 16384);
      }
      if constexpr (t < 0 && i == 15) {
        asm volatile("s_nop 11" : "+v"(S[0][0]), "+v"(S[0][1]));
        mxa = max3f(S[0][0][0], S[0][0][1], S[0][0][2]); mxb = max3f(S[0][1][0], S[0][1][1], S[0][1][2]);
#pragma unroll
        for (int r2 = 3; r2 < 15; r2 += 2) { mxa = max3f(mxa, S[0][0][r2], S[0][0][r2 + 1]); mxb = max3f(mxb, S[0][1][r2], S[0][1][r2 + 1]); }
        mxa = max3f(mxa, S[0][0][15], S[0][1][15]);
      }
    } else {
      constexpr int ii = i - 16, kk = ii >> 2, db = ii & 3, Y = t & 1, X = (t + 1) & 1;
      const u32x4 w = {pw[4 * kk], pw[4 * kk + 1], pw[4 * kk + 2], pw[4 * kk + 3]};
      o[db] = mfma32(fr[c % (LA + 1)], __builtin_bit_cast(bf16x8, w), o[db]);
      if constexpr (t <= 4 && (ii & 3) == 1 && 4 * t + (ii >> 2) < 19) background(integral_constant<int, 4 * t + (ii >> 2)>{});
      l2 += (f32x2){S[Y][ii >> 3][(2 * ii) & 15], S[Y][ii >> 3][((2 * ii) & 15) + 1]};
      if constexpr (t < 7) {
        if constexpr (ii == 4) { mxa = max3f(S[X][0][0], S[X][0][1], S[X][0][2]); mxb = max3f(S[X][0][3], S[X][0][4], S[X][0][5]); }
        if constexpr (ii == 5) { mxa = max3f(mxa, S[X][0][6], S[X][0][7]); mxb = max3f(mxb, S[X][0][8], S[X][0][9]); }
        if constexpr (ii == 6) { mxa = max3f(mxa, S[X][0][10], S[X][0][11]); mxb = max3f(mxb, S[X][0][12], S[X][0][13]); }
        if constexpr (ii == 7) { mxa = max3f(mxa, S[X][0][14], S[X][0][15]); mxb = max3f(mxb, S[X][1][0], S[X][1][1]); }
        if constexpr (ii == 8) { mxa = max3f(mxa, S[X][1][2], S[X][1][3]); mxb = max3f(mxb, S[X][1][4], S[X][1][5]); }
        if constexpr (ii == 9) { mxa = max3f(mxa, S[X][1][6], S[X][1][7]); mxb = max3f(mxb, S[X][1][8], S[X][1][9]); }
        if constexpr (ii == 10) { mxa = max3f(mxa, S[X][1][10], S[X][1][11]); mxb = max3f(mxb, S[X][1][12], S[X][1][13]); }
        if constexpr (ii == 11) { mxa = max3f(mxa, S[X][1][14], S[X][1][15]); }
      }
    }
    SB();
  });
  ST(12);
  float l = l2[0] + l2[1];
  { auto rr = __builtin_amdgcn_permlane32_swap(__float_as_uint(l), __float_as_uint(l), false, false); l = __uint_as_float(rr[0]) + __uint_as_float(rr[1]); }
  const float inv = __builtin_amdgcn_rcpf(l);
  char* ob = ws + WS_O + ((size_t)rb * 256 + head * 8) * 1024;
#pragma unroll
  for (int db = 0; db < 4; ++db)
#pragma unroll
    for (int s = 0; s < 2; ++s) { u32x4 w;
#pragma unroll
      for (int i = 0; i < 4; ++i) w[i] = pk2(o[db][8 * s + 2 * i] * inv, o[db][8 * s + 2 * i + 1] * inv);
      st16c(ob, lane16 + (2 * db + s) * 1024, w); }
  signal_done(ws, CNT_O + rt * 4 + (head >> 3), wid);
  ST_END(0);
}

#define P_LA 4
DI void ffn1_prefetch(const MainArgs& ar, int wid);
template <bool FINAL>
DI void phase_proj(const MainArgs& ar, LAS char* lds, const int wid DBG_PARAM) {
  PH_BEGIN
  char* const ws = ar.ws; const char* W = FINAL ? ar.wd : ar.wo; const unsigned short* xin = ar.x; unsigned short* out = ar.out;
  constexpr int NS16 = FINAL ? 288 : 256, NST = NS16 / 16;
  const char* act = ws + (FINAL ? WS_H : WS_O);
  const float* mod = (const float*)(ws + WS_MOD);
  char* part = ws + (FINAL ? WS_PD : WS_PB);
  const int lane = lane_id(), tid = wid * 64 + lane, rg = wid & 3, kh = wid >> 2;
  const int x = blockIdx.x & 7, j = blockIdx.x >> 3, ks = x >> 1, rb = 2 * (j >> 2) + (x & 1), ct = j & 3;
  const int own = ct * 4 + ks;
  const int g = lane >> 4, m16 = lane & 15, q4 = (lane & 15) >> 2, p = lane & 3;
  const int kb32 = (ks * 2 + kh) * NST;
  unsigned aoff[2];
#pragma unroll
  for (int a = 0; a < 2; ++a) { const int row = rb * 128 + rg * 32 + a * 16 + m16; aoff[a] = (unsigned)(row >> 5) * (NS16 * 1024) + (row & 31) * 16 + (g >> 1) * 1024 + (g & 1) * 512; }
  const char* actw = act + (size_t)kb32 * 2048;
  unsigned wsrc3[3];
#pragma unroll
  for (int e = 0; e < 3; ++e) { const int o = 64 * e + lane, row = o / 18, ch = o - row * 18; wsrc3[e] = row * 1152 + ch * 16; }
  const char* Ww = W + (size_t)(kb32 * 32 + rg * 8) * 1152 + ct * 288;
  const unsigned wdst = kh * (P_NSTG * P_STG) + rg * 2304;
  bf16x8 af[NST][2];
  auto issue_piece = [&](auto S, auto E) { constexpr int s = S, e = E;
    const char* wsrc = Ww + (size_t)s * 32 * 1152; const unsigned m = (s % P_NSTG) * P_STG + wdst;
    if constexpr (e == 0) dma16<0>(m, wsrc3[0], wsrc);
    if constexpr (e == 1) dma16<0>(m + 1024, wsrc3[1], wsrc);
    if constexpr (e == 2) { if (lane < 16) dma16<0>(m + 2048, wsrc3[2], wsrc); }
    if constexpr (e == 3) gload16c<0>(af[s][0], aoff[0], actw + s * 2048);
    if constexpr (e == 4) gload16c<0>(af[s][1], aoff[1], actw + s * 2048); };
  if (!FINAL && wid != 0) { ffn1_prefetch(ar, wid); if (wid == 1) ffn1_prefetch(ar, 0); }
  wait_count(ws, (FINAL ? CNT_H : CNT_O) + (rb >> 1) * 4 + ks, 8u, wid);
  static_for<3>([&](auto S) { issue_piece(S, integral_constant<int, 3>{}); issue_piece(S, integral_constant<int, 4>{}); });
  unsigned tb[P_NSTG];
#pragma unroll
  for (int i = 0; i < P_NSTG; ++i) tb[i] = kh * (P_NSTG * P_STG) + i * P_STG + 288 * (16 * (g >> 1) + 4 * (g & 1) + q4) + 8 * p;
  f32x4 acc[2][9] = {};
  constexpr int LA = P_LA, NIT = NST * 9;
  bf16x8 wf[LA + 1];
  auto rd = [&](auto I) { constexpr int i = I; constexpr int s = i / 9, c = i % 9;
    wf[i % (LA + 1)] = cat(tr4(lds + tb[s % P_NSTG] + 32 * c), tr4(lds + tb[s % P_NSTG] + 288 * 8 + 32 * c)); };
  constexpr int NPF = 0, NTB = FINAL ? 3 : 2;
  constexpr int TB = FINAL ? 81920 : 137600;
  SB(); vm_bar<4 + NPF>(); pin(af[0][0]); pin(af[0][1]);
#pragma unroll
  for (int e = 0; e < NTB; ++e) { const int n = NTB * wid + e;
    if (!FINAL) dma16<0>(TB + n * 1024, lane * 16, n < 7 ? (const char*)(mod + 1152) + n * 1024 : (const char*)xin + (size_t)(rb * 128 + own * 8) * 1152 + (n - 7) * 1024);
    else { const int n2 = n < 21 ? n : 0; dma16<0>(TB + n2 * 1024, lane * 16, n2 < 3 ? (const char*)(mod + 2880) + n2 * 1024 : ws + WS_X1R + (size_t)blockIdx.x * 576 * 32 + (n2 - 3) * 1024); } }
  SB();
  ST(1);
  static_for<LA>([&](auto I) { rd(I); });
  SB();
  static_for<NIT>([&](auto I) { constexpr int i = I; constexpr int s = i / 9, c = i % 9;
    if constexpr (c == 0 && s > 0) {
      constexpr int outst = s == 1 ? 2 + NPF + NTB + 5 : ((s + 2 < NST) ? 5 : 0);
      vm_bar<outst>(); pin(af[s][0]); pin(af[s][1]);
      if constexpr (s == 8) ST(2);
      SB();
    }
    if constexpr (i + LA < NIT) rd(integral_constant<int, i + LA>{});
    acc[0][c] = mfma16(wf[i % (LA + 1)], af[s][0], acc[0][c]);
    acc[1][c] = mfma16(wf[i % (LA + 1)], af[s][1], acc[1][c]);
    if constexpr (s + 3 < NST && c >= 1 && c <= 5) issue_piece(integral_constant<int, s + 3>{}, integral_constant<int, c - 1>{});
    SB();
  });
  ST(5);
  const int r8 = tid & 7, rown = rb * 128 + own * 8 + r8;
  int colA[2], colB[2];
  colA[0] = 144 * (tid >> 7) + 32 * ((tid >> 5) & 3) + 4 * ((tid >> 3) & 3); colB[0] = colA[0] + 16;
  colA[1] = 288 * ((tid >> 5) & 1) + 128 + 4 * ((tid >> 3) & 3); colB[1] = colA[1] + 144;
  unsigned marked;
  if (!FINAL) asm volatile("global_load_dword %0, %1, %2 sc1" : "=v"(marked) : "v"(0u), "s"(ws + WS_CNT + (size_t)(CNT_CLEAR + (rb >> 1)) * 256));
  {
    LAS char* exw = lds + (rg * 2 + kh) * 9216 + lane * 16; LAS char* exr = lds + (rg * 2 + (kh ^ 1)) * 9216 + lane * 16;
    bar();
    ST(3);
    f32x4 fin[9];
    if (kh) {
#pragma unroll
      for (int c = 0; c < 9; ++c) { *(LAS f32x4*)(exw + c * 1024) = acc[0][c]; fin[c] = acc[1][c]; }
    } else {
#pragma unroll
      for (int c = 0; c < 9; ++c) { *(LAS f32x4*)(exw + c * 1024) = acc[1][c]; fin[c] = acc[0][c]; }
    }
    asm volatile("s_waitcnt lgkmcnt(0)\n\ts_barrier" ::: "memory");
    ST(4);
    char* pw = part + (size_t)(rb * 16 + own) * 36864;
#pragma unroll
    for (int c = 0; c < 9; ++c) fin[c] += *(LAS f32x4*)(exr + c * 1024);
    const unsigned jo = (unsigned)(rg * 4 + kh * 2 + (m16 >> 3)) * 2304, lo = g * 8 + (m16 & 7);
    u32x4 cw[4];
#pragma unroll
    for (int cp = 0; cp < 4; ++cp) cw[cp] = (u32x4){pk2(fin[2 * cp][0], fin[2 * cp][1]), pk2(fin[2 * cp][2], fin[2 * cp][3]), pk2(fin[2 * cp + 1][0], fin[2 * cp + 1][1]), pk2(fin[2 * cp + 1][2], fin[2 * cp + 1][3])};
    const u32x2 w8 = {pk2(fin[8][0], fin[8][1]), pk2(fin[8][2], fin[8][3])};
#pragma unroll
    for (int cp = 0; cp < 4; ++cp) st16c(pw, jo + (cp * 32 + lo) * 16, cw[cp]);
    asm volatile("global_store_dwordx2 %0, %1, %2" STF "\n\ts_nop 1" ::"v"(jo + 2048 + lo * 8), "v"(w8), "s"(pw) : "memory");
  }
  ST(7);
  const char* pr = part + (size_t)rb * 16 * 36864 + own * 2304;
  const bool two = wid == 0;
  u32x4 pv[4]; u32x2 pq[4][2];
  if (!FINAL) {
    asm volatile("s_waitcnt vmcnt(5)" : "+v"(marked) :: "memory");
    if (!__all(marked >= 32u)) {
      const unsigned* cnt = (const unsigned*)(ws + WS_CNT) + (CNT_CLEAR + (rb >> 1)) * 64;
      for (unsigned spin = 0; spin < (1u << 15); ++spin) { if (__all(__hip_atomic_load(cnt, __ATOMIC_RELAXED, __HIP_MEMORY_SCOPE_AGENT) >= 32u)) break; __builtin_amdgcn_s_sleep(1); }
    }
  }
#define POLLMODE 1
#define P_SLEEP 20
  if (wid == 7) {
    for (unsigned spin = 0; spin < (1u << 15); ++spin) {
      unsigned t; asm volatile("global_load_dword %0, %1, %2 sc1\n\ts_waitcnt vmcnt(0)" : "=&v"(t) : "v"((unsigned)((lane & 15) * 36864 + 2300)), "s"(pr) : "memory");
      if (__all(t != ~0u)) break;
      __builtin_amdgcn_s_sleep(1);
    } }
  bar();
  ST(8);
  for (unsigned spin = 0; spin < (1u << 15); ++spin) {
    const unsigned o0 = (unsigned)((tid >> 7) * 4 * 36864 + (tid & 127) * 16), o1 = (unsigned)(((tid >> 5) & 1) * 8 * 36864 + 2048 + (tid & 31) * 8);
#pragma unroll
    for (int k = 0; k < 4; ++k) gload16c<0>(pv[k], o0 + k * 36864, pr);
    if (two) {
#pragma unroll
      for (int k = 0; k < 4; ++k) {
        asm volatile("global_load_dwordx2 %0, %1, %2" LDF : "=v"(pq[k][0]) : "v"(o1 + k * 36864), "s"(pr));
        asm volatile("global_load_dwordx2 %0, %1, %2" LDF : "=v"(pq[k][1]) : "v"(o1 + (4 + k) * 36864), "s"(pr)); } }
    wait_vm<0>();
    bool full = true;
#pragma unroll
    for (int k = 0; k < 4; ++k) { pin(pv[k]); full &= pv[k][0] != ~0u && pv[k][1] != ~0u && pv[k][2] != ~0u && pv[k][3] != ~0u; }
    if (two) {
#pragma unroll
      for (int k = 0; k < 4; ++k) { pin(pq[k][0]); pin(pq[k][1]); full &= pq[k][0][0] != ~0u && pq[k][0][1] != ~0u && pq[k][1][0] != ~0u && pq[k][1][1] != ~0u; } }
    if (__all(full)) break;
    __builtin_amdgcn_s_sleep(4);
  }
  ST(9);
  f32x4 v[2][2], sc[2][2], sh[2][2];
#pragma unroll
  for (int i = 0; i < 2; ++i) if (i == 0 || two) {
#pragma unroll
    for (int hh = 0; hh < 2; ++hh) { const int col = hh ? colB[i] : colA[i]; f32x4 rs;
      const f32x4 gt = *(LAS f32x4*)(lds + TB + col * 4);
      if (FINAL) rs = *(LAS f32x4*)(lds + TB + 3072 + (i ? 512 + (tid & 63) : tid) * 32 + hh * 16);
      else { const u32x2 xr = *(LAS u32x2*)(lds + TB + 7168 + r8 * 1152 + col * 2); rs = (f32x4){bflo(xr[0]), bfhi(xr[0]), bflo(xr[1]), bfhi(xr[1])};
        sh[i][hh] = *(LAS f32x4*)(lds + TB + (576 + col) * 4); sc[i][hh] = *(LAS f32x4*)(lds + TB + (1152 + col) * 4); }
      f32x4 t = {0.f, 0.f, 0.f, 0.f};
#pragma unroll
      for (int k = 0; k < 4; ++k) { const unsigned w0 = i ? pq[k][hh][0] : pv[k][2 * hh], w1 = i ? pq[k][hh][1] : pv[k][2 * hh + 1]; t += (f32x4){bflo(w0), bfhi(w0), bflo(w1), bfhi(w1)}; }
      v[i][hh] = rs + gt * t; }
  }
  ST(10);
  if (FINAL) {
#pragma unroll
    for (int i = 0; i < 2; ++i) if (i == 0 || two) {
      *(u32x2*)(out + (size_t)rown * D_ + colA[i]) = (u32x2){pk2(v[i][0][0], v[i][0][1]), pk2(v[i][0][2], v[i][0][3])};
      *(u32x2*)(out + (size_t)rown * D_ + colB[i]) = (u32x2){pk2(v[i][1][0], v[i][1][1]), pk2(v[i][1][2], v[i][1][3])}; }
  } else {
    LAS float* st = (LAS float*)(lds + P_T);
    auto s8 = [](const f32x4& a, const f32x4& b) { return ((a[0] + a[1]) + (a[2] + a[3])) + ((b[0] + b[1]) + (b[2] + b[3])); };
    float s = s8(v[0][0], v[0][1]) + (two ? s8(v[1][0], v[1][1]) : 0.f);
    s = sum_x8_16_32(s);
    if (lane < 8) st[wid * 8 + lane] = s;
    asm volatile("s_waitcnt lgkmcnt(0)\n\ts_barrier" ::: "memory");
    float tot = 0.f;
#pragma unroll
    for (int w = 0; w < 8; ++w) tot += st[w * 8 + r8];
    const float mean = tot * (1.f / 576.f);
    f32x4 d[2][2];
#pragma unroll
    for (int i = 0; i < 2; ++i) { d[i][0] = v[i][0] - mean; d[i][1] = v[i][1] - mean; }
    float qq = s8(d[0][0] * d[0][0], d[0][1] * d[0][1]) + (two ? s8(d[1][0] * d[1][0], d[1][1] * d[1][1]) : 0.f);
    qq = sum_x8_16_32(qq);
    if (lane < 8) st[64 + wid * 8 + lane] = qq;
    asm volatile("s_waitcnt lgkmcnt(0)\n\ts_barrier" ::: "memory");
    float tq = 0.f;
#pragma unroll
    for (int w = 0; w < 8; ++w) tq += st[64 + w * 8 + r8];
    const float rstd = __builtin_amdgcn_rsqf(tq * (1.f / 576.f) + 1e-5f);
#pragma unroll
    for (int i = 0; i < 2; ++i) if (i == 0 || two) {
      const f32x4 h0 = d[i][0] * rstd * (sc[i][0] + 1.f) + sh[i][0], h1 = d[i][1] * rstd * (sc[i][1] + 1.f) + sh[i][1];
      const int kc = i ? 64 + (tid >> 3) : (tid >> 3);
      st16c(ws + WS_H2, (unsigned)(((rown >> 5) * 36 + (kc >> 1)) * 1024 + ((kc & 1) * 32 + (rown & 31)) * 16), (u32x4){pk2(h0[0], h0[1]), pk2(h0[2], h0[3]), pk2(h1[0], h1[1]), pk2(h1[2], h1[3])});
      char* xb = ws + WS_X1R + (size_t)blockIdx.x * 576 * 32 + (i ? 512 + (tid & 63) : tid) * 32;
      *(f32x4*)xb = v[i][0]; *(f32x4*)(xb + 16) = v[i][1]; }
  }
  ST_END(FINAL ? 3 : 1);
}

constexpr int C_STG = 9216 + 128 + 9216, C_UOFF = 9216 + 128, C_NSTG = 4;
DI unsigned geglu2(f32x2 g, f32x2 u) {
  const float c0 = -2.f * 0.7978845608028654f * 1.4426950408889634f, c1 = c0 * 0.044715f;
  const f32x2 z = g * (g * g * c1 + c0);
  const f32x2 d = (f32x2){__builtin_amdgcn_exp2f(z[0]), __builtin_amdgcn_exp2f(z[1])} + 1.f;
  const f32x2 o = (g * u) * (f32x2){__builtin_amdgcn_rcpf(d[0]), __builtin_amdgcn_rcpf(d[1])};
  return pk2(o[0], o[1]);
}
#define C_LA 6
DI void ffn1_prefetch(const MainArgs& ar, int wid) {
  const int lane = lane_id();
  const int x = blockIdx.x & 7, j = blockIdx.x >> 3, ct = 4 * x + (j & 3);
  unsigned wsrc3[3];
#pragma unroll
  for (int e = 0; e < 3; ++e) { const int o = 64 * e + lane, row = o / 18, ch = o - row * 18; wsrc3[e] = (16 * (row >> 2) + (row & 3)) * (FF * 2) + ch * 16; }
  const char* Ww = (wid < 4 ? ar.wg : ar.wu) + (size_t)(wid & 3) * 4 * (FF * 2) + ct * 288;
  const unsigned wdst = C_BASE + (wid < 4 ? 0 : C_UOFF) + (wid & 3) * 2304;
#pragma unroll
  for (int s = 0; s < 3; ++s) { const char* wsrc = Ww + (size_t)(32 * s) * (FF * 2); const unsigned m = s * C_STG + wdst;
    dma16<0>(m, wsrc3[0], wsrc); dma16<0>(m + 1024, wsrc3[1], wsrc); if (lane < 16) dma16<0>(m + 2048, wsrc3[2], wsrc); }
}
DI void phase_ffn1(const MainArgs& ar, LAS char* lds, const int wid DBG_PARAM) {
  PH_BEGIN
  char* const ws = ar.ws; const char* wg = ar.wg; const char* wu = ar.wu;
  const int lane = lane_id(), tid = wid * 64 + lane;
  const int x = blockIdx.x & 7, j = blockIdx.x >> 3, ct = 4 * x + (j & 3), rt = j >> 2;
  const int rb = rt * 8 + wid;
  const int h = lane >> 5, blk = (lane >> 4) & 1, q = (lane & 15) >> 2, p = lane & 3;
  unsigned wsrc3[3], wsrc3b[3];
#pragma unroll
  for (int e = 0; e < 3; ++e) { const int o = 64 * e + lane, row = o / 18, ch = o - row * 18;
    wsrc3[e] = (16 * (row >> 2) + (row & 3)) * (FF * 2) + ch * 16; wsrc3b[e] = (144 * (row >> 2) + (row & 3)) * (FF * 2) + ch * 16; }
  const char* Ww = (wid < 4 ? wg : wu) + (size_t)(wid & 3) * 4 * (FF * 2) + ct * 288;
  const unsigned wdst = C_BASE + (wid < 4 ? 0 : C_UOFF) + (wid & 3) * 2304;
  const char* hw = ws + WS_H2 + (size_t)rb * 36 * 1024;
  const unsigned lane16 = lane * 16;
  bf16x8 af[18][2];
  auto issue_piece = [&](auto S, auto E) { constexpr int s = S, e = E;
    constexpr int kbase = s < 16 ? 144 * (s >> 2) + 32 * (s & 3) : 288 * (s - 16) + 128;
    const char* wsrc = Ww + (size_t)kbase * (FF * 2); const unsigned m = (s % C_NSTG) * C_STG + wdst;
    if constexpr (e == 0) dma16<0>(m, s < 16 ? wsrc3[0] : wsrc3b[0], wsrc);
    if constexpr (e == 1) dma16<0>(m + 1024, s < 16 ? wsrc3[1] : wsrc3b[1], wsrc);
    if constexpr (e == 2) { if (lane < 16) dma16<0>(m + 2048, s < 16 ? wsrc3[2] : wsrc3b[2], wsrc); }
    if constexpr (e == 3) gload16c<0>(af[s][0], lane16, hw + s * 2048);
    if constexpr (e == 4) gload16c<1024>(af[s][1], lane16, hw + s * 2048); };
  static_for<3>([&](auto S) { issue_piece(S, integral_constant<int, 3>{}); issue_piece(S, integral_constant<int, 4>{}); });
  unsigned tb[C_NSTG];
#pragma unroll
  for (int i = 0; i < C_NSTG; ++i) { tb[i] = C_BASE + blk * C_UOFF + 288 * (8 * h + q) + 8 * p + i * C_STG; pin(tb[i]); }
  f32x16 acc[9] = {};
  constexpr int LA = C_LA, NIT = 18 * 18;
  bf16x8 wf[LA + 1];
  auto rd = [&](auto I) { constexpr int i = I; constexpr int s = i / 18, ks = (i % 18) / 9, f = i % 9;
    const bf16x4 lo = tr4(lds + tb[s % C_NSTG] + 288 * 16 * ks + 32 * f), hi = tr4(lds + tb[s % C_NSTG] + 288 * (16 * ks + 4) + 32 * f);
    wf[i % (LA + 1)] = cat(lo, hi); };
  SB(); vm_bar<4>(); pin(af[0][0]); pin(af[0][1]); revalidate<1024>(af[0][0], af[0][1], lane16, lane16, hw); SB();
  ST(1);
  static_for<LA>([&](auto I) { rd(I); });
  SB();
  static_for<NIT>([&](auto I) { constexpr int i = I; constexpr int s = i / 18, r = i % 18, ks = r / 9, f = r % 9;
    if constexpr (r == 0 && s > 0) {
      constexpr int outst = (s + 2 < 18) ? 5 : 0;
      vm_bar<outst>(); pin(af[s][0]); pin(af[s][1]);
      revalidate<1024>(af[s][0], af[s][1], lane16, lane16, hw + s * 2048);
      if constexpr (s == 4) ST(2);
      if constexpr (s == 8) ST(3);
      if constexpr (s == 12) ST(4);
      if constexpr (s == 16) ST(5);
      if constexpr (s == 17) proj_prefetch(ar.wd, 18, wid);
      SB();
    }
    if constexpr (i + LA < NIT) rd(integral_constant<int, i + LA>{});
    acc[f] = mfma32(wf[i % (LA + 1)], af[s][ks], acc[f]);
    if constexpr (s + 3 < 18 && (r % 3) == 1 && r / 3 < 5) issue_piece(integral_constant<int, s + 3>{}, integral_constant<int, r / 3>{});
    SB();
  });
  ST(6);
  char* ob = ws + WS_H + ((size_t)rb * 288 + ct * 9) * 1024;
#pragma unroll
  for (int f = 0; f < 9; ++f) { u32x4 w;
#pragma unroll
    for (int i = 0; i < 4; ++i) w[i] = geglu2((f32x2){acc[f][2 * i], acc[f][2 * i + 1]}, (f32x2){acc[f][8 + 2 * i], acc[f][8 + 2 * i + 1]});
    st16c(ob, lane16 + f * 1024, w); }
  signal_done(ws, CNT_H + rt * 4 + (ct >> 3), wid);
  ST_END(2);
}

__global__ __launch_bounds__(512, 2) void k_main(MainArgs ar DBG_PARAM) {
  extern __shared__ __attribute__((aligned(16))) char lds_[];
  LAS char* lds = (LAS char*)lds_;
  const int wid = rfl(threadIdx.x >> 6);
  if (wid == 7) {
    unsigned long long pc; unsigned len;
    asm volatile("s_getpc_b64 %0\n.Lkm_beg:\n\ts_mov_b32 %1, .Lkm_fin-.Lkm_beg" : "=s"(pc), "=s"(len));
    const unsigned per = (len / 128 + 31) / 32, l0 = (blockIdx.x >> 3) * per + lane_id();
    unsigned off = (lane_id() < (int)per && l0 * 128 < len) ? l0 * 128 : 0;
    dma4(A_TAB + 2048 + 7 * 256, off, (const void*)pc);
  }
  phase_attn(ar, lds, wid PH_DBG);
  phase_proj<false>(ar, lds, wid PH_DBG);
  phase_ffn1(ar, lds, wid PH_DBG);
  phase_proj<true>(ar, lds, wid PH_DBG);
  asm volatile(".Lkm_fin:");
}

extern "C" void kernel_launch(void* const* d_in, const int* in_sizes, int n_in, void* d_out, int out_size, void* d_ws, size_t ws_size, hipStream_t stream) {
  const unsigned short* x = (const unsigned short*)d_in[0];
  const unsigned short* c = (const unsigned short*)d_in[9]; const unsigned short* wmod = (const unsigned short*)d_in[10];
  char* ws = (char*)d_ws;
  (void)hipFuncSetAttribute((const void*)k_pre, hipFuncAttributeMaxDynamicSharedMemorySize, PRE_LDS);
  (void)hipFuncSetAttribute((const void*)k_main, hipFuncAttributeMaxDynamicSharedMemorySize, MAIN_LDS);
  const char* kc = (const char*)d_in[1]; const char* vc = (const char*)d_in[2];
  const char* wq = (const char*)d_in[3]; const char* wo = (const char*)d_in[4];
  const char* wg = (const char*)d_in[6]; const char* wu = (const char*)d_in[7]; const char* wd = (const char*)d_in[8];
  k_pre<<<PRE_RQ + PRE_MOD + PRE_CLR, 512, PRE_LDS, stream>>>(c, wmod, x, wq, ws DBG_ARG);
  MainArgs ar{kc, vc, wg, wu, wo, wd, x, (unsigned short*)d_out, ws};
  k_main<<<256, 512, MAIN_LDS, stream>>>(ar DBG_ARG);
}
```

```cpp
#include <hip/hip_runtime.h>
#include <stdint.h>
#include <utility>

#define DI __device__ __forceinline__
#define LAS __attribute__((address_space(3)))
#define GAS __attribute__((address_space(1)))
typedef __bf16 bf16x8 __attribute__((ext_vector_type(8)));
typedef __bf16 bf16x4 __attribute__((ext_vector_type(4)));
typedef __bf16 bf16x2 __attribute__((ext_vector_type(2)));
typedef float f32x2 __attribute__((ext_vector_type(2)));
typedef float f32x4 __attribute__((ext_vector_type(4)));
typedef float f32x16 __attribute__((ext_vector_type(16)));
typedef unsigned u32x2 __attribute__((ext_vector_type(2)));
typedef unsigned u32x4 __attribute__((ext_vector_type(4)));
using std::integral_constant;

constexpr int M_ = 2048, D_ = 576, SK = 2048, WIN = 512, FF = 4608;

constexpr size_t WS_MOD = 0;
constexpr size_t WS_STAT = 16384;
constexpr size_t WS_H1 = WS_STAT + 149760 + 3840;
constexpr size_t WS_O = WS_H1 + (size_t)64 * 36 * 1024;
constexpr size_t WS_X1 = WS_O + (size_t)64 * 256 * 1024;
constexpr size_t WS_H2 = WS_X1 + (size_t)M_ * D_ * 4;
constexpr size_t WS_H = WS_H2 + (size_t)64 * 36 * 1024;
constexpr size_t WS_WQ = WS_H + (size_t)64 * 288 * 1024;
constexpr size_t WS_BP = WS_WQ + (size_t)32 * 36 * 4 * 1024;
constexpr size_t WS_CNT = WS_BP + (size_t)18 * 4096 * 4;
constexpr size_t WS_X1R = WS_CNT + 128 * 256;
constexpr size_t WS_PB = WS_X1R + (size_t)256 * 576 * 32;
constexpr size_t WS_PD = WS_PB + (size_t)16 * 16 * 16 * 2304;
constexpr size_t WS_END = WS_PD + (size_t)16 * 16 * 16 * 2304;

template <int... Is, class F> DI void static_for_impl(std::integer_sequence<int, Is...>, F&& f) { (f(std::integral_constant<int, Is>{}), ...); }
template <int N, class F> DI void static_for(F&& f) { static_for_impl(std::make_integer_sequence<int, N>{}, f); }

DI float bflo(unsigned w) { return __uint_as_float(w << 16); }
DI float bfhi(unsigned w) { return __uint_as_float(w & 0xffff0000u); }
DI unsigned pk2(float lo, float hi) { f32x2 v = {lo, hi}; bf16x2 b = __builtin_convertvector(v, bf16x2); return __builtin_bit_cast(unsigned, b); }
DI unsigned rfl(unsigned x) { return __builtin_amdgcn_readfirstlane(x); }
DI int lane_id() { int l; asm volatile("v_mbcnt_lo_u32_b32 %0, -1, 0\n\tv_mbcnt_hi_u32_b32 %0, -1, %0" : "=v"(l)); return l; }
DI void SB() { __builtin_amdgcn_sched_barrier(0); }

template <int IMM> DI void dma16(unsigned m0v, unsigned voff, const void* sbase) {
  asm volatile("s_mov_b32 m0, %0\n\ts_nop 0\n\tglobal_load_lds_dwordx4 %1, %2 offset:%3" ::"s"(m0v), "v"(voff), "s"(sbase), "i"(IMM) : "memory");
}
DI void dma4(unsigned m0v, unsigned voff, const void* sbase) {
  asm volatile("s_mov_b32 m0, %0\n\ts_nop 0\n\tglobal_load_lds_dword %1, %2" ::"s"(m0v), "v"(voff), "s"(sbase) : "memory");
}
template <int IMM, class T> DI void gload16(T& dst, unsigned voff, const void* sbase) {
  asm volatile("global_load_dwordx4 %0, %1, %2 offset:%3" : "=v"(dst) : "v"(voff), "s"(sbase), "i"(IMM));
}
#define LDF " sc1"
#define STF " sc1"
#define SC1L_S LDF
#define SC1S 1
template <int IMM, class T> DI void gload16c(T& dst, unsigned voff, const void* sbase) {
  asm volatile("global_load_dwordx4 %0, %1, %2 offset:%3" SC1L_S : "=v"(dst) : "v"(voff), "s"(sbase), "i"(IMM));
}
DI void st16c(char* base, unsigned voff, u32x4 v) {
  asm volatile("global_store_dwordx4 %0, %1, %2" STF "\n\ts_nop 1" ::"v"(voff), "v"(v), "s"(base) : "memory");
}
template <int N> DI void wait_vm() { asm volatile("s_waitcnt vmcnt(%0)" ::"i"(N) : "memory"); }
template <int N> DI void vm_bar() { asm volatile("s_waitcnt vmcnt(%0)\n\ts_barrier" ::"i"(N) : "memory"); }
template <int N> DI void vm_lgkm_bar() { asm volatile("s_waitcnt vmcnt(%0) lgkmcnt(0)\n\ts_barrier" ::"i"(N) : "memory"); }
DI void bar() { asm volatile("s_barrier" ::: "memory"); }
template <class T> DI void pin(T& a) { asm volatile("" : "+v"(a)); }
DI bf16x4 tr4(LAS char* p) { return __builtin_amdgcn_ds_read_tr16_b64_v4bf16((LAS bf16x4*)p); }
DI bf16x8 cat(bf16x4 a, bf16x4 b) { return __builtin_shufflevector(a, b, 0, 1, 2, 3, 4, 5, 6, 7); }
DI f32x16 mfma32(bf16x8 a, bf16x8 b, f32x16 c) { return __builtin_amdgcn_mfma_f32_32x32x16_bf16(a, b, c, 0, 0, 0); }
DI f32x4 mfma16(bf16x8 a, bf16x8 b, f32x4 c) { return __builtin_amdgcn_mfma_f32_16x16x32_bf16(a, b, c, 0, 0, 0); }
DI float max3f(float a, float b, float c) { float r; asm("v_max3_f32 %0, %1, %2, %3" : "=v"(r) : "v"(a), "v"(b), "v"(c)); return r; }
DI float max2f(float a, float b) { float r; asm("v_max_f32_e32 %0, %1, %2" : "=v"(r) : "v"(a), "v"(b)); return r; }
template <int CTRL> DI float dppf(float v) { return __builtin_bit_cast(float, __builtin_amdgcn_update_dpp(0, __builtin_bit_cast(int, v), CTRL, 0xf, 0xf, true)); }
DI float sum8(float v) { v += dppf<0xB1>(v); v += dppf<0x4E>(v); v += dppf<0x141>(v); return v; }
DI f32x16 splat16(float v) { f32x16 r; for (int i = 0; i < 16; ++i) r[i] = v; return r; }

#define ST_DECL
#define ST(i)
#define ST_END(kid)
#define DBG_PARAM
#define DBG_ARG
#define PH_BEGIN
#define PH_DBG

constexpr float QSCALE = 0.08838834764831845f * 1.4426950408889634f;
constexpr int PRE_RQ = 144, PRE_MOD = 108, PRE_CLR = 64;
constexpr int PRE_LDS = 32768;
DI float silu_f(float cv) { return cv * __builtin_amdgcn_rcpf(1.f + __builtin_amdgcn_exp2f(-1.4426950408889634f * cv)); }
DI float sum_x8_16_32(float v) {
  v += dppf<0x128>(v);
  { auto rr = __builtin_amdgcn_permlane16_swap(__float_as_uint(v), __float_as_uint(v), false, false); v = __uint_as_float(rr[0]) + __uint_as_float(rr[1]); }
  { auto rr = __builtin_amdgcn_permlane32_swap(__float_as_uint(v), __float_as_uint(v), false, false); v = __uint_as_float(rr[0]) + __uint_as_float(rr[1]); }
  return v;
}
__global__ __launch_bounds__(512) void k_pre(const unsigned short* __restrict__ c, const unsigned short* __restrict__ wmod, const unsigned short* __restrict__ xin, const char* __restrict__ wq, char* __restrict__ ws DBG_PARAM) {
  ST_DECL
  extern __shared__ __attribute__((aligned(16))) char lds_[];
  LAS char* lds = (LAS char*)lds_;
  const int tid = threadIdx.x, lane = tid & 63, wid = rfl(tid >> 6);
  int b = blockIdx.x;
  if (b < PRE_MOD) {
    LAS float* sl = (LAS float*)lds; LAS float* red = (LAS float*)(lds + 2304);
    const int rg = tid >> 2, col0 = b * 32 + (tid & 3) * 8;
    u32x4 w[5]; unsigned cw;
    asm volatile("global_load_dword %0, %1, %2" : "=v"(cw) : "v"((unsigned)(tid < 288 ? tid : 0) * 4), "s"(c));
#pragma unroll
    for (int i = 0; i < 5; ++i) { const int row = rg + 128 * i < 576 ? rg + 128 * i : rg; gload16<0>(w[i], (unsigned)(row * 3456 + col0) * 2, wmod); }
    asm volatile("s_waitcnt vmcnt(5)" : "+v"(cw));
    if (tid < 288) *(LAS f32x2*)(sl + 2 * tid) = (f32x2){silu_f(bflo(cw)), silu_f(bfhi(cw))};
    asm volatile("s_waitcnt vmcnt(0) lgkmcnt(0)\n\ts_barrier" ::: "memory");
    float acc[8] = {};
#pragma unroll
    for (int i = 0; i < 5; ++i) { pin(w[i]); const float s = rg + 128 * i < 576 ? sl[rg + 128 * i] : 0.f;
#pragma unroll
      for (int j = 0; j < 4; ++j) { acc[2 * j] += s * bflo(w[i][j]); acc[2 * j + 1] += s * bfhi(w[i][j]); } }
#pragma unroll
    for (int j = 0; j < 8; ++j) { float v = acc[j]; v += dppf<0x124>(v); acc[j] = sum_x8_16_32(v); }
    if (lane < 4) { *(LAS f32x4*)(red + wid * 32 + lane * 8) = (f32x4){acc[0], acc[1], acc[2], acc[3]}; *(LAS f32x4*)(red + wid * 32 + lane * 8 + 4) = (f32x4){acc[4], acc[5], acc[6], acc[7]}; }
    asm volatile("s_waitcnt lgkmcnt(0)\n\ts_barrier" ::: "memory");
    if (tid < 32) { float t = 0.f;
#pragma unroll
      for (int w2 = 0; w2 < 8; ++w2) t += red[w2 * 32 + tid];
      ((float*)(ws + WS_MOD))[b * 32 + tid] = t; }
    ST_END(5);
    return;
  }
  b -= PRE_MOD;
  if (b < PRE_CLR) {
    const int cc = lane & 15, row = (b * 8 + wid) * 4 + (lane >> 4);
    u32x4 xv[5];
#pragma unroll
    for (int i = 0; i < 5; ++i) xv[i] = *(const u32x4*)(xin + (size_t)row * D_ + (cc + 16 * i < 72 ? cc + 16 * i : cc) * 8);
    if (tid < 32) *(u32x4*)(ws + WS_CNT + (size_t)(b * 32 + tid) * 16) = (u32x4){0u, 0u, 0u, 0u};
#pragma unroll
    for (int i = 0; i < 5; ++i) { const unsigned o = (unsigned)(b * 36864 + i * 8192 + tid * 16); if (i < 4 || tid < 256) st16c(ws + WS_H1, o, (u32x4){~0u, ~0u, ~0u, ~0u}); }
    const bool last = cc < 8;
    float sm = 0.f;
#pragma unroll
    for (int i = 0; i < 5; ++i)
#pragma unroll
      for (int j = 0; j < 4; ++j) { const float t = bflo(xv[i][j]) + bfhi(xv[i][j]); sm += (i < 4 || last) ? t : 0.f; }
    sm = sum8(sm); sm += dppf<0x128>(sm);
    const float mean = sm * (1.f / 576.f);
    float q = 0.f;
#pragma unroll
    for (int i = 0; i < 5; ++i)
#pragma unroll
      for (int j = 0; j < 4; ++j) { const float d0 = bflo(xv[i][j]) - mean, d1 = bfhi(xv[i][j]) - mean; q += (i < 4 || last) ? d0 * d0 + d1 * d1 : 0.f; }
    q = sum8(q); q += dppf<0x128>(q);
    const float rstd = __builtin_amdgcn_rsqf(q * (1.f / 576.f) + 1e-5f);
    if (cc == 0) ((f32x2*)(ws + WS_STAT))[row] = (f32x2){mean, rstd};
    ST_END(6);
    return;
  }
  b -= PRE_CLR;
  {
    const int u = b * 8 + wid, s32 = u >> 6, cg = u & 63;
    { const int rr = lane >> 3, pp = lane & 7;
      const unsigned v = rr * 8192 + ((((pp >> 1) ^ ((rr >> 1) & 3)) << 1) + (pp & 1)) * 16;
      const char* src = wq + (size_t)s32 * 32 * 8192 + cg * 128; const unsigned m = wid * 4096;
      dma16<0>(m, v, src); dma16<0>(m + 1024, v, src + 8 * 8192); dma16<0>(m + 2048, v, src + 16 * 8192); dma16<0>(m + 3072, v, src + 24 * 8192); }
    wait_vm<0>();
    const int g = lane >> 4, q = (lane & 15) >> 2, p = lane & 3, h = lane >> 5, blk = g & 1, pc = ((p & 1) << 1) | (p >> 1);
    LAS char* scr = lds + wid * 4096;
#pragma unroll
    for (int dbl = 0; dbl < 2; ++dbl)
#pragma unroll
      for (int ks = 0; ks < 2; ++ks) { const int r0 = 16 * ks + 8 * h + q, r1 = r0 + 4, cx = 2 * dbl + blk;
        const bf16x8 w = cat(tr4(scr + 128 * r0 + 32 * (cx ^ ((r0 >> 1) & 3)) + 8 * pc), tr4(scr + 128 * r1 + 32 * (cx ^ ((r1 >> 1) & 3)) + 8 * pc));
        st16c(ws + WS_WQ, (unsigned)((((cg >> 1) * 36 + 2 * s32 + ks) * 4 + 2 * (cg & 1) + dbl) * 1024 + lane * 16), __builtin_bit_cast(u32x4, w)); }
    ST_END(4);
  }
}

constexpr float THR = 8.0f;
constexpr int A_TAB = 131072;
constexpr int A_LDS = 131072 + 2048;
#define A_LA 4
#define Q_LA 5
struct MainArgs { const char* kc; const char* vc; const char* wg; const char* wu; const char* wo; const char* wd; const unsigned short* x; unsigned short* out; char* ws; };
constexpr int P_STG = 9216, P_NSTG = 4, P_T = 73728;
constexpr int C_BASE = 81920;
constexpr int MAIN_LDS = 163840;
constexpr int CNT_CLEAR = 0;
constexpr int CNT_O = 8, CNT_H = 40;
DI void signal_done(char* ws, int idx, int wid) {
  vm_bar<0>();
  if (wid == 0 && lane_id() == 0) __hip_atomic_fetch_add((unsigned*)(ws + WS_CNT) + idx * 64, 1u, __ATOMIC_RELAXED, __HIP_MEMORY_SCOPE_AGENT);
}
DI void wait_count(const char* ws, int idx, unsigned target, int wid) {
  if (wid == 0) {
    const unsigned* cnt = (const unsigned*)(ws + WS_CNT) + idx * 64;
    for (unsigned spin = 0; spin < (1u << 15); ++spin) {
      if (rfl(__hip_atomic_load(cnt, __ATOMIC_RELAXED, __HIP_MEMORY_SCOPE_AGENT)) >= target) break;
      __builtin_amdgcn_s_sleep(1);
    }
  }
  bar();
}
DI void st_empty(char* base, unsigned voff) { st16c(base, voff, (u32x4){~0u, ~0u, ~0u, ~0u}); }
DI bool has_empty(const bf16x8& a, const bf16x8& b) {
  const u32x4 x = __builtin_bit_cast(u32x4, a), y = __builtin_bit_cast(u32x4, b);
  unsigned m = x[0] > x[1] ? x[0] : x[1]; m = m > x[2] ? m : x[2]; m = m > x[3] ? m : x[3]; m = m > y[0] ? m : y[0]; m = m > y[1] ? m : y[1]; m = m > y[2] ? m : y[2]; m = m > y[3] ? m : y[3];
  return __any(m == ~0u);
}
template <int IMM1> DI void revalidate(bf16x8& a, bf16x8& b, unsigned oa, unsigned ob, const char* base) {
  if (__builtin_expect(has_empty(a, b), 0)) {
    for (unsigned spin = 0; spin < (1u << 15); ++spin) {
      __builtin_amdgcn_s_sleep(3);
      asm volatile("global_load_dwordx4 %0, %2, %4 sc1\n\tglobal_load_dwordx4 %1, %3, %4 offset:%5 sc1\n\ts_waitcnt vmcnt(0)" : "+v"(a), "+v"(b) : "v"(oa), "v"(ob), "s"(base), "i"(IMM1) : "memory");
      if (!has_empty(a, b)) break;
    }
  }
}
DI void proj_prefetch(const char* W, int nst, int wid) {
  const int lane = lane_id();
  const int x = blockIdx.x & 7, j = blockIdx.x >> 3, ks = x >> 1, ct = j & 3, rg = wid & 3, kh = wid >> 2;
  unsigned wsrc3[3];
#pragma unroll
  for (int e = 0; e < 3; ++e) { const int o = 64 * e + lane, row = o / 18, ch = o - row * 18; wsrc3[e] = row * 1152 + ch * 16; }
  const char* Ww = W + (size_t)((ks * 2 + kh) * nst * 32 + rg * 8) * 1152 + ct * 288;
  const unsigned wdst = kh * (P_NSTG * P_STG) + rg * 2304;
#pragma unroll
  for (int s = 0; s < 3; ++s) { const char* wsrc = Ww + (size_t)s * 32 * 1152; const unsigned m = s * P_STG + wdst;
    dma16<0>(m, wsrc3[0], wsrc); dma16<0>(m + 1024, wsrc3[1], wsrc); if (lane < 16) dma16<0>(m + 2048, wsrc3[2], wsrc); }
}
DI void phase_attn(const MainArgs& ar, LAS char* lds, const int wid DBG_PARAM) {
  PH_BEGIN
  char* const ws = ar.ws;
  const int lane = lane_id(), tid = wid * 64 + lane;
  const int x = blockIdx.x & 7, j = blockIdx.x >> 3, head = 4 * x + (j & 3), rt = j >> 2, kvh = head >> 1;
  const int rb = rt * 8 + wid;
  const int prb = 2 * (j >> 2) + (x & 1), pown = (j & 3) * 4 + (x >> 1);
  const int h = lane >> 5, r = lane & 31, blk = (lane >> 4) & 1, q = (lane & 15) >> 2, p = lane & 3;
  const int l4 = lane >> 4, pos = lane & 15;
  unsigned ksrc2[2];
#pragma unroll
  for (int e = 0; e < 2; ++e) { const int n = 2 * wid + e, row = 4 * n + l4, ch = pos ^ ((l4 << 2) | (n & 3)); ksrc2[e] = row * 256 + ch * 16 - e * 1024; }
  const char* wq_h = ws + WS_WQ + (size_t)head * 36 * 4096 + wid * 2048;
  const char* kb_ = ar.kc + ((size_t)kvh * SK + (SK - WIN)) * 256;
  const char* vb_ = ar.vc + ((size_t)kvh * SK + (SK - WIN)) * 256;
  const char* h1w = ws + WS_H1 + (size_t)rb * 36 * 1024;
  const unsigned lane16 = lane * 16;
  auto dmaW = [&](int qq, int e) { const unsigned m = 65536 + (qq & 3) * 16384 + wid * 2048; if (e == 0) dma16<0>(m, lane16, wq_h + qq * 16384); else dma16<1024>(m, lane16, wq_h + qq * 16384); };
  auto dmaK8 = [&](int t) { const unsigned m = (t & 3) * 16384 + wid * 2048; const char* s = kb_ + t * 16384; dma16<0>(m, ksrc2[0], s); dma16<1024>(m, ksrc2[1], s); };
  auto dmaV8 = [&](int t) { const unsigned m = 65536 + ((t + 1) & 3) * 16384 + wid * 2048; const char* s = vb_ + t * 16384; dma16<0>(m, ksrc2[0], s); dma16<1024>(m, ksrc2[1], s); };
  bf16x8 g[9][4];
  auto loadG1 = [&](auto QQ, auto E) { constexpr int qq = QQ, e = E; gload16c<e * 1024>(g[qq][e], lane16, h1w + qq * 4096); };
  auto checkG = [&](auto QQ) { constexpr int qq = QQ; static_for<4>([&](auto E) { pin(g[qq][E]); });
    revalidate<1024>(g[qq][0], g[qq][1], lane16, lane16, h1w + qq * 4096); revalidate<1024>(g[qq][2], g[qq][3], lane16, lane16, h1w + qq * 4096 + 2048); };
  auto loadG = [&](auto QQ) { static_for<4>([&](auto E) { loadG1(QQ, E); }); };
  auto mark = [&](auto MI) { constexpr int mi = MI;
    if constexpr (mi < 2) { const int rown = prb * 128 + pown * 8 + (tid & 7), kc = mi == 0 ? (tid >> 3) : 64 + (lane >> 3);
      st_empty(ws + WS_H2, (unsigned)(((rown >> 5) * 36 + (kc >> 1)) * 1024 + ((kc & 1) * 32 + (rown & 31)) * 16)); }
    else { const int n = tid + 512 * (mi - 2);
      st_empty(ws + WS_PB + (size_t)(prb * 16 + pown) * 36864, (unsigned)(n * 16 + (n < 2304 ? 0 : (unsigned)(WS_PD - WS_PB) - 36864u))); } };
  {
    const int row = rt * 256 + (8 * (j & 3) + x) * 8 + (tid & 7), u0 = tid >> 3, u1 = 64 + (lane >> 3);
    u32x4 xv[2]; f32x4 sa[2][2], ca[2][2]; f32x2 mr;
    asm volatile("global_load_dwordx2 %0, %1, %2" : "=v"(mr) : "v"((unsigned)row * 8), "s"(ws + WS_STAT));
    gload16<0>(xv[0], (unsigned)(row * D_ + u0 * 8) * 2, ar.x); gload16<0>(xv[1], (unsigned)(row * D_ + u1 * 8) * 2, ar.x);
#pragma unroll
    for (int i = 0; i < 2; ++i) { const unsigned o = (i ? u1 : u0) * 32; gload16<0>(sa[i][0], o, ws + WS_MOD); gload16<16>(sa[i][1], o, ws + WS_MOD); gload16<0>(ca[i][0], o, ws + WS_MOD + 2304); gload16<16>(ca[i][1], o, ws + WS_MOD + 2304); }
    dmaW(0, 0); dmaW(0, 1); dmaW(1, 0); dmaW(1, 1); dmaW(2, 0); dmaW(2, 1); dmaK8(0);
    wait_vm<8>(); pin(mr);
    const float rq = mr[1] * QSCALE;
#pragma unroll
    for (int i = 0; i < 2; ++i) { pin(xv[i]); pin(sa[i][0]); pin(sa[i][1]); pin(ca[i][0]); pin(ca[i][1]);
      const f32x4 v0 = {bflo(xv[i][0]), bfhi(xv[i][0]), bflo(xv[i][1]), bfhi(xv[i][1])}, v1 = {bflo(xv[i][2]), bfhi(xv[i][2]), bflo(xv[i][3]), bfhi(xv[i][3])};
      const f32x4 h0 = (v0 - mr[0]) * rq * (ca[i][0] + 1.f) + sa[i][0] * QSCALE, h1v = (v1 - mr[0]) * rq * (ca[i][1] + 1.f) + sa[i][1] * QSCALE; const int u = i ? u1 : u0;
      st16c(ws + WS_H1, (unsigned)(((row >> 5) * 36 + (u >> 1)) * 1024 + ((u & 1) * 32 + (row & 31)) * 16), (u32x4){pk2(h0[0], h0[1]), pk2(h0[2], h0[3]), pk2(h1v[0], h1v[1]), pk2(h1v[2], h1v[3])}); }
  }
  ST(10);
#define H1_SLEEP 16
  __builtin_amdgcn_s_sleep(H1_SLEEP);
  for (unsigned spin = 0; spin < (1u << 15); ++spin) {
    loadG(integral_constant<int, 0>{}); wait_vm<0>();
    static_for<4>([&](auto E) { pin(g[0][E]); });
    if (!has_empty(g[0][0], g[0][1]) && !has_empty(g[0][2], g[0][3])) break;
    __builtin_amdgcn_s_sleep(2);
  }
  ST(11);
  loadG(integral_constant<int, 1>{}); loadG(integral_constant<int, 2>{});
  unsigned wb0 = 65536 + lane16;
  pin(wb0);
  f32x16 acc[4] = {};
  {
    constexpr int LA = Q_LA, NIT = 9 * 16;
    bf16x8 wf[LA + 1];
    auto rd = [&](auto I) { constexpr int i = I; constexpr int qq = i / 16, ks = (i % 16) / 4, db = i % 4;
      wf[i % (LA + 1)] = *(LAS bf16x8*)(lds + wb0 + (qq & 3) * 16384 + ks * 4096 + db * 1024); };
    SB(); bar();
    SB();
    ST(1);
    static_for<LA>([&](auto I) { rd(I); });
    SB();
    static_for<NIT>([&](auto I) { constexpr int i = I; constexpr int qq = i / 16, rr = i % 16, ks = rr / 4, db = rr % 4;
      if constexpr (rr == 0 && qq > 0) {
        constexpr int NW[9] = {8, 10, 10, 10, 10, 10, 10, 8, 8};
        vm_bar<NW[qq]>();
        checkG(integral_constant<int, qq>{});
        if constexpr (qq == 4) ST(2);
        SB();
      }
      if constexpr (i + LA < NIT) rd(integral_constant<int, i + LA>{});
      acc[db] = mfma32(wf[i % (LA + 1)], g[qq][ks], acc[db]);
      if constexpr (qq + 3 < 9) {
        if constexpr (rr == 1) dmaW(qq + 3, 0);
        if constexpr (rr == 3) dmaW(qq + 3, 1);
        if constexpr (rr >= 5 && rr <= 11 && (rr & 1)) loadG1(integral_constant<int, qq + 3>{}, integral_constant<int, (rr - 5) / 2>{});
      } else {
        if constexpr (rr == 1) dmaV8(qq - 6);
        if constexpr (rr == 7) dmaK8(qq - 5);
      }
      SB();
    });
  }
  ST(3);
  bf16x8 qf[8];
#pragma unroll
  for (int db = 0; db < 4; ++db)
#pragma unroll
    for (int s = 0; s < 2; ++s) { u32x4 w;
#pragma unroll
      for (int i = 0; i < 4; ++i) w[i] = pk2(acc[db][8 * s + 2 * i], acc[db][8 * s + 2 * i + 1]);
      qf[2 * db + s] = __builtin_bit_cast(bf16x8, w); }
  const unsigned kb0 = 256 * r + 16 * (h ^ (((r & 3) << 2) | ((r >> 2) & 3)));
  const unsigned vb0 = 65536 + 256 * (4 * h + q) + 16 * ((q << 2) | ((2 * blk + (p >> 1)) ^ h)) + 8 * (p & 1);
  unsigned ka[8], va[4][2];
#pragma unroll
  for (int s = 0; s < 8; ++s) ka[s] = kb0 ^ (s << 5);
#pragma unroll
  for (int db = 0; db < 4; ++db) { va[db][0] = vb0 ^ (db << 6); va[db][1] = vb0 ^ (db << 6) ^ 32; }
  f32x16 o[4] = {}; f32x16 S[2][2], negm; unsigned pw[16]; float m; f32x2 l2 = {0.f, 0.f};
  constexpr int LA = A_LA;
  bf16x8 fr[LA + 1];
  struct Map { int g[272]; int n; };
  constexpr Map MP = []() constexpr { Map mp{}; mp.n = 0; for (int G = 0; G < 9 * 32; ++G) { const int t = G / 32 - 1, i = G % 32; if (i < 16 ? (t + 1 <= 7) : (t >= 0 && t <= 7)) mp.g[mp.n++] = G; } return mp; }();
  constexpr int NIT = MP.n;
  auto rdA = [&](auto C) { constexpr int c = C; constexpr int G = MP.g[c], t = G / 32 - 1, i = G % 32;
    if constexpr (i < 16) { constexpr int s = i >> 1, kb = i & 1; fr[c % (LA + 1)] = *(LAS bf16x8*)(lds + ka[s] + ((t + 1) & 3) * 16384 + kb * 8192); }
    else { constexpr int ii = i - 16, kk = ii >> 2, db = ii & 3; constexpr int so = ((t + 1) & 3) * 16384 + kk * 4096;
      fr[c % (LA + 1)] = cat(tr4(lds + va[db][0] + so), tr4(lds + va[db][1] + so + 2048)); } };
  auto rowmax_finish = [&](float a) { auto rr = __builtin_amdgcn_permlane32_swap(__float_as_uint(a), __float_as_uint(a), false, false); return max2f(__uint_as_float(rr[0]), __uint_as_float(rr[1])); };
  float mxa, mxb;
  const unsigned gw = blockIdx.x * 8 + wid;
  auto touch = [&](auto TI) { constexpr int ti = TI; constexpr int m = ti >> 1, half = ti & 1;
    constexpr unsigned per = m == 0 ? 9 : 11, tot = m == 0 ? 36864 : 41472;
    unsigned line = (gw * 2 + half) * per + (lane < (int)per ? lane : 0); if (line >= tot) line = tot - 1;
    dma4(A_TAB + 2048 + wid * 256, line * 128, m == 0 ? ar.wo : (m == 1 ? ar.wg : (m == 2 ? ar.wu : ar.wd))); };
  auto background = [&](auto BI) { constexpr int bi = BI;
    if constexpr ((bi & 3) == 0) touch(integral_constant<int, (bi >> 2) < 2 ? (bi >> 2) : (bi >> 2) + 4>{}); else mark(integral_constant<int, (bi >> 2) * 3 + (bi & 3) - 1>{}); };
  SB(); vm_bar<8>(); SB();
  ST(4);
  static_for<LA>([&](auto C) { rdA(C); });
  SB();
  static_for<NIT>([&](auto C) { constexpr int c = C; constexpr int G = MP.g[c], t = G / 32 - 1, i = G % 32;
    constexpr bool firstX = (i == 0) || (t == 7 && i == 16);
    if constexpr (firstX && t >= 0) {
      constexpr int NW[8] = {4, 8, 12, 12, 11, 5, 0, 0};
      float a = rowmax_finish(max2f(mxa, mxb));
      if constexpr (t == 0) {
        m = a; negm = splat16(-a);
#pragma unroll
        for (int r2 = 0; r2 < 16; ++r2) { S[0][0][r2] -= a; S[0][1][r2] -= a; }
      } else if (__builtin_expect(__any(a > THR), 0)) {
        const float dl = fmaxf(a, 0.f), f = __builtin_amdgcn_exp2f(-dl);
        m += dl; negm = splat16(-m); l2 *= f;
#pragma unroll
        for (int r2 = 0; r2 < 16; ++r2) { S[t & 1][0][r2] -= dl; S[t & 1][1][r2] -= dl; }
#pragma unroll
        for (int d = 0; d < 4; ++d)
#pragma unroll
          for (int r2 = 0; r2 < 16; ++r2) o[d][r2] *= f;
      }
      SB(); vm_bar<NW[t]>(); SB();
      if constexpr (t == 7) { if (wid == 0 && lane == 0) __hip_atomic_fetch_add((unsigned*)(ws + WS_CNT) + (CNT_CLEAR + rt) * 64, 1u, __ATOMIC_RELAXED, __HIP_MEMORY_SCOPE_AGENT); }
      if constexpr (t == 3) ST(5);
      if constexpr (t == 4) ST(7);
      if constexpr (t == 5) ST(8);
      if constexpr (t == 7) proj_prefetch(ar.wo, 16, wid);
      if constexpr (t == 7) {
#pragma unroll
        for (int e = 0; e < 32; ++e) S[1][e >> 4][e & 15] = __builtin_amdgcn_exp2f(S[1][e >> 4][e & 15]);
#pragma unroll
        for (int e = 0; e < 16; ++e) pw[e] = pk2(S[1][e >> 3][(2 * e) & 15], S[1][e >> 3][((2 * e) & 15) + 1]);
        SB();
      }
    }
    if constexpr (c + LA < NIT) rdA(integral_constant<int, c + LA>{});
    if constexpr (i < 16) {
      constexpr int s = i >> 1, kb = i & 1, X = (t + 1) & 1;
      if constexpr (s == 0) { if constexpr (t < 0) S[X][kb] = mfma32(fr[c % (LA + 1)], qf[s], f32x16{}); else S[X][kb] = mfma32(fr[c % (LA + 1)], qf[s], negm); }
      else S[X][kb] = mfma32(fr[c % (LA + 1)], qf[s], S[X][kb]);
      if constexpr (t >= 0) {
        constexpr int e0 = 2 * i, e1 = 2 * i + 1, Y = t & 1;
        S[Y][e0 >> 4][e0 & 15] = __builtin_amdgcn_exp2f(S[Y][e0 >> 4][e0 & 15]);
        S[Y][e1 >> 4][e1 & 15] = __builtin_amdgcn_exp2f(S[Y][e1 >> 4][e1 & 15]);
        if constexpr (i >= 1) { constexpr int e = i - 1; pw[e] = pk2(S[Y][e >> 3][(2 * e) & 15], S[Y][e >> 3][((2 * e) & 15) + 1]); }
        if constexpr (i == 15) { SB(); pw[15] = pk2(S[Y][1][14], S[Y][1][15]); }
        if constexpr (t <= 3 && i == 6) dma16<0>((t & 3) * 16384 + wid * 2048, ksrc2[0], kb_ + (t + 4) * 16384);
        if constexpr (t <= 3 && i == 9) dma16<1024>((t & 3) * 16384 + wid * 2048, ksrc2[1], kb_ + (t + 4) * 16384);
        if constexpr (t <= 4 && i == 12) dma16<0>(65536 + (t & 3) * 16384 + wid * 2048, ksrc2[0], vb_ + (t + 3) * 16384);
        if constexpr (t <= 4 && i == 15) dma16<1024>(65536 + (t & 3) * 16384 + wid * 2048, ksrc2[1], vb_ + (t + 3) * 16384);
      }
      if constexpr (t < 0 && i == 15) {
        asm volatile("s_nop 11" : "+v"(S[0][0]), "+v"(S[0][1]));
        mxa = max3f(S[0][0][0], S[0][0][1], S[0][0][2]); mxb = max3f(S[0][1][0], S[0][1][1], S[0][1][2]);
#pragma unroll
        for (int r2 = 3; r2 < 15; r2 += 2) { mxa = max3f(mxa, S[0][0][r2], S[0][0][r2 + 1]); mxb = max3f(mxb, S[0][1][r2], S[0][1][r2 + 1]); }
        mxa = max3f(mxa, S[0][0][15], S[0][1][15]);
      }
    } else {
      constexpr int ii = i - 16, kk = ii >> 2, db = ii & 3, Y = t & 1, X = (t + 1) & 1;
      const u32x4 w = {pw[4 * kk], pw[4 * kk + 1], pw[4 * kk + 2], pw[4 * kk + 3]};
      o[db] = mfma32(fr[c % (LA + 1)], __builtin_bit_cast(bf16x8, w), o[db]);
      if constexpr (t <= 3 && (ii & 3) == 1 && 4 * t + (ii >> 2) < 15) background(integral_constant<int, 4 * t + (ii >> 2)>{});
      l2 += (f32x2){S[Y][ii >> 3][(2 * ii) & 15], S[Y][ii >> 3][((2 * ii) & 15) + 1]};
      if constexpr (t < 7) {
        if constexpr (ii == 4) { mxa = max3f(S[X][0][0], S[X][0][1], S[X][0][2]); mxb = max3f(S[X][0][3], S[X][0][4], S[X][0][5]); }
        if constexpr (ii == 5) { mxa = max3f(mxa, S[X][0][6], S[X][0][7]); mxb = max3f(mxb, S[X][0][8], S[X][0][9]); }
        if constexpr (ii == 6) { mxa = max3f(mxa, S[X][0][10], S[X][0][11]); mxb = max3f(mxb, S[X][0][12], S[X][0][13]); }
        if constexpr (ii == 7) { mxa = max3f(mxa, S[X][0][14], S[X][0][15]); mxb = max3f(mxb, S[X][1][0], S[X][1][1]); }
        if constexpr (ii == 8) { mxa = max3f(mxa, S[X][1][2], S[X][1][3]); mxb = max3f(mxb, S[X][1][4], S[X][1][5]); }
        if constexpr (ii == 9) { mxa = max3f(mxa, S[X][1][6], S[X][1][7]); mxb = max3f(mxb, S[X][1][8], S[X][1][9]); }
        if constexpr (ii == 10) { mxa = max3f(mxa, S[X][1][10], S[X][1][11]); mxb = max3f(mxb, S[X][1][12], S[X][1][13]); }
        if constexpr (ii == 11) { mxa = max3f(mxa, S[X][1][14], S[X][1][15]); }
      }
    }
    SB();
  });
  ST(12);
  float l = l2[0] + l2[1];
  { auto rr = __builtin_amdgcn_permlane32_swap(__float_as_uint(l), __float_as_uint(l), false, false); l = __uint_as_float(rr[0]) + __uint_as_float(rr[1]); }
  const float inv = __builtin_amdgcn_rcpf(l);
  char* ob = ws + WS_O + ((size_t)rb * 256 + head * 8) * 1024;
#pragma unroll
  for (int db = 0; db < 4; ++db)
#pragma unroll
    for (int s = 0; s < 2; ++s) { u32x4 w;
#pragma unroll
      for (int i = 0; i < 4; ++i) w[i] = pk2(o[db][8 * s + 2 * i] * inv, o[db][8 * s + 2 * i + 1] * inv);
      st16c(ob, lane16 + (2 * db + s) * 1024, w); }
  signal_done(ws, CNT_O + rt * 4 + (head >> 3), wid);
  ST_END(0);
}

#define P_LA 4
DI void ffn1_prefetch(const MainArgs& ar, int wid);
template <bool FINAL>
DI void phase_proj(const MainArgs& ar, LAS char* lds, const int wid DBG_PARAM) {
  PH_BEGIN
  char* const ws = ar.ws; const char* W = FINAL ? ar.wd : ar.wo; const unsigned short* xin = ar.x; unsigned short* out = ar.out;
  constexpr int NS16 = FINAL ? 288 : 256, NST = NS16 / 16;
  const char* act = ws + (FINAL ? WS_H : WS_O);
  const float* mod = (const float*)(ws + WS_MOD);
  char* part = ws + (FINAL ? WS_PD : WS_PB);
  const int lane = lane_id(), tid = wid * 64 + lane, rg = wid & 3, kh = wid >> 2;
  const int x = blockIdx.x & 7, j = blockIdx.x >> 3, ks = x >> 1, rb = 2 * (j >> 2) + (x & 1), ct = j & 3;
  const int own = ct * 4 + ks;
  const int g = lane >> 4, m16 = lane & 15, q4 = (lane & 15) >> 2, p = lane & 3;
  const int kb32 = (ks * 2 + kh) * NST;
  unsigned aoff[2];
#pragma unroll
  for (int a = 0; a < 2; ++a) { const int row = rb * 128 + rg * 32 + a * 16 + m16; aoff[a] = (unsigned)(row >> 5) * (NS16 * 1024) + (row & 31) * 16 + (g >> 1) * 1024 + (g & 1) * 512; }
  const char* actw = act + (size_t)kb32 * 2048;
  unsigned wsrc3[3];
#pragma unroll
  for (int e = 0; e < 3; ++e) { const int o = 64 * e + lane, row = o / 18, ch = o - row * 18; wsrc3[e] = row * 1152 + ch * 16; }
  const char* Ww = W + (size_t)(kb32 * 32 + rg * 8) * 1152 + ct * 288;
  const unsigned wdst = kh * (P_NSTG * P_STG) + rg * 2304;
  bf16x8 af[NST][2];
  auto issue_piece = [&](auto S, auto E) { constexpr int s = S, e = E;
    const char* wsrc = Ww + (size_t)s * 32 * 1152; const unsigned m = (s % P_NSTG) * P_STG + wdst;
    if constexpr (e == 0) dma16<0>(m, wsrc3[0], wsrc);
    if constexpr (e == 1) dma16<0>(m + 1024, wsrc3[1], wsrc);
    if constexpr (e == 2) { if (lane < 16) dma16<0>(m + 2048, wsrc3[2], wsrc); }
    if constexpr (e == 3) gload16c<0>(af[s][0], aoff[0], actw + s * 2048);
    if constexpr (e == 4) gload16c<0>(af[s][1], aoff[1], actw + s * 2048); };
  if (!FINAL && wid != 0) { ffn1_prefetch(ar, wid); if (wid == 1) ffn1_prefetch(ar, 0); }
  wait_count(ws, (FINAL ? CNT_H : CNT_O) + (rb >> 1) * 4 + ks, 8u, wid);
  static_for<3>([&](auto S) { issue_piece(S, integral_constant<int, 3>{}); issue_piece(S, integral_constant<int, 4>{}); });
  unsigned tb[P_NSTG];
#pragma unroll
  for (int i = 0; i < P_NSTG; ++i) tb[i] = kh * (P_NSTG * P_STG) + i * P_STG + 288 * (16 * (g >> 1) + 4 * (g & 1) + q4) + 8 * p;
  f32x4 acc[2][9] = {};
  constexpr int LA = P_LA, NIT = NST * 9;
  bf16x8 wf[LA + 1];
  auto rd = [&](auto I) { constexpr int i = I; constexpr int s = i / 9, c = i % 9;
    wf[i % (LA + 1)] = cat(tr4(lds + tb[s % P_NSTG] + 32 * c), tr4(lds + tb[s % P_NSTG] + 288 * 8 + 32 * c)); };
  constexpr int NPF = 0, NTB = FINAL ? 3 : 2;
  constexpr int TB = FINAL ? 81920 : 137600;
  SB(); vm_bar<4 + NPF>(); pin(af[0][0]); pin(af[0][1]);
#pragma unroll
  for (int e = 0; e < NTB; ++e) { const int n = NTB * wid + e;
    if (!FINAL) dma16<0>(TB + n * 1024, lane * 16, n < 7 ? (const char*)(mod + 1152) + n * 1024 : (const char*)xin + (size_t)(rb * 128 + own * 8) * 1152 + (n - 7) * 1024);
    else { const int n2 = n < 21 ? n : 0; dma16<0>(TB + n2 * 1024, lane * 16, n2 < 3 ? (const char*)(mod + 2880) + n2 * 1024 : ws + WS_X1R + (size_t)blockIdx.x * 576 * 32 + (n2 - 3) * 1024); } }
  SB();
  ST(1);
  static_for<LA>([&](auto I) { rd(I); });
  SB();
  static_for<NIT>([&](auto I) { constexpr int i = I; constexpr int s = i / 9, c = i % 9;
    if constexpr (c == 0 && s > 0) {
      constexpr int outst = s == 1 ? 2 + NPF + NTB + 5 : ((s + 2 < NST) ? 5 : 0);
      vm_bar<outst>(); pin(af[s][0]); pin(af[s][1]);
      if constexpr (s == 8) ST(2);
      SB();
    }
    if constexpr (i + LA < NIT) rd(integral_constant<int, i + LA>{});
    acc[0][c] = mfma16(wf[i % (LA + 1)], af[s][0], acc[0][c]);
    acc[1][c] = mfma16(wf[i % (LA + 1)], af[s][1], acc[1][c]);
    if constexpr (s + 3 < NST && c >= 1 && c <= 5) issue_piece(integral_constant<int, s + 3>{}, integral_constant<int, c - 1>{});
    SB();
  });
  ST(5);
  const int r8 = tid & 7, rown = rb * 128 + own * 8 + r8;
  int colA[2], colB[2];
  colA[0] = 144 * (tid >> 7) + 32 * ((tid >> 5) & 3) + 4 * ((tid >> 3) & 3); colB[0] = colA[0] + 16;
  colA[1] = 288 * ((tid >> 5) & 1) + 128 + 4 * ((tid >> 3) & 3); colB[1] = colA[1] + 144;
  unsigned marked;
  if (!FINAL) asm volatile("global_load_dword %0, %1, %2 sc1" : "=v"(marked) : "v"(0u), "s"(ws + WS_CNT + (size_t)(CNT_CLEAR + (rb >> 1)) * 256));
  {
    LAS char* exw = lds + (rg * 2 + kh) * 9216 + lane * 16; LAS char* exr = lds + (rg * 2 + (kh ^ 1)) * 9216 + lane * 16;
    bar();
    ST(3);
    f32x4 fin[9];
    if (kh) {
#pragma unroll
      for (int c = 0; c < 9; ++c) { *(LAS f32x4*)(exw + c * 1024) = acc[0][c]; fin[c] = acc[1][c]; }
    } else {
#pragma unroll
      for (int c = 0; c < 9; ++c) { *(LAS f32x4*)(exw + c * 1024) = acc[1][c]; fin[c] = acc[0][c]; }
    }
    asm volatile("s_waitcnt lgkmcnt(0)\n\ts_barrier" ::: "memory");
    ST(4);
    char* pw = part + (size_t)(rb * 16 + own) * 36864;
#pragma unroll
    for (int c = 0; c < 9; ++c) fin[c] += *(LAS f32x4*)(exr + c * 1024);
    const unsigned jo = (unsigned)(rg * 4 + kh * 2 + (m16 >> 3)) * 2304, lo = g * 8 + (m16 & 7);
    u32x4 cw[4];
#pragma unroll
    for (int cp = 0; cp < 4; ++cp) cw[cp] = (u32x4){pk2(fin[2 * cp][0], fin[2 * cp][1]), pk2(fin[2 * cp][2], fin[2 * cp][3]), pk2(fin[2 * cp + 1][0], fin[2 * cp + 1][1]), pk2(fin[2 * cp + 1][2], fin[2 * cp + 1][3])};
    const u32x2 w8 = {pk2(fin[8][0], fin[8][1]), pk2(fin[8][2], fin[8][3])};
#pragma unroll
    for (int cp = 0; cp < 4; ++cp) st16c(pw, jo + (cp * 32 + lo) * 16, cw[cp]);
    asm volatile("global_store_dwordx2 %0, %1, %2" STF "\n\ts_nop 1" ::"v"(jo + 2048 + lo * 8), "v"(w8), "s"(pw) : "memory");
  }
  ST(7);
  const char* pr = part + (size_t)rb * 16 * 36864 + own * 2304;
  const bool two = wid == 0;
  u32x4 pv[4]; u32x2 pq[4][2];
  if (!FINAL) {
    asm volatile("s_waitcnt vmcnt(5)" : "+v"(marked) :: "memory");
    if (!__all(marked >= 32u)) {
      const unsigned* cnt = (const unsigned*)(ws + WS_CNT) + (CNT_CLEAR + (rb >> 1)) * 64;
      for (unsigned spin = 0; spin < (1u << 15); ++spin) { if (__all(__hip_atomic_load(cnt, __ATOMIC_RELAXED, __HIP_MEMORY_SCOPE_AGENT) >= 32u)) break; __builtin_amdgcn_s_sleep(1); }
    }
  }
#define POLLMODE 1
#define P_SLEEP 20
  if (wid == 7) {
    for (unsigned spin = 0; spin < (1u << 15); ++spin) {
      unsigned t; asm volatile("global_load_dword %0, %1, %2 sc1\n\ts_waitcnt vmcnt(0)" : "=&v"(t) : "v"((unsigned)((lane & 15) * 36864 + 2300)), "s"(pr) : "memory");
      if (__all(t != ~0u)) break;
      __builtin_amdgcn_s_sleep(1);
    } }
  bar();
  ST(8);
  for (unsigned spin = 0; spin < (1u << 15); ++spin) {
    const unsigned o0 = (unsigned)((tid >> 7) * 4 * 36864 + (tid & 127) * 16), o1 = (unsigned)(((tid >> 5) & 1) * 8 * 36864 + 2048 + (tid & 31) * 8);
#pragma unroll
    for (int k = 0; k < 4; ++k) gload16c<0>(pv[k], o0 + k * 36864, pr);
    if (two) {
#pragma unroll
      for (int k = 0; k < 4; ++k) {
        asm volatile("global_load_dwordx2 %0, %1, %2" LDF : "=v"(pq[k][0]) : "v"(o1 + k * 36864), "s"(pr));
        asm volatile("global_load_dwordx2 %0, %1, %2" LDF : "=v"(pq[k][1]) : "v"(o1 + (4 + k) * 36864), "s"(pr)); } }
    wait_vm<0>();
    bool full = true;
#pragma unroll
    for (int k = 0; k < 4; ++k) { pin(pv[k]); full &= pv[k][0] != ~0u && pv[k][1] != ~0u && pv[k][2] != ~0u && pv[k][3] != ~0u; }
    if (two) {
#pragma unroll
      for (int k = 0; k < 4; ++k) { pin(pq[k][0]); pin(pq[k][1]); full &= pq[k][0][0] != ~0u && pq[k][0][1] != ~0u && pq[k][1][0] != ~0u && pq[k][1][1] != ~0u; } }
    if (__all(full)) break;
    __builtin_amdgcn_s_sleep(4);
  }
  ST(9);
  f32x4 v[2][2], sc[2][2], sh[2][2];
#pragma unroll
  for (int i = 0; i < 2; ++i) if (i == 0 || two) {
#pragma unroll
    for (int hh = 0; hh < 2; ++hh) { const int col = hh ? colB[i] : colA[i]; f32x4 rs;
      const f32x4 gt = *(LAS f32x4*)(lds + TB + col * 4);
      if (FINAL) rs = *(LAS f32x4*)(lds + TB + 3072 + (i ? 512 + (tid & 63) : tid) * 32 + hh * 16);
      else { const u32x2 xr = *(LAS u32x2*)(lds + TB + 7168 + r8 * 1152 + col * 2); rs = (f32x4){bflo(xr[0]), bfhi(xr[0]), bflo(xr[1]), bfhi(xr[1])};
        sh[i][hh] = *(LAS f32x4*)(lds + TB + (576 + col) * 4); sc[i][hh] = *(LAS f32x4*)(lds + TB + (1152 + col) * 4); }
      f32x4 t = {0.f, 0.f, 0.f, 0.f};
#pragma unroll
      for (int k = 0; k < 4; ++k) { const unsigned w0 = i ? pq[k][hh][0] : pv[k][2 * hh], w1 = i ? pq[k][hh][1] : pv[k][2 * hh + 1]; t += (f32x4){bflo(w0), bfhi(w0), bflo(w1), bfhi(w1)}; }
      v[i][hh] = rs + gt * t; }
  }
  ST(10);
  if (FINAL) {
#pragma unroll
    for (int i = 0; i < 2; ++i) if (i == 0 || two) {
      *(u32x2*)(out + (size_t)rown * D_ + colA[i]) = (u32x2){pk2(v[i][0][0], v[i][0][1]), pk2(v[i][0][2], v[i][0][3])};
      *(u32x2*)(out + (size_t)rown * D_ + colB[i]) = (u32x2){pk2(v[i][1][0], v[i][1][1]), pk2(v[i][1][2], v[i][1][3])}; }
  } else {
    LAS float* st = (LAS float*)(lds + P_T);
    auto s8 = [](const f32x4& a, const f32x4& b) { return ((a[0] + a[1]) + (a[2] + a[3])) + ((b[0] + b[1]) + (b[2] + b[3])); };
    float s = s8(v[0][0], v[0][1]) + (two ? s8(v[1][0], v[1][1]) : 0.f);
    s = sum_x8_16_32(s);
    if (lane < 8) st[wid * 8 + lane] = s;
    asm volatile("s_waitcnt lgkmcnt(0)\n\ts_barrier" ::: "memory");
    float tot = 0.f;
#pragma unroll
    for (int w = 0; w < 8; ++w) tot += st[w * 8 + r8];
    const float mean = tot * (1.f / 576.f);
    f32x4 d[2][2];
#pragma unroll
    for (int i = 0; i < 2; ++i) { d[i][0] = v[i][0] - mean; d[i][1] = v[i][1] - mean; }
    float qq = s8(d[0][0] * d[0][0], d[0][1] * d[0][1]) + (two ? s8(d[1][0] * d[1][0], d[1][1] * d[1][1]) : 0.f);
    qq = sum_x8_16_32(qq);
    if (lane < 8) st[64 + wid * 8 + lane] = qq;
    asm volatile("s_waitcnt lgkmcnt(0)\n\ts_barrier" ::: "memory");
    float tq = 0.f;
#pragma unroll
    for (int w = 0; w < 8; ++w) tq += st[64 + w * 8 + r8];
    const float rstd = __builtin_amdgcn_rsqf(tq * (1.f / 576.f) + 1e-5f);
#pragma unroll
    for (int i = 0; i < 2; ++i) if (i == 0 || two) {
      const f32x4 h0 = d[i][0] * rstd * (sc[i][0] + 1.f) + sh[i][0], h1 = d[i][1] * rstd * (sc[i][1] + 1.f) + sh[i][1];
      const int kc = i ? 64 + (tid >> 3) : (tid >> 3);
      st16c(ws + WS_H2, (unsigned)(((rown >> 5) * 36 + (kc >> 1)) * 1024 + ((kc & 1) * 32 + (rown & 31)) * 16), (u32x4){pk2(h0[0], h0[1]), pk2(h0[2], h0[3]), pk2(h1[0], h1[1]), pk2(h1[2], h1[3])});
      char* xb = ws + WS_X1R + (size_t)blockIdx.x * 576 * 32 + (i ? 512 + (tid & 63) : tid) * 32;
      *(f32x4*)xb = v[i][0]; *(f32x4*)(xb + 16) = v[i][1]; }
  }
  ST_END(FINAL ? 3 : 1);
}

constexpr int C_STG = 9216 + 128 + 9216, C_UOFF = 9216 + 128, C_NSTG = 4;
DI unsigned geglu2(f32x2 g, f32x2 u) {
  const float c0 = -2.f * 0.7978845608028654f * 1.4426950408889634f, c1 = c0 * 0.044715f;
  const f32x2 z = g * (g * g * c1 + c0);
  const f32x2 d = (f32x2){__builtin_amdgcn_exp2f(z[0]), __builtin_amdgcn_exp2f(z[1])} + 1.f;
  const f32x2 o = (g * u) * (f32x2){__builtin_amdgcn_rcpf(d[0]), __builtin_amdgcn_rcpf(d[1])};
  return pk2(o[0], o[1]);
}
#define C_LA 6
DI void ffn1_prefetch(const MainArgs& ar, int wid) {
  const int lane = lane_id();
  const int x = blockIdx.x & 7, j = blockIdx.x >> 3, ct = 4 * x + (j & 3);
  unsigned wsrc3[3];
#pragma unroll
  for (int e = 0; e < 3; ++e) { const int o = 64 * e + lane, row = o / 18, ch = o - row * 18; wsrc3[e] = (16 * (row >> 2) + (row & 3)) * (FF * 2) + ch * 16; }
  const char* Ww = (wid < 4 ? ar.wg : ar.wu) + (size_t)(wid & 3) * 4 * (FF * 2) + ct * 288;
  const unsigned wdst = C_BASE + (wid < 4 ? 0 : C_UOFF) + (wid & 3) * 2304;
#pragma unroll
  for (int s = 0; s < 3; ++s) { const char* wsrc = Ww + (size_t)(32 * s) * (FF * 2); const unsigned m = s * C_STG + wdst;
    dma16<0>(m, wsrc3[0], wsrc); dma16<0>(m + 1024, wsrc3[1], wsrc); if (lane < 16) dma16<0>(m + 2048, wsrc3[2], wsrc); }
}
DI void phase_ffn1(const MainArgs& ar, LAS char* lds, const int wid DBG_PARAM) {
  PH_BEGIN
  char* const ws = ar.ws; const char* wg = ar.wg; const char* wu = ar.wu;
  const int lane = lane_id(), tid = wid * 64 + lane;
  const int x = blockIdx.x & 7, j = blockIdx.x >> 3, ct = 4 * x + (j & 3), rt = j >> 2;
  const int rb = rt * 8 + wid;
  const int h = lane >> 5, blk = (lane >> 4) & 1, q = (lane & 15) >> 2, p = lane & 3;
  unsigned wsrc3[3], wsrc3b[3];
#pragma unroll
  for (int e = 0; e < 3; ++e) { const int o = 64 * e + lane, row = o / 18, ch = o - row * 18;
    wsrc3[e] = (16 * (row >> 2) + (row & 3)) * (FF * 2) + ch * 16; wsrc3b[e] = (144 * (row >> 2) + (row & 3)) * (FF * 2) + ch * 16; }
  const char* Ww = (wid < 4 ? wg : wu) + (size_t)(wid & 3) * 4 * (FF * 2) + ct * 288;
  const unsigned wdst = C_BASE + (wid < 4 ? 0 : C_UOFF) + (wid & 3) * 2304;
  const char* hw = ws + WS_H2 + (size_t)rb * 36 * 1024;
  const unsigned lane16 = lane * 16;
  bf16x8 af[18][2];
  auto issue_piece = [&](auto S, auto E) { constexpr int s = S, e = E;
    constexpr int kbase = s < 16 ? 144 * (s >> 2) + 32 * (s & 3) : 288 * (s - 16) + 128;
    const char* wsrc = Ww + (size_t)kbase * (FF * 2); const unsigned m = (s % C_NSTG) * C_STG + wdst;
    if constexpr (e == 0) dma16<0>(m, s < 16 ? wsrc3[0] : wsrc3b[0], wsrc);
    if constexpr (e == 1) dma16<0>(m + 1024, s < 16 ? wsrc3[1] : wsrc3b[1], wsrc);
    if constexpr (e == 2) { if (lane < 16) dma16<0>(m + 2048, s < 16 ? wsrc3[2] : wsrc3b[2], wsrc); }
    if constexpr (e == 3) gload16c<0>(af[s][0], lane16, hw + s * 2048);
    if constexpr (e == 4) gload16c<1024>(af[s][1], lane16, hw + s * 2048); };
  static_for<3>([&](auto S) { issue_piece(S, integral_constant<int, 3>{}); issue_piece(S, integral_constant<int, 4>{}); });
  unsigned tb[C_NSTG];
#pragma unroll
  for (int i = 0; i < C_NSTG; ++i) { tb[i] = C_BASE + blk * C_UOFF + 288 * (8 * h + q) + 8 * p + i * C_STG; pin(tb[i]); }
  f32x16 acc[9] = {};
  constexpr int LA = C_LA, NIT = 18 * 18;
  bf16x8 wf[LA + 1];
  auto rd = [&](auto I) { constexpr int i = I; constexpr int s = i / 18, ks = (i % 18) / 9, f = i % 9;
    const bf16x4 lo = tr4(lds + tb[s % C_NSTG] + 288 * 16 * ks + 32 * f), hi = tr4(lds + tb[s % C_NSTG] + 288 * (16 * ks + 4) + 32 * f);
    wf[i % (LA + 1)] = cat(lo, hi); };
  SB(); vm_bar<4>(); pin(af[0][0]); pin(af[0][1]); revalidate<1024>(af[0][0], af[0][1], lane16, lane16, hw); SB();
  ST(1);
  static_for<LA>([&](auto I) { rd(I); });
  SB();
  static_for<NIT>([&](auto I) { constexpr int i = I; constexpr int s = i / 18, r = i % 18, ks = r / 9, f = r % 9;
    if constexpr (r == 0 && s > 0) {
      constexpr int outst = (s + 2 < 18) ? 5 : 0;
      vm_bar<outst>(); pin(af[s][0]); pin(af[s][1]);
      revalidate<1024>(af[s][0], af[s][1], lane16, lane16, hw + s * 2048);
      if constexpr (s == 4) ST(2);
      if constexpr (s == 8) ST(3);
      if constexpr (s == 12) ST(4);
      if constexpr (s == 16) ST(5);
      if constexpr (s == 17) proj_prefetch(ar.wd, 18, wid);
      SB();
    }
    if constexpr (i + LA < NIT) rd(integral_constant<int, i + LA>{});
    acc[f] = mfma32(wf[i % (LA + 1)], af[s][ks], acc[f]);
    if constexpr (s + 3 < 18 && (r % 3) == 1 && r / 3 < 5) issue_piece(integral_constant<int, s + 3>{}, integral_constant<int, r / 3>{});
    SB();
  });
  ST(6);
  char* ob = ws + WS_H + ((size_t)rb * 288 + ct * 9) * 1024;
#pragma unroll
  for (int f = 0; f < 9; ++f) { u32x4 w;
#pragma unroll
    for (int i = 0; i < 4; ++i) w[i] = geglu2((f32x2){acc[f][2 * i], acc[f][2 * i + 1]}, (f32x2){acc[f][8 + 2 * i], acc[f][8 + 2 * i + 1]});
    st16c(ob, lane16 + f * 1024, w); }
  signal_done(ws, CNT_H + rt * 4 + (ct >> 3), wid);
  ST_END(2);
}

__global__ __launch_bounds__(512, 2) void k_main(MainArgs ar DBG_PARAM) {
  extern __shared__ __attribute__((aligned(16))) char lds_[];
  LAS char* lds = (LAS char*)lds_;
  const int wid = rfl(threadIdx.x >> 6);
  if (wid == 7) {
    unsigned long long pc; unsigned len;
    asm volatile("s_getpc_b64 %0\n.Lkm_beg:\n\ts_mov_b32 %1, .Lkm_fin-.Lkm_beg" : "=s"(pc), "=s"(len));
    const unsigned per = (len / 128 + 31) / 32, l0 = (blockIdx.x >> 3) * per + lane_id();
    unsigned off = (lane_id() < (int)per && l0 * 128 < len) ? l0 * 128 : 0;
    dma4(A_TAB + 2048 + 7 * 256, off, (const void*)pc);
  }
  phase_attn(ar, lds, wid PH_DBG);
  phase_proj<false>(ar, lds, wid PH_DBG);
  phase_ffn1(ar, lds, wid PH_DBG);
  phase_proj<true>(ar, lds, wid PH_DBG);
  asm volatile(".Lkm_fin:");
}

extern "C" void kernel_launch(void* const* d_in, const int* in_sizes, int n_in, void* d_out, int out_size, void* d_ws, size_t ws_size, hipStream_t stream) {
  const unsigned short* x = (const unsigned short*)d_in[0];
  const unsigned short* c = (const unsigned short*)d_in[9]; const unsigned short* wmod = (const unsigned short*)d_in[10];
  char* ws = (char*)d_ws;
  (void)hipFuncSetAttribute((const void*)k_pre, hipFuncAttributeMaxDynamicSharedMemorySize, PRE_LDS);
  (void)hipFuncSetAttribute((const void*)k_main, hipFuncAttributeMaxDynamicSharedMemorySize, MAIN_LDS);
  const char* kc = (const char*)d_in[1]; const char* vc = (const char*)d_in[2];
  const char* wq = (const char*)d_in[3]; const char* wo = (const char*)d_in[4];
  const char* wg = (const char*)d_in[6]; const char* wu = (const char*)d_in[7]; const char* wd = (const char*)d_in[8];
  k_pre<<<PRE_RQ + PRE_MOD + PRE_CLR, 512, PRE_LDS, stream>>>(c, wmod, x, wq, ws DBG_ARG);
  MainArgs ar{kc, vc, wg, wu, wo, wd, x, (unsigned short*)d_out, ws};
  k_main<<<256, 512, MAIN_LDS, stream>>>(ar DBG_ARG);
}
```

```cpp
#include <hip/hip_runtime.h>
#include <stdint.h>
#include <utility>

#define DI __device__ __forceinline__
#define LAS __attribute__((address_space(3)))
#define GAS __attribute__((address_space(1)))
typedef __bf16 bf16x8 __attribute__((ext_vector_type(8)));
typedef __bf16 bf16x4 __attribute__((ext_vector_type(4)));
typedef __bf16 bf16x2 __attribute__((ext_vector_type(2)));
typedef float f32x2 __attribute__((ext_vector_type(2)));
typedef float f32x4 __attribute__((ext_vector_type(4)));
typedef float f32x16 __attribute__((ext_vector_type(16)));
typedef unsigned u32x2 __attribute__((ext_vector_type(2)));
typedef unsigned u32x4 __attribute__((ext_vector_type(4)));
using std::integral_constant;

constexpr int M_ = 2048, D_ = 576, SK = 2048, WIN = 512, FF = 4608;

constexpr size_t WS_MOD = 0;
constexpr size_t WS_STAT = 16384;
constexpr size_t WS_H1 = WS_STAT + 149760 + 3840;
constexpr size_t WS_O = WS_H1 + (size_t)64 * 36 * 1024;
constexpr size_t WS_X1 = WS_O + (size_t)64 * 256 * 1024;
constexpr size_t WS_H2 = WS_X1 + (size_t)M_ * D_ * 4;
constexpr size_t WS_H = WS_H2 + (size_t)64 * 36 * 1024;
constexpr size_t WS_WQ = WS_H + (size_t)64 * 288 * 1024;
constexpr size_t WS_BP = WS_WQ + (size_t)32 * 36 * 4 * 1024;
constexpr size_t WS_CNT = WS_BP + (size_t)18 * 4096 * 4;
constexpr size_t WS_X1R = WS_CNT + 128 * 256;
constexpr size_t WS_PB = WS_X1R + (size_t)256 * 576 * 32;
constexpr size_t WS_PD = WS_PB + (size_t)16 * 16 * 16 * 2304;
constexpr size_t WS_END = WS_PD + (size_t)16 * 16 * 16 * 2304;

template <int... Is, class F> DI void static_for_impl(std::integer_sequence<int, Is...>, F&& f) { (f(std::integral_constant<int, Is>{}), ...); }
template <int N, class F> DI void static_for(F&& f) { static_for_impl(std::make_integer_sequence<int, N>{}, f); }

DI float bflo(unsigned w) { return __uint_as_float(w << 16); }
DI float bfhi(unsigned w) { return __uint_as_float(w & 0xffff0000u); }
DI unsigned pk2(float lo, float hi) { f32x2 v = {lo, hi}; bf16x2 b = __builtin_convertvector(v, bf16x2); return __builtin_bit_cast(unsigned, b); }
DI unsigned rfl(unsigned x) { return __builtin_amdgcn_readfirstlane(x); }
DI int lane_id() { int l; asm volatile("v_mbcnt_lo_u32_b32 %0, -1, 0\n\tv_mbcnt_hi_u32_b32 %0, -1, %0" : "=v"(l)); return l; }
DI void SB() { __builtin_amdgcn_sched_barrier(0); }

template <int IMM> DI void dma16(unsigned m0v, unsigned voff, const void* sbase) {
  asm volatile("s_mov_b32 m0, %0\n\ts_nop 0\n\tglobal_load_lds_dwordx4 %1, %2 offset:%3" ::"s"(m0v), "v"(voff), "s"(sbase), "i"(IMM) : "memory");
}
DI void dma4(unsigned m0v, unsigned voff, const void* sbase) {
  asm volatile("s_mov_b32 m0, %0\n\ts_nop 0\n\tglobal_load_lds_dword %1, %2" ::"s"(m0v), "v"(voff), "s"(sbase) : "memory");
}
template <int IMM, class T> DI void gload16(T& dst, unsigned voff, const void* sbase) {
  asm volatile("global_load_dwordx4 %0, %1, %2 offset:%3" : "=v"(dst) : "v"(voff), "s"(sbase), "i"(IMM));
}
#define LDF " sc1"
#define STF " sc1"
#define SC1L_S LDF
#define SC1S 1
template <int IMM, class T> DI void gload16c(T& dst, unsigned voff, const void* sbase) {
  asm volatile("global_load_dwordx4 %0, %1, %2 offset:%3" SC1L_S : "=v"(dst) : "v"(voff), "s"(sbase), "i"(IMM));
}
DI void st16c(char* base, unsigned voff, u32x4 v) {
  asm volatile("global_store_dwordx4 %0, %1, %2" STF "\n\ts_nop 1" ::"v"(voff), "v"(v), "s"(base) : "memory");
}
template <int N> DI void wait_vm() { asm volatile("s_waitcnt vmcnt(%0)" ::"i"(N) : "memory"); }
template <int N> DI void vm_bar() { asm volatile("s_waitcnt vmcnt(%0)\n\ts_barrier" ::"i"(N) : "memory"); }
template <int N> DI void vm_lgkm_bar() { asm volatile("s_waitcnt vmcnt(%0) lgkmcnt(0)\n\ts_barrier" ::"i"(N) : "memory"); }
DI void bar() { asm volatile("s_barrier" ::: "memory"); }
template <class T> DI void pin(T& a) { asm volatile("" : "+v"(a)); }
DI bf16x4 tr4(LAS char* p) { return __builtin_amdgcn_ds_read_tr16_b64_v4bf16((LAS bf16x4*)p); }
DI bf16x8 cat(bf16x4 a, bf16x4 b) { return __builtin_shufflevector(a, b, 0, 1, 2, 3, 4, 5, 6, 7); }
DI f32x16 mfma32(bf16x8 a, bf16x8 b, f32x16 c) { return __builtin_amdgcn_mfma_f32_32x32x16_bf16(a, b, c, 0, 0, 0); }
DI f32x4 mfma16(bf16x8 a, bf16x8 b, f32x4 c) { return __builtin_amdgcn_mfma_f32_16x16x32_bf16(a, b, c, 0, 0, 0); }
DI float max3f(float a, float b, float c) { float r; asm("v_max3_f32 %0, %1, %2, %3" : "=v"(r) : "v"(a), "v"(b), "v"(c)); return r; }
DI float max2f(float a, float b) { float r; asm("v_max_f32_e32 %0, %1, %2" : "=v"(r) : "v"(a), "v"(b)); return r; }
template <int CTRL> DI float dppf(float v) { return __builtin_bit_cast(float, __builtin_amdgcn_update_dpp(0, __builtin_bit_cast(int, v), CTRL, 0xf, 0xf, true)); }
DI float sum8(float v) { v += dppf<0xB1>(v); v += dppf<0x4E>(v); v += dppf<0x141>(v); return v; }
DI f32x16 splat16(float v) { f32x16 r; for (int i = 0; i < 16; ++i) r[i] = v; return r; }

#define ST_DECL
#define ST(i)
#define ST_END(kid)
#define DBG_PARAM
#define DBG_ARG
#define PH_BEGIN
#define PH_DBG

constexpr float QSCALE = 0.08838834764831845f * 1.4426950408889634f;
constexpr int PRE_RQ = 144, PRE_MOD = 108, PRE_CLR = 64;
constexpr int PRE_LDS = 32768 + 256;
DI float silu_f(float cv) { return cv * __builtin_amdgcn_rcpf(1.f + __builtin_amdgcn_exp2f(-1.4426950408889634f * cv)); }
DI float sum_x8_16_32(float v) {
  v += dppf<0x128>(v);
  { auto rr = __builtin_amdgcn_permlane16_swap(__float_as_uint(v), __float_as_uint(v), false, false); v = __uint_as_float(rr[0]) + __uint_as_float(rr[1]); }
  { auto rr = __builtin_amdgcn_permlane32_swap(__float_as_uint(v), __float_as_uint(v), false, false); v = __uint_as_float(rr[0]) + __uint_as_float(rr[1]); }
  return v;
}
__global__ __launch_bounds__(512) void k_pre(const unsigned short* __restrict__ c, const unsigned short* __restrict__ wmod, const unsigned short* __restrict__ xin, const char* __restrict__ wq, char* __restrict__ ws DBG_PARAM) {
  ST_DECL
  extern __shared__ __attribute__((aligned(16))) char lds_[];
  LAS char* lds = (LAS char*)lds_;
  const int tid = threadIdx.x, lane = tid & 63, wid = rfl(tid >> 6);
  int b = blockIdx.x;
  if (wid == 7 && b < 224) {
    unsigned long long pc; int d0; unsigned len;
    asm volatile("s_getpc_b64 %0\n.Lkp_here:\n\ts_mov_b32 %1, .Lkm_beg-.Lkp_here\n\ts_mov_b32 %2, .Lkm_fin-.Lkm_beg" : "=s"(pc), "=s"(d0), "=s"(len));
    const unsigned per = (len / 128 + 27) / 28, l0 = (b >> 3) * per + lane;
    unsigned off = (lane < (int)per && l0 * 128 < len) ? l0 * 128 : 0;
    dma4(32768, off, (const void*)(pc + (long long)d0 - 256));
  }
  if (b < PRE_MOD) {
    LAS float* sl = (LAS float*)lds; LAS float* red = (LAS float*)(lds + 2304);
    const int rg = tid >> 2, col0 = b * 32 + (tid & 3) * 8;
    u32x4 w[5]; unsigned cw;
    asm volatile("global_load_dword %0, %1, %2" : "=v"(cw) : "v"((unsigned)(tid < 288 ? tid : 0) * 4), "s"(c));
#pragma unroll
    for (int i = 0; i < 5; ++i) { const int row = rg + 128 * i < 576 ? rg + 128 * i : rg; gload16<0>(w[i], (unsigned)(row * 3456 + col0) * 2, wmod); }
    asm volatile("s_waitcnt vmcnt(5)" : "+v"(cw));
    if (tid < 288) *(LAS f32x2*)(sl + 2 * tid) = (f32x2){silu_f(bflo(cw)), silu_f(bfhi(cw))};
    asm volatile("s_waitcnt vmcnt(0) lgkmcnt(0)\n\ts_barrier" ::: "memory");
    float acc[8] = {};
#pragma unroll
    for (int i = 0; i < 5; ++i) { pin(w[i]); const float s = rg + 128 * i < 576 ? sl[rg + 128 * i] : 0.f;
#pragma unroll
      for (int j = 0; j < 4; ++j) { acc[2 * j] += s * bflo(w[i][j]); acc[2 * j + 1] += s * bfhi(w[i][j]); } }
#pragma unroll
    for (int j = 0; j < 8; ++j) { float v = acc[j]; v += dppf<0x124>(v); acc[j] = sum_x8_16_32(v); }
    if (lane < 4) { *(LAS f32x4*)(red + wid * 32 + lane * 8) = (f32x4){acc[0], acc[1], acc[2], acc[3]}; *(LAS f32x4*)(red + wid * 32 + lane * 8 + 4) = (f32x4){acc[4], acc[5], acc[6], acc[7]}; }
    asm volatile("s_waitcnt lgkmcnt(0)\n\ts_barrier" ::: "memory");
    if (tid < 32) { float t = 0.f;
#pragma unroll
      for (int w2 = 0; w2 < 8; ++w2) t += red[w2 * 32 + tid];
      ((float*)(ws + WS_MOD))[b * 32 + tid] = t; }
    ST_END(5);
    return;
  }
  b -= PRE_MOD;
  if (b < PRE_CLR) {
    const int cc = lane & 15, row = (b * 8 + wid) * 4 + (lane >> 4);
    u32x4 xv[5];
#pragma unroll
    for (int i = 0; i < 5; ++i) xv[i] = *(const u32x4*)(xin + (size_t)row * D_ + (cc + 16 * i < 72 ? cc + 16 * i : cc) * 8);
    if (tid < 32) *(u32x4*)(ws + WS_CNT + (size_t)(b * 32 + tid) * 16) = (u32x4){0u, 0u, 0u, 0u};
#pragma unroll
    for (int i = 0; i < 5; ++i) { const unsigned o = (unsigned)(b * 36864 + i * 8192 + tid * 16); if (i < 4 || tid < 256) st16c(ws + WS_H1, o, (u32x4){~0u, ~0u, ~0u, ~0u}); }
    const bool last = cc < 8;
    float sm = 0.f;
#pragma unroll
    for (int i = 0; i < 5; ++i)
#pragma unroll
      for (int j = 0; j < 4; ++j) { const float t = bflo(xv[i][j]) + bfhi(xv[i][j]); sm += (i < 4 || last) ? t : 0.f; }
    sm = sum8(sm); sm += dppf<0x128>(sm);
    const float mean = sm * (1.f / 576.f);
    float q = 0.f;
#pragma unroll
    for (int i = 0; i < 5; ++i)
#pragma unroll
      for (int j = 0; j < 4; ++j) { const float d0 = bflo(xv[i][j]) - mean, d1 = bfhi(xv[i][j]) - mean; q += (i < 4 || last) ? d0 * d0 + d1 * d1 : 0.f; }
    q = sum8(q); q += dppf<0x128>(q);
    const float rstd = __builtin_amdgcn_rsqf(q * (1.f / 576.f) + 1e-5f);
    if (cc == 0) ((f32x2*)(ws + WS_STAT))[row] = (f32x2){mean, rstd};
    ST_END(6);
    return;
  }
  b -= PRE_CLR;
  {
    const int u = b * 8 + wid, s32 = u >> 6, cg = u & 63;
    { const int rr = lane >> 3, pp = lane & 7;
      const unsigned v = rr * 8192 + ((((pp >> 1) ^ ((rr >> 1) & 3)) << 1) + (pp & 1)) * 16;
      const char* src = wq + (size_t)s32 * 32 * 8192 + cg * 128; const unsigned m = wid * 4096;
      dma16<0>(m, v, src); dma16<0>(m + 1024, v, src + 8 * 8192); dma16<0>(m + 2048, v, src + 16 * 8192); dma16<0>(m + 3072, v, src + 24 * 8192); }
    wait_vm<0>();
    const int g = lane >> 4, q = (lane & 15) >> 2, p = lane & 3, h = lane >> 5, blk = g & 1, pc = ((p & 1) << 1) | (p >> 1);
    LAS char* scr = lds + wid * 4096;
#pragma unroll
    for (int dbl = 0; dbl < 2; ++dbl)
#pragma unroll
      for (int ks = 0; ks < 2; ++ks) { const int r0 = 16 * ks + 8 * h + q, r1 = r0 + 4, cx = 2 * dbl + blk;
        const bf16x8 w = cat(tr4(scr + 128 * r0 + 32 * (cx ^ ((r0 >> 1) & 3)) + 8 * pc), tr4(scr + 128 * r1 + 32 * (cx ^ ((r1 >> 1) & 3)) + 8 * pc));
        st16c(ws + WS_WQ, (unsigned)((((cg >> 1) * 36 + 2 * s32 + ks) * 4 + 2 * (cg & 1) + dbl) * 1024 + lane * 16), __builtin_bit_cast(u32x4, w)); }
    ST_END(4);
  }
}

constexpr float THR = 8.0f;
constexpr int A_TAB = 131072;
constexpr int A_LDS = 131072 + 2048;
#define A_LA 4
#define Q_LA 5
struct MainArgs { const char* kc; const char* vc; const char* wg; const char* wu; const char* wo; const char* wd; const unsigned short* x; unsigned short* out; char* ws; };
constexpr int P_STG = 9216, P_NSTG = 4, P_T = 73728;
constexpr int C_BASE = 81920;
constexpr int MAIN_LDS = 163840;
constexpr int CNT_CLEAR = 0;
constexpr int CNT_O = 8, CNT_H = 40;
DI void signal_done(char* ws, int idx, int wid) {
  vm_bar<0>();
  if (wid == 0 && lane_id() == 0) __hip_atomic_fetch_add((unsigned*)(ws + WS_CNT) + idx * 64, 1u, __ATOMIC_RELAXED, __HIP_MEMORY_SCOPE_AGENT);
}
DI void wait_count(const char* ws, int idx, unsigned target, int wid) {
  if (wid == 0) {
    const unsigned* cnt = (const unsigned*)(ws + WS_CNT) + idx * 64;
    for (unsigned spin = 0; spin < (1u << 15); ++spin) {
      if (rfl(__hip_atomic_load(cnt, __ATOMIC_RELAXED, __HIP_MEMORY_SCOPE_AGENT)) >= target) break;
      __builtin_amdgcn_s_sleep(1);
    }
  }
  bar();
}
DI void st_empty(char* base, unsigned voff) { st16c(base, voff, (u32x4){~0u, ~0u, ~0u, ~0u}); }
DI bool has_empty(const bf16x8& a, const bf16x8& b) {
  const u32x4 x = __builtin_bit_cast(u32x4, a), y = __builtin_bit_cast(u32x4, b);
  unsigned m = x[0] > x[1] ? x[0] : x[1]; m = m > x[2] ? m : x[2]; m = m > x[3] ? m : x[3]; m = m > y[0] ? m : y[0]; m = m > y[1] ? m : y[1]; m = m > y[2] ? m : y[2]; m = m > y[3] ? m : y[3];
  return __any(m == ~0u);
}
template <int IMM1> DI void revalidate(bf16x8& a, bf16x8& b, unsigned oa, unsigned ob, const char* base) {
  if (__builtin_expect(has_empty(a, b), 0)) {
    for (unsigned spin = 0; spin < (1u << 15); ++spin) {
      __builtin_amdgcn_s_sleep(3);
      asm volatile("global_load_dwordx4 %0, %2, %4 sc1\n\tglobal_load_dwordx4 %1, %3, %4 offset:%5 sc1\n\ts_waitcnt vmcnt(0)" : "+v"(a), "+v"(b) : "v"(oa), "v"(ob), "s"(base), "i"(IMM1) : "memory");
      if (!has_empty(a, b)) break;
    }
  }
}
DI void proj_prefetch(const char* W, int nst, int wid) {
  const int lane = lane_id();
  const int x = blockIdx.x & 7, j = blockIdx.x >> 3, ks = x >> 1, ct = j & 3, rg = wid & 3, kh = wid >> 2;
  unsigned wsrc3[3];
#pragma unroll
  for (int e = 0; e < 3; ++e) { const int o = 64 * e + lane, row = o / 18, ch = o - row * 18; wsrc3[e] = row * 1152 + ch * 16; }
  const char* Ww = W + (size_t)((ks * 2 + kh) * nst * 32 + rg * 8) * 1152 + ct * 288;
  const unsigned wdst = kh * (P_NSTG * P_STG) + rg * 2304;
#pragma unroll
  for (int s = 0; s < 3; ++s) { const char* wsrc = Ww + (size_t)s * 32 * 1152; const unsigned m = s * P_STG + wdst;
    dma16<0>(m, wsrc3[0], wsrc); dma16<0>(m + 1024, wsrc3[1], wsrc); if (lane < 16) dma16<0>(m + 2048, wsrc3[2], wsrc); }
}
DI void phase_attn(const MainArgs& ar, LAS char* lds, const int wid DBG_PARAM) {
  PH_BEGIN
  char* const ws = ar.ws;
  const int lane = lane_id(), tid = wid * 64 + lane;
  const int x = blockIdx.x & 7, j = blockIdx.x >> 3, head = 4 * x + (j & 3), rt = j >> 2, kvh = head >> 1;
  const int rb = rt * 8 + wid;
  const int prb = 2 * (j >> 2) + (x & 1), pown = (j & 3) * 4 + (x >> 1);
  const int h = lane >> 5, r = lane & 31, blk = (lane >> 4) & 1, q = (lane & 15) >> 2, p = lane & 3;
  const int l4 = lane >> 4, pos = lane & 15;
  unsigned ksrc2[2];
#pragma unroll
  for (int e = 0; e < 2; ++e) { const int n = 2 * wid + e, row = 4 * n + l4, ch = pos ^ ((l4 << 2) | (n & 3)); ksrc2[e] = row * 256 + ch * 16 - e * 1024; }
  const char* wq_h = ws + WS_WQ + (size_t)head * 36 * 4096 + wid * 2048;
  const char* kb_ = ar.kc + ((size_t)kvh * SK + (SK - WIN)) * 256;
  const char* vb_ = ar.vc + ((size_t)kvh * SK + (SK - WIN)) * 256;
  const char* h1w = ws + WS_H1 + (size_t)rb * 36 * 1024;
  const unsigned lane16 = lane * 16;
  auto dmaW = [&](int qq, int e) { const unsigned m = 65536 + (qq & 3) * 16384 + wid * 2048; if (e == 0) dma16<0>(m, lane16, wq_h + qq * 16384); else dma16<1024>(m, lane16, wq_h + qq * 16384); };
  auto dmaK8 = [&](int t) { const unsigned m = (t & 3) * 16384 + wid * 2048; const char* s = kb_ + t * 16384; dma16<0>(m, ksrc2[0], s); dma16<1024>(m, ksrc2[1], s); };
  auto dmaV8 = [&](int t) { const unsigned m = 65536 + ((t + 1) & 3) * 16384 + wid * 2048; const char* s = vb_ + t * 16384; dma16<0>(m, ksrc2[0], s); dma16<1024>(m, ksrc2[1], s); };
  bf16x8 g[9][4];
  auto loadG1 = [&](auto QQ, auto E) { constexpr int qq = QQ, e = E; gload16c<e * 1024>(g[qq][e], lane16, h1w + qq * 4096); };
  auto checkG = [&](auto QQ) { constexpr int qq = QQ; static_for<4>([&](auto E) { pin(g[qq][E]); });
    revalidate<1024>(g[qq][0], g[qq][1], lane16, lane16, h1w + qq * 4096); revalidate<1024>(g[qq][2], g[qq][3], lane16, lane16, h1w + qq * 4096 + 2048); };
  auto loadG = [&](auto QQ) { static_for<4>([&](auto E) { loadG1(QQ, E); }); };
  auto mark = [&](auto MI) { constexpr int mi = MI;
    if constexpr (mi < 2) { const int rown = prb * 128 + pown * 8 + (tid & 7), kc = mi == 0 ? (tid >> 3) : 64 + (lane >> 3);
      st_empty(ws + WS_H2, (unsigned)(((rown >> 5) * 36 + (kc >> 1)) * 1024 + ((kc & 1) * 32 + (rown & 31)) * 16)); }
    else { const int n = tid + 512 * (mi - 2);
      st_empty(ws + WS_PB + (size_t)(prb * 16 + pown) * 36864, (unsigned)(n * 16 + (n < 2304 ? 0 : (unsigned)(WS_PD - WS_PB) - 36864u))); } };
  {
    const int row = rt * 256 + (8 * (j & 3) + x) * 8 + (tid & 7), u0 = tid >> 3, u1 = 64 + (lane >> 3);
    u32x4 xv[2]; f32x4 sa[2][2], ca[2][2]; f32x2 mr;
    asm volatile("global_load_dwordx2 %0, %1, %2" : "=v"(mr) : "v"((unsigned)row * 8), "s"(ws + WS_STAT));
    gload16<0>(xv[0], (unsigned)(row * D_ + u0 * 8) * 2, ar.x); gload16<0>(xv[1], (unsigned)(row * D_ + u1 * 8) * 2, ar.x);
#pragma unroll
    for (int i = 0; i < 2; ++i) { const unsigned o = (i ? u1 : u0) * 32; gload16<0>(sa[i][0], o, ws + WS_MOD); gload16<16>(sa[i][1], o, ws + WS_MOD); gload16<0>(ca[i][0], o, ws + WS_MOD + 2304); gload16<16>(ca[i][1], o, ws + WS_MOD + 2304); }
    dmaW(0, 0); dmaW(0, 1); dmaW(1, 0); dmaW(1, 1); dmaW(2, 0); dmaW(2, 1); dmaK8(0);
    wait_vm<8>(); pin(mr);
    const float rq = mr[1] * QSCALE;
#pragma unroll
    for (int i = 0; i < 2; ++i) { pin(xv[i]); pin(sa[i][0]); pin(sa[i][1]); pin(ca[i][0]); pin(ca[i][1]);
      const f32x4 v0 = {bflo(xv[i][0]), bfhi(xv[i][0]), bflo(xv[i][1]), bfhi(xv[i][1])}, v1 = {bflo(xv[i][2]), bfhi(xv[i][2]), bflo(xv[i][3]), bfhi(xv[i][3])};
      const f32x4 h0 = (v0 - mr[0]) * rq * (ca[i][0] + 1.f) + sa[i][0] * QSCALE, h1v = (v1 - mr[0]) * rq * (ca[i][1] + 1.f) + sa[i][1] * QSCALE; const int u = i ? u1 : u0;
      st16c(ws + WS_H1, (unsigned)(((row >> 5) * 36 + (u >> 1)) * 1024 + ((u & 1) * 32 + (row & 31)) * 16), (u32x4){pk2(h0[0], h0[1]), pk2(h0[2], h0[3]), pk2(h1v[0], h1v[1]), pk2(h1v[2], h1v[3])}); }
  }
  ST(10);
#define H1_SLEEP 16
  __builtin_amdgcn_s_sleep(H1_SLEEP);
  for (unsigned spin = 0; spin < (1u << 15); ++spin) {
    loadG(integral_constant<int, 0>{}); wait_vm<0>();
    static_for<4>([&](auto E) { pin(g[0][E]); });
    if (!has_empty(g[0][0], g[0][1]) && !has_empty(g[0][2], g[0][3])) break;
    __builtin_amdgcn_s_sleep(2);
  }
  ST(11);
  loadG(integral_constant<int, 1>{}); loadG(integral_constant<int, 2>{});
  unsigned wb0 = 65536 + lane16;
  pin(wb0);
  f32x16 acc[4] = {};
  {
    constexpr int LA = Q_LA, NIT = 9 * 16;
    bf16x8 wf[LA + 1];
    auto rd = [&](auto I) { constexpr int i = I; constexpr int qq = i / 16, ks = (i % 16) / 4, db = i % 4;
      wf[i % (LA + 1)] = *(LAS bf16x8*)(lds + wb0 + (qq & 3) * 16384 + ks * 4096 + db * 1024); };
    SB(); bar();
    SB();
    ST(1);
    static_for<LA>([&](auto I) { rd(I); });
    SB();
    static_for<NIT>([&](auto I) { constexpr int i = I; constexpr int qq = i / 16, rr = i % 16, ks = rr / 4, db = rr % 4;
      if constexpr (rr == 0 && qq > 0) {
        constexpr int NW[9] = {8, 10, 10, 10, 10, 10, 10, 8, 8};
        vm_bar<NW[qq]>();
        checkG(integral_constant<int, qq>{});
        if constexpr (qq == 4) ST(2);
        SB();
      }
      if constexpr (i + LA < NIT) rd(integral_constant<int, i + LA>{});
      acc[db] = mfma32(wf[i % (LA + 1)], g[qq][ks], acc[db]);
      if constexpr (qq + 3 < 9) {
        if constexpr (rr == 1) dmaW(qq + 3, 0);
        if constexpr (rr == 3) dmaW(qq + 3, 1);
        if constexpr (rr >= 5 && rr <= 11 && (rr & 1)) loadG1(integral_constant<int, qq + 3>{}, integral_constant<int, (rr - 5) / 2>{});
      } else {
        if constexpr (rr == 1) dmaV8(qq - 6);
        if constexpr (rr == 7) dmaK8(qq - 5);
      }
      SB();
    });
  }
  ST(3);
  bf16x8 qf[8];
#pragma unroll
  for (int db = 0; db < 4; ++db)
#pragma unroll
    for (int s = 0; s < 2; ++s) { u32x4 w;
#pragma unroll
      for (int i = 0; i < 4; ++i) w[i] = pk2(acc[db][8 * s + 2 * i], acc[db][8 * s + 2 * i + 1]);
      qf[2 * db + s] = __builtin_bit_cast(bf16x8, w); }
  const unsigned kb0 = 256 * r + 16 * (h ^ (((r & 3) << 2) | ((r >> 2) & 3)));
  const unsigned vb0 = 65536 + 256 * (4 * h + q) + 16 * ((q << 2) | ((2 * blk + (p >> 1)) ^ h)) + 8 * (p & 1);
  unsigned ka[8], va[4][2];
#pragma unroll
  for (int s = 0; s < 8; ++s) ka[s] = kb0 ^ (s << 5);
#pragma unroll
  for (int db = 0; db < 4; ++db) { va[db][0] = vb0 ^ (db << 6); va[db][1] = vb0 ^ (db << 6) ^ 32; }
  f32x16 o[4] = {}; f32x16 S[2][2], negm; unsigned pw[16]; float m; f32x2 l2 = {0.f, 0.f};
  constexpr int LA = A_LA;
  bf16x8 fr[LA + 1];
  struct Map { int g[272]; int n; };
  constexpr Map MP = []() constexpr { Map mp{}; mp.n = 0; for (int G = 0; G < 9 * 32; ++G) { const int t = G / 32 - 1, i = G % 32; if (i < 16 ? (t + 1 <= 7) : (t >= 0 && t <= 7)) mp.g[mp.n++] = G; } return mp; }();
  constexpr int NIT = MP.n;
  auto rdA = [&](auto C) { constexpr int c = C; constexpr int G = MP.g[c], t = G / 32 - 1, i = G % 32;
    if constexpr (i < 16) { constexpr int s = i >> 1, kb = i & 1; fr[c % (LA + 1)] = *(LAS bf16x8*)(lds + ka[s] + ((t + 1) & 3) * 16384 + kb * 8192); }
    else { constexpr int ii = i - 16, kk = ii >> 2, db = ii & 3; constexpr int so = ((t + 1) & 3) * 16384 + kk * 4096;
      fr[c % (LA + 1)] = cat(tr4(lds + va[db][0] + so), tr4(lds + va[db][1] + so + 2048)); } };
  auto rowmax_finish = [&](float a) { auto rr = __builtin_amdgcn_permlane32_swap(__float_as_uint(a), __float_as_uint(a), false, false); return max2f(__uint_as_float(rr[0]), __uint_as_float(rr[1])); };
  float mxa, mxb;
  const unsigned gw = blockIdx.x * 8 + wid;
  auto touch = [&](auto TI) { constexpr int ti = TI; constexpr int m = ti >> 1, half = ti & 1;
    constexpr unsigned per = m == 0 ? 9 : 11, tot = m == 0 ? 36864 : 41472;
    unsigned line = (gw * 2 + half) * per + (lane < (int)per ? lane : 0); if (line >= tot) line = tot - 1;
    dma4(A_TAB + 2048 + wid * 256, line * 128, m == 0 ? ar.wo : (m == 1 ? ar.wg : (m == 2 ? ar.wu : ar.wd))); };
  auto background = [&](auto BI) { constexpr int bi = BI;
    if constexpr ((bi & 3) == 0) touch(integral_constant<int, (bi >> 2) < 2 ? (bi >> 2) : (bi >> 2) + 4>{}); else mark(integral_constant<int, (bi >> 2) * 3 + (bi & 3) - 1>{}); };
  SB(); vm_bar<8>(); SB();
  ST(4);
  static_for<LA>([&](auto C) { rdA(C); });
  SB();
  static_for<NIT>([&](auto C) { constexpr int c = C; constexpr int G = MP.g[c], t = G / 32 - 1, i = G % 32;
    constexpr bool firstX = (i == 0) || (t == 7 && i == 16);
    if constexpr (firstX && t >= 0) {
      constexpr int NW[8] = {4, 8, 12, 12, 11, 5, 0, 0};
      float a = rowmax_finish(max2f(mxa, mxb));
      if constexpr (t == 0) {
        m = a; negm = splat16(-a);
#pragma unroll
        for (int r2 = 0; r2 < 16; ++r2) { S[0][0][r2] -= a; S[0][1][r2] -= a; }
      } else if (__builtin_expect(__any(a > THR), 0)) {
        const float dl = fmaxf(a, 0.f), f = __builtin_amdgcn_exp2f(-dl);
        m += dl; negm = splat16(-m); l2 *= f;
#pragma unroll
        for (int r2 = 0; r2 < 16; ++r2) { S[t & 1][0][r2] -= dl; S[t & 1][1][r2] -= dl; }
#pragma unroll
        for (int d = 0; d < 4; ++d)
#pragma unroll
          for (int r2 = 0; r2 < 16; ++r2) o[d][r2] *= f;
      }
      SB(); vm_bar<NW[t]>(); SB();
      if constexpr (t == 7) { if (wid == 0 && lane == 0) __hip_atomic_fetch_add((unsigned*)(ws + WS_CNT) + (CNT_CLEAR + rt) * 64, 1u, __ATOMIC_RELAXED, __HIP_MEMORY_SCOPE_AGENT); }
      if constexpr (t == 3) ST(5);
      if constexpr (t == 4) ST(7);
      if constexpr (t == 5) ST(8);
      if constexpr (t == 7) proj_prefetch(ar.wo, 16, wid);
      if constexpr (t == 7) {
#pragma unroll
        for (int e = 0; e < 32; ++e) S[1][e >> 4][e & 15] = __builtin_amdgcn_exp2f(S[1][e >> 4][e & 15]);
#pragma unroll
        for (int e = 0; e < 16; ++e) pw[e] = pk2(S[1][e >> 3][(2 * e) & 15], S[1][e >> 3][((2 * e) & 15) + 1]);
        SB();
      }
    }
    if constexpr (c + LA < NIT) rdA(integral_constant<int, c + LA>{});
    if constexpr (i < 16) {
      constexpr int s = i >> 1, kb = i & 1, X = (t + 1) & 1;
      if constexpr (s == 0) { if constexpr (t < 0) S[X][kb] = mfma32(fr[c % (LA + 1)], qf[s], f32x16{}); else S[X][kb] = mfma32(fr[c % (LA + 1)], qf[s], negm); }
      else S[X][kb] = mfma32(fr[c % (LA + 1)], qf[s], S[X][kb]);
      if constexpr (t >= 0) {
        constexpr int e0 = 2 * i, e1 = 2 * i + 1, Y = t & 1;
        S[Y][e0 >> 4][e0 & 15] = __builtin_amdgcn_exp2f(S[Y][e0 >> 4][e0 & 15]);
        S[Y][e1 >> 4][e1 & 15] = __builtin_amdgcn_exp2f(S[Y][e1 >> 4][e1 & 15]);
        if constexpr (i >= 1) { constexpr int e = i - 1; pw[e] = pk2(S[Y][e >> 3][(2 * e) & 15], S[Y][e >> 3][((2 * e) & 15) + 1]); }
        if constexpr (i == 15) { SB(); pw[15] = pk2(S[Y][1][14], S[Y][1][15]); }
        if constexpr (t <= 3 && i == 6) dma16<0>((t & 3) * 16384 + wid * 2048, ksrc2[0], kb_ + (t + 4) * 16384);
        if constexpr (t <= 3 && i == 9) dma16<1024>((t & 3) * 16384 + wid * 2048, ksrc2[1], kb_ + (t + 4) * 16384);
        if constexpr (t <= 4 && i == 12) dma16<0>(65536 + (t & 3) * 16384 + wid * 2048, ksrc2[0], vb_ + (t + 3) * 16384);
        if constexpr (t <= 4 && i == 15) dma16<1024>(65536 + (t & 3) * 16384 + wid * 2048, ksrc2[1], vb_ + (t + 3) * 16384);
      }
      if constexpr (t < 0 && i == 15) {
        asm volatile("s_nop 11" : "+v"(S[0][0]), "+v"(S[0][1]));
        mxa = max3f(S[0][0][0], S[0][0][1], S[0][0][2]); mxb = max3f(S[0][1][0], S[0][1][1], S[0][1][2]);
#pragma unroll
        for (int r2 = 3; r2 < 15; r2 += 2) { mxa = max3f(mxa, S[0][0][r2], S[0][0][r2 + 1]); mxb = max3f(mxb, S[0][1][r2], S[0][1][r2 + 1]); }
        mxa = max3f(mxa, S[0][0][15], S[0][1][15]);
      }
    } else {
      constexpr int ii = i - 16, kk = ii >> 2, db = ii & 3, Y = t & 1, X = (t + 1) & 1;
      const u32x4 w = {pw[4 * kk], pw[4 * kk + 1], pw[4 * kk + 2], pw[4 * kk + 3]};
      o[db] = mfma32(fr[c % (LA + 1)], __builtin_bit_cast(bf16x8, w), o[db]);
      if constexpr (t <= 3 && (ii & 3) == 1 && 4 * t + (ii >> 2) < 15) background(integral_constant<int, 4 * t + (ii >> 2)>{});
      l2 += (f32x2){S[Y][ii >> 3][(2 * ii) & 15], S[Y][ii >> 3][((2 * ii) & 15) + 1]};
      if constexpr (t < 7) {
        if constexpr (ii == 4) { mxa = max3f(S[X][0][0], S[X][0][1], S[X][0][2]); mxb = max3f(S[X][0][3], S[X][0][4], S[X][0][5]); }
        if constexpr (ii == 5) { mxa = max3f(mxa, S[X][0][6], S[X][0][7]); mxb = max3f(mxb, S[X][0][8], S[X][0][9]); }
        if constexpr (ii == 6) { mxa = max3f(mxa, S[X][0][10], S[X][0][11]); mxb = max3f(mxb, S[X][0][12], S[X][0][13]); }
        if constexpr (ii == 7) { mxa = max3f(mxa, S[X][0][14], S[X][0][15]); mxb = max3f(mxb, S[X][1][0], S[X][1][1]); }
        if constexpr (ii == 8) { mxa = max3f(mxa, S[X][1][2], S[X][1][3]); mxb = max3f(mxb, S[X][1][4], S[X][1][5]); }
        if constexpr (ii == 9) { mxa = max3f(mxa, S[X][1][6], S[X][1][7]); mxb = max3f(mxb, S[X][1][8], S[X][1][9]); }
        if constexpr (ii == 10) { mxa = max3f(mxa, S[X][1][10], S[X][1][11]); mxb = max3f(mxb, S[X][1][12], S[X][1][13]); }
        if constexpr (ii == 11) { mxa = max3f(mxa, S[X][1][14], S[X][1][15]); }
      }
    }
    SB();
  });
  ST(12);
  float l = l2[0] + l2[1];
  { auto rr = __builtin_amdgcn_permlane32_swap(__float_as_uint(l), __float_as_uint(l), false, false); l = __uint_as_float(rr[0]) + __uint_as_float(rr[1]); }
  const float inv = __builtin_amdgcn_rcpf(l);
  char* ob = ws + WS_O + ((size_t)rb * 256 + head * 8) * 1024;
#pragma unroll
  for (int db = 0; db < 4; ++db)
#pragma unroll
    for (int s = 0; s < 2; ++s) { u32x4 w;
#pragma unroll
      for (int i = 0; i < 4; ++i) w[i] = pk2(o[db][8 * s + 2 * i] * inv, o[db][8 * s + 2 * i + 1] * inv);
      st16c(ob, lane16 + (2 * db + s) * 1024, w); }
  signal_done(ws, CNT_O + rt * 4 + (head >> 3), wid);
  ST_END(0);
}

#define P_LA 4
DI void ffn1_prefetch(const MainArgs& ar, int wid);
template <bool FINAL>
DI void phase_proj(const MainArgs& ar, LAS char* lds, const int wid DBG_PARAM) {
  PH_BEGIN
  char* const ws = ar.ws; const char* W = FINAL ? ar.wd : ar.wo; const unsigned short* xin = ar.x; unsigned short* out = ar.out;
  constexpr int NS16 = FINAL ? 288 : 256, NST = NS16 / 16;
  const char* act = ws + (FINAL ? WS_H : WS_O);
  const float* mod = (const float*)(ws + WS_MOD);
  char* part = ws + (FINAL ? WS_PD : WS_PB);
  const int lane = lane_id(), tid = wid * 64 + lane, rg = wid & 3, kh = wid >> 2;
  const int x = blockIdx.x & 7, j = blockIdx.x >> 3, ks = x >> 1, rb = 2 * (j >> 2) + (x & 1), ct = j & 3;
  const int own = ct * 4 + ks;
  const int g = lane >> 4, m16 = lane & 15, q4 = (lane & 15) >> 2, p = lane & 3;
  const int kb32 = (ks * 2 + kh) * NST;
  unsigned aoff[2];
#pragma unroll
  for (int a = 0; a < 2; ++a) { const int row = rb * 128 + rg * 32 + a * 16 + m16; aoff[a] = (unsigned)(row >> 5) * (NS16 * 1024) + (row & 31) * 16 + (g >> 1) * 1024 + (g & 1) * 512; }
  const char* actw = act + (size_t)kb32 * 2048;
  unsigned wsrc3[3];
#pragma unroll
  for (int e = 0; e < 3; ++e) { const int o = 64 * e + lane, row = o / 18, ch = o - row * 18; wsrc3[e] = row * 1152 + ch * 16; }
  const char* Ww = W + (size_t)(kb32 * 32 + rg * 8) * 1152 + ct * 288;
  const unsigned wdst = kh * (P_NSTG * P_STG) + rg * 2304;
  bf16x8 af[NST][2];
  auto issue_piece = [&](auto S, auto E) { constexpr int s = S, e = E;
    const char* wsrc = Ww + (size_t)s * 32 * 1152; const unsigned m = (s % P_NSTG) * P_STG + wdst;
    if constexpr (e == 0) dma16<0>(m, wsrc3[0], wsrc);
    if constexpr (e == 1) dma16<0>(m + 1024, wsrc3[1], wsrc);
    if constexpr (e == 2) { if (lane < 16) dma16<0>(m + 2048, wsrc3[2], wsrc); }
    if constexpr (e == 3) gload16c<0>(af[s][0], aoff[0], actw + s * 2048);
    if constexpr (e == 4) gload16c<0>(af[s][1], aoff[1], actw + s * 2048); };
  if (!FINAL && wid != 0) { ffn1_prefetch(ar, wid); if (wid == 1) ffn1_prefetch(ar, 0); }
  wait_count(ws, (FINAL ? CNT_H : CNT_O) + (rb >> 1) * 4 + ks, 8u, wid);
  static_for<3>([&](auto S) { issue_piece(S, integral_constant<int, 3>{}); issue_piece(S, integral_constant<int, 4>{}); });
  unsigned tb[P_NSTG];
#pragma unroll
  for (int i = 0; i < P_NSTG; ++i) tb[i] = kh * (P_NSTG * P_STG) + i * P_STG + 288 * (16 * (g >> 1) + 4 * (g & 1) + q4) + 8 * p;
  f32x4 acc[2][9] = {};
  constexpr int LA = P_LA, NIT = NST * 9;
  bf16x8 wf[LA + 1];
  auto rd = [&](auto I) { constexpr int i = I; constexpr int s = i / 9, c = i % 9;
    wf[i % (LA + 1)] = cat(tr4(lds + tb[s % P_NSTG] + 32 * c), tr4(lds + tb[s % P_NSTG] + 288 * 8 + 32 * c)); };
  constexpr int NPF = 0, NTB = FINAL ? 3 : 2;
  constexpr int TB = FINAL ? 81920 : 137600;
  SB(); vm_bar<4 + NPF>(); pin(af[0][0]); pin(af[0][1]);
#pragma unroll
  for (int e = 0; e < NTB; ++e) { const int n = NTB * wid + e;
    if (!FINAL) dma16<0>(TB + n * 1024, lane * 16, n < 7 ? (const char*)(mod + 1152) + n * 1024 : (const char*)xin + (size_t)(rb * 128 + own * 8) * 1152 + (n - 7) * 1024);
    else { const int n2 = n < 21 ? n : 0; dma16<0>(TB + n2 * 1024, lane * 16, n2 < 3 ? (const char*)(mod + 2880) + n2 * 1024 : ws + WS_X1R + (size_t)blockIdx.x * 576 * 32 + (n2 - 3) * 1024); } }
  SB();
  ST(1);
  static_for<LA>([&](auto I) { rd(I); });
  SB();
  static_for<NIT>([&](auto I) { constexpr int i = I; constexpr int s = i / 9, c = i % 9;
    if constexpr (c == 0 && s > 0) {
      constexpr int outst = s == 1 ? 2 + NPF + NTB + 5 : ((s + 2 < NST) ? 5 : 0);
      vm_bar<outst>(); pin(af[s][0]); pin(af[s][1]);
      if constexpr (s == 8) ST(2);
      SB();
    }
    if constexpr (i + LA < NIT) rd(integral_constant<int, i + LA>{});
    acc[0][c] = mfma16(wf[i % (LA + 1)], af[s][0], acc[0][c]);
    acc[1][c] = mfma16(wf[i % (LA + 1)], af[s][1], acc[1][c]);
    if constexpr (s + 3 < NST && c >= 1 && c <= 5) issue_piece(integral_constant<int, s + 3>{}, integral_constant<int, c - 1>{});
    SB();
  });
  ST(5);
  const int r8 = tid & 7, rown = rb * 128 + own * 8 + r8;
  int colA[2], colB[2];
  colA[0] = 144 * (tid >> 7) + 32 * ((tid >> 5) & 3) + 4 * ((tid >> 3) & 3); colB[0] = colA[0] + 16;
  colA[1] = 288 * ((tid >> 5) & 1) + 128 + 4 * ((tid >> 3) & 3); colB[1] = colA[1] + 144;
  unsigned marked;
  if (!FINAL) asm volatile("global_load_dword %0, %1, %2 sc1" : "=v"(marked) : "v"(0u), "s"(ws + WS_CNT + (size_t)(CNT_CLEAR + (rb >> 1)) * 256));
  {
    LAS char* exw = lds + (rg * 2 + kh) * 9216 + lane * 16; LAS char* exr = lds + (rg * 2 + (kh ^ 1)) * 9216 + lane * 16;
    bar();
    ST(3);
    f32x4 fin[9];
    if (kh) {
#pragma unroll
      for (int c = 0; c < 9; ++c) { *(LAS f32x4*)(exw + c * 1024) = acc[0][c]; fin[c] = acc[1][c]; }
    } else {
#pragma unroll
      for (int c = 0; c < 9; ++c) { *(LAS f32x4*)(exw + c * 1024) = acc[1][c]; fin[c] = acc[0][c]; }
    }
    asm volatile("s_waitcnt lgkmcnt(0)\n\ts_barrier" ::: "memory");
    ST(4);
    char* pw = part + (size_t)(rb * 16 + own) * 36864;
#pragma unroll
    for (int c = 0; c < 9; ++c) fin[c] += *(LAS f32x4*)(exr + c * 1024);
    const unsigned jo = (unsigned)(rg * 4 + kh * 2 + (m16 >> 3)) * 2304, lo = g * 8 + (m16 & 7);
    u32x4 cw[4];
#pragma unroll
    for (int cp = 0; cp < 4; ++cp) cw[cp] = (u32x4){pk2(fin[2 * cp][0], fin[2 * cp][1]), pk2(fin[2 * cp][2], fin[2 * cp][3]), pk2(fin[2 * cp + 1][0], fin[2 * cp + 1][1]), pk2(fin[2 * cp + 1][2], fin[2 * cp + 1][3])};
    const u32x2 w8 = {pk2(fin[8][0], fin[8][1]), pk2(fin[8][2], fin[8][3])};
#pragma unroll
    for (int cp = 0; cp < 4; ++cp) st16c(pw, jo + (cp * 32 + lo) * 16, cw[cp]);
    asm volatile("global_store_dwordx2 %0, %1, %2" STF "\n\ts_nop 1" ::"v"(jo + 2048 + lo * 8), "v"(w8), "s"(pw) : "memory");
  }
  ST(7);
  const char* pr = part + (size_t)rb * 16 * 36864 + own * 2304;
  const bool two = wid == 0;
  u32x4 pv[4]; u32x2 pq[4][2];
  if (!FINAL) {
    asm volatile("s_waitcnt vmcnt(5)" : "+v"(marked) :: "memory");
    if (!__all(marked >= 32u)) {
      const unsigned* cnt = (const unsigned*)(ws + WS_CNT) + (CNT_CLEAR + (rb >> 1)) * 64;
      for (unsigned spin = 0; spin < (1u << 15); ++spin) { if (__all(__hip_atomic_load(cnt, __ATOMIC_RELAXED, __HIP_MEMORY_SCOPE_AGENT) >= 32u)) break; __builtin_amdgcn_s_sleep(1); }
    }
  }
#define POLLMODE 1
#define P_SLEEP 20
  if (wid == 7) {
    for (unsigned spin = 0; spin < (1u << 15); ++spin) {
      unsigned t; asm volatile("global_load_dword %0, %1, %2 sc1\n\ts_waitcnt vmcnt(0)" : "=&v"(t) : "v"((unsigned)((lane & 15) * 36864 + 2300)), "s"(pr) : "memory");
      if (__all(t != ~0u)) break;
      __builtin_amdgcn_s_sleep(1);
    } }
  bar();
  ST(8);
  for (unsigned spin = 0; spin < (1u << 15); ++spin) {
    const unsigned o0 = (unsigned)((tid >> 7) * 4 * 36864 + (tid & 127) * 16), o1 = (unsigned)(((tid >> 5) & 1) * 8 * 36864 + 2048 + (tid & 31) * 8);
#pragma unroll
    for (int k = 0; k < 4; ++k) gload16c<0>(pv[k], o0 + k * 36864, pr);
    if (two) {
#pragma unroll
      for (int k = 0; k < 4; ++k) {
        asm volatile("global_load_dwordx2 %0, %1, %2" LDF : "=v"(pq[k][0]) : "v"(o1 + k * 36864), "s"(pr));
        asm volatile("global_load_dwordx2 %0, %1, %2" LDF : "=v"(pq[k][1]) : "v"(o1 + (4 + k) * 36864), "s"(pr)); } }
    wait_vm<0>();
    bool full = true;
#pragma unroll
    for (int k = 0; k < 4; ++k) { pin(pv[k]); full &= pv[k][0] != ~0u && pv[k][1] != ~0u && pv[k][2] != ~0u && pv[k][3] != ~0u; }
    if (two) {
#pragma unroll
      for (int k = 0; k < 4; ++k) { pin(pq[k][0]); pin(pq[k][1]); full &= pq[k][0][0] != ~0u && pq[k][0][1] != ~0u && pq[k][1][0] != ~0u && pq[k][1][1] != ~0u; } }
    if (__all(full)) break;
    __builtin_amdgcn_s_sleep(4);
  }
  ST(9);
  f32x4 v[2][2], sc[2][2], sh[2][2];
#pragma unroll
  for (int i = 0; i < 2; ++i) if (i == 0 || two) {
#pragma unroll
    for (int hh = 0; hh < 2; ++hh) { const int col = hh ? colB[i] : colA[i]; f32x4 rs;
      const f32x4 gt = *(LAS f32x4*)(lds + TB + col * 4);
      if (FINAL) rs = *(LAS f32x4*)(lds + TB + 3072 + (i ? 512 + (tid & 63) : tid) * 32 + hh * 16);
      else { const u32x2 xr = *(LAS u32x2*)(lds + TB + 7168 + r8 * 1152 + col * 2); rs = (f32x4){bflo(xr[0]), bfhi(xr[0]), bflo(xr[1]), bfhi(xr[1])};
        sh[i][hh] = *(LAS f32x4*)(lds + TB + (576 + col) * 4); sc[i][hh] = *(LAS f32x4*)(lds + TB + (1152 + col) * 4); }
      f32x4 t = {0.f, 0.f, 0.f, 0.f};
#pragma unroll
      for (int k = 0; k < 4; ++k) { const unsigned w0 = i ? pq[k][hh][0] : pv[k][2 * hh], w1 = i ? pq[k][hh][1] : pv[k][2 * hh + 1]; t += (f32x4){bflo(w0), bfhi(w0), bflo(w1), bfhi(w1)}; }
      v[i][hh] = rs + gt * t; }
  }
  ST(10);
  if (FINAL) {
#pragma unroll
    for (int i = 0; i < 2; ++i) if (i == 0 || two) {
      *(u32x2*)(out + (size_t)rown * D_ + colA[i]) = (u32x2){pk2(v[i][0][0], v[i][0][1]), pk2(v[i][0][2], v[i][0][3])};
      *(u32x2*)(out + (size_t)rown * D_ + colB[i]) = (u32x2){pk2(v[i][1][0], v[i][1][1]), pk2(v[i][1][2], v[i][1][3])}; }
  } else {
    LAS float* st = (LAS float*)(lds + P_T);
    auto s8 = [](const f32x4& a, const f32x4& b) { return ((a[0] + a[1]) + (a[2] + a[3])) + ((b[0] + b[1]) + (b[2] + b[3])); };
    float s = s8(v[0][0], v[0][1]) + (two ? s8(v[1][0], v[1][1]) : 0.f);
    s = sum_x8_16_32(s);
    if (lane < 8) st[wid * 8 + lane] = s;
    asm volatile("s_waitcnt lgkmcnt(0)\n\ts_barrier" ::: "memory");
    float tot = 0.f;
#pragma unroll
    for (int w = 0; w < 8; ++w) tot += st[w * 8 + r8];
    const float mean = tot * (1.f / 576.f);
    f32x4 d[2][2];
#pragma unroll
    for (int i = 0; i < 2; ++i) { d[i][0] = v[i][0] - mean; d[i][1] = v[i][1] - mean; }
    float qq = s8(d[0][0] * d[0][0], d[0][1] * d[0][1]) + (two ? s8(d[1][0] * d[1][0], d[1][1] * d[1][1]) : 0.f);
    qq = sum_x8_16_32(qq);
    if (lane < 8) st[64 + wid * 8 + lane] = qq;
    asm volatile("s_waitcnt lgkmcnt(0)\n\ts_barrier" ::: "memory");
    float tq = 0.f;
#pragma unroll
    for (int w = 0; w < 8; ++w) tq += st[64 + w * 8 + r8];
    const float rstd = __builtin_amdgcn_rsqf(tq * (1.f / 576.f) + 1e-5f);
#pragma unroll
    for (int i = 0; i < 2; ++i) if (i == 0 || two) {
      const f32x4 h0 = d[i][0] * rstd * (sc[i][0] + 1.f) + sh[i][0], h1 = d[i][1] * rstd * (sc[i][1] + 1.f) + sh[i][1];
      const int kc = i ? 64 + (tid >> 3) : (tid >> 3);
      st16c(ws + WS_H2, (unsigned)(((rown >> 5) * 36 + (kc >> 1)) * 1024 + ((kc & 1) * 32 + (rown & 31)) * 16), (u32x4){pk2(h0[0], h0[1]), pk2(h0[2], h0[3]), pk2(h1[0], h1[1]), pk2(h1[2], h1[3])});
      char* xb = ws + WS_X1R + (size_t)blockIdx.x * 576 * 32 + (i ? 512 + (tid & 63) : tid) * 32;
      *(f32x4*)xb = v[i][0]; *(f32x4*)(xb + 16) = v[i][1]; }
  }
  ST_END(FINAL ? 3 : 1);
}

constexpr int C_STG = 9216 + 128 + 9216, C_UOFF = 9216 + 128, C_NSTG = 4;
DI unsigned geglu2(f32x2 g, f32x2 u) {
  const float c0 = -2.f * 0.7978845608028654f * 1.4426950408889634f, c1 = c0 * 0.044715f;
  const f32x2 z = g * (g * g * c1 + c0);
  const f32x2 d = (f32x2){__builtin_amdgcn_exp2f(z[0]), __builtin_amdgcn_exp2f(z[1])} + 1.f;
  const f32x2 o = (g * u) * (f32x2){__builtin_amdgcn_rcpf(d[0]), __builtin_amdgcn_rcpf(d[1])};
  return pk2(o[0], o[1]);
}
#define C_LA 6
DI void ffn1_prefetch(const MainArgs& ar, int wid) {
  const int lane = lane_id();
  const int x = blockIdx.x & 7, j = blockIdx.x >> 3, ct = 4 * x + (j & 3);
  unsigned wsrc3[3];
#pragma unroll
  for (int e = 0; e < 3; ++e) { const int o = 64 * e + lane, row = o / 18, ch = o - row * 18; wsrc3[e] = (16 * (row >> 2) + (row & 3)) * (FF * 2) + ch * 16; }
  const char* Ww = (wid < 4 ? ar.wg : ar.wu) + (size_t)(wid & 3) * 4 * (FF * 2) + ct * 288;
  const unsigned wdst = C_BASE + (wid < 4 ? 0 : C_UOFF) + (wid & 3) * 2304;
#pragma unroll
  for (int s = 0; s < 3; ++s) { const char* wsrc = Ww + (size_t)(32 * s) * (FF * 2); const unsigned m = s * C_STG + wdst;
    dma16<0>(m, wsrc3[0], wsrc); dma16<0>(m + 1024, wsrc3[1], wsrc); if (lane < 16) dma16<0>(m + 2048, wsrc3[2], wsrc); }
}
DI void phase_ffn1(const MainArgs& ar, LAS char* lds, const int wid DBG_PARAM) {
  PH_BEGIN
  char* const ws = ar.ws; const char* wg = ar.wg; const char* wu = ar.wu;
  const int lane = lane_id(), tid = wid * 64 + lane;
  const int x = blockIdx.x & 7, j = blockIdx.x >> 3, ct = 4 * x + (j & 3), rt = j >> 2;
  const int rb = rt * 8 + wid;
  const int h = lane >> 5, blk = (lane >> 4) & 1, q = (lane & 15) >> 2, p = lane & 3;
  unsigned wsrc3[3], wsrc3b[3];
#pragma unroll
  for (int e = 0; e < 3; ++e) { const int o = 64 * e + lane, row = o / 18, ch = o - row * 18;
    wsrc3[e] = (16 * (row >> 2) + (row & 3)) * (FF * 2) + ch * 16; wsrc3b[e] = (144 * (row >> 2) + (row & 3)) * (FF * 2) + ch * 16; }
  const char* Ww = (wid < 4 ? wg : wu) + (size_t)(wid & 3) * 4 * (FF * 2) + ct * 288;
  const unsigned wdst = C_BASE + (wid < 4 ? 0 : C_UOFF) + (wid & 3) * 2304;
  const char* hw = ws + WS_H2 + (size_t)rb * 36 * 1024;
  const unsigned lane16 = lane * 16;
  bf16x8 af[18][2];
  auto issue_piece = [&](auto S, auto E) { constexpr int s = S, e = E;
    constexpr int kbase = s < 16 ? 144 * (s >> 2) + 32 * (s & 3) : 288 * (s - 16) + 128;
    const char* wsrc = Ww + (size_t)kbase * (FF * 2); const unsigned m = (s % C_NSTG) * C_STG + wdst;
    if constexpr (e == 0) dma16<0>(m, s < 16 ? wsrc3[0] : wsrc3b[0], wsrc);
    if constexpr (e == 1) dma16<0>(m + 1024, s < 16 ? wsrc3[1] : wsrc3b[1], wsrc);
    if constexpr (e == 2) { if (lane < 16) dma16<0>(m + 2048, s < 16 ? wsrc3[2] : wsrc3b[2], wsrc); }
    if constexpr (e == 3) gload16c<0>(af[s][0], lane16, hw + s * 2048);
    if constexpr (e == 4) gload16c<1024>(af[s][1], lane16, hw + s * 2048); };
  static_for<3>([&](auto S) { issue_piece(S, integral_constant<int, 3>{}); issue_piece(S, integral_constant<int, 4>{}); });
  unsigned tb[C_NSTG];
#pragma unroll
  for (int i = 0; i < C_NSTG; ++i) { tb[i] = C_BASE + blk * C_UOFF + 288 * (8 * h + q) + 8 * p + i * C_STG; pin(tb[i]); }
  f32x16 acc[9] = {};
  constexpr int LA = C_LA, NIT = 18 * 18;
  bf16x8 wf[LA + 1];
  auto rd = [&](auto I) { constexpr int i = I; constexpr int s = i / 18, ks = (i % 18) / 9, f = i % 9;
    const bf16x4 lo = tr4(lds + tb[s % C_NSTG] + 288 * 16 * ks + 32 * f), hi = tr4(lds + tb[s % C_NSTG] + 288 * (16 * ks + 4) + 32 * f);
    wf[i % (LA + 1)] = cat(lo, hi); };
  SB(); vm_bar<4>(); pin(af[0][0]); pin(af[0][1]); revalidate<1024>(af[0][0], af[0][1], lane16, lane16, hw); SB();
  ST(1);
  static_for<LA>([&](auto I) { rd(I); });
  SB();
  static_for<NIT>([&](auto I) { constexpr int i = I; constexpr int s = i / 18, r = i % 18, ks = r / 9, f = r % 9;
    if constexpr (r == 0 && s > 0) {
      constexpr int outst = (s + 2 < 18) ? 5 : 0;
      vm_bar<outst>(); pin(af[s][0]); pin(af[s][1]);
      revalidate<1024>(af[s][0], af[s][1], lane16, lane16, hw + s * 2048);
      if constexpr (s == 4) ST(2);
      if constexpr (s == 8) ST(3);
      if constexpr (s == 12) ST(4);
      if constexpr (s == 16) ST(5);
      if constexpr (s == 17) proj_prefetch(ar.wd, 18, wid);
      SB();
    }
    if constexpr (i + LA < NIT) rd(integral_constant<int, i + LA>{});
    acc[f] = mfma32(wf[i % (LA + 1)], af[s][ks], acc[f]);
    if constexpr (s + 3 < 18 && (r % 3) == 1 && r / 3 < 5) issue_piece(integral_constant<int, s + 3>{}, integral_constant<int, r / 3>{});
    SB();
  });
  ST(6);
  char* ob = ws + WS_H + ((size_t)rb * 288 + ct * 9) * 1024;
#pragma unroll
  for (int f = 0; f < 9; ++f) { u32x4 w;
#pragma unroll
    for (int i = 0; i < 4; ++i) w[i] = geglu2((f32x2){acc[f][2 * i], acc[f][2 * i + 1]}, (f32x2){acc[f][8 + 2 * i], acc[f][8 + 2 * i + 1]});
    st16c(ob, lane16 + f * 1024, w); }
  signal_done(ws, CNT_H + rt * 4 + (ct >> 3), wid);
  ST_END(2);
}

__global__ __launch_bounds__(512, 2) void k_main(MainArgs ar DBG_PARAM) {
  extern __shared__ __attribute__((aligned(16))) char lds_[];
  LAS char* lds = (LAS char*)lds_;
  const int wid = rfl(threadIdx.x >> 6);
  if (wid == 7) {
    unsigned long long pc; unsigned len;
    asm volatile("s_getpc_b64 %0\n.Lkm_beg:\n\ts_mov_b32 %1, .Lkm_fin-.Lkm_beg" : "=s"(pc), "=s"(len));
    const unsigned per = (len / 128 + 31) / 32, l0 = (blockIdx.x >> 3) * per + lane_id();
    unsigned off = (lane_id() < (int)per && l0 * 128 < len) ? l0 * 128 : 0;
    dma4(A_TAB + 2048 + 7 * 256, off, (const void*)pc);
  }
  phase_attn(ar, lds, wid PH_DBG);
  phase_proj<false>(ar, lds, wid PH_DBG);
  phase_ffn1(ar, lds, wid PH_DBG);
  phase_proj<true>(ar, lds, wid PH_DBG);
  asm volatile(".Lkm_fin:");
}

extern "C" void kernel_launch(void* const* d_in, const int* in_sizes, int n_in, void* d_out, int out_size, void* d_ws, size_t ws_size, hipStream_t stream) {
  const unsigned short* x = (const unsigned short*)d_in[0];
  const unsigned short* c = (const unsigned short*)d_in[9]; const unsigned short* wmod = (const unsigned short*)d_in[10];
  char* ws = (char*)d_ws;
  (void)hipFuncSetAttribute((const void*)k_pre, hipFuncAttributeMaxDynamicSharedMemorySize, PRE_LDS);
  (void)hipFuncSetAttribute((const void*)k_main, hipFuncAttributeMaxDynamicSharedMemorySize, MAIN_LDS);
  const char* kc = (const char*)d_in[1]; const char* vc = (const char*)d_in[2];
  const char* wq = (const char*)d_in[3]; const char* wo = (const char*)d_in[4];
  const char* wg = (const char*)d_in[6]; const char* wu = (const char*)d_in[7]; const char* wd = (const char*)d_in[8];
  k_pre<<<PRE_RQ + PRE_MOD + PRE_CLR, 512, PRE_LDS, stream>>>(c, wmod, x, wq, ws DBG_ARG);
  MainArgs ar{kc, vc, wg, wu, wo, wd, x, (unsigned short*)d_out, ws};
  k_main<<<256, 512, MAIN_LDS, stream>>>(ar DBG_ARG);
}
```

```cpp
#include <hip/hip_runtime.h>
#include <stdint.h>
#include <utility>

#define DI __device__ __forceinline__
#define LAS __attribute__((address_space(3)))
typedef __bf16 bf16x8 __attribute__((ext_vector_type(8)));
typedef __bf16 bf16x4 __attribute__((ext_vector_type(4)));
typedef __bf16 bf16x2 __attribute__((ext_vector_type(2)));
typedef float f32x2 __attribute__((ext_vector_type(2)));
typedef float f32x4 __attribute__((ext_vector_type(4)));
typedef float f32x16 __attribute__((ext_vector_type(16)));
typedef unsigned u32x2 __attribute__((ext_vector_type(2)));
typedef unsigned u32x4 __attribute__((ext_vector_type(4)));
using std::integral_constant;

constexpr int M_ = 2048, D_ = 576, SK = 2048, WIN = 512, FF = 4608;

constexpr size_t WS_MOD = 0;
constexpr size_t WS_STAT = 16384;
constexpr size_t WS_H1 = WS_STAT + 16384;
constexpr size_t WS_O = WS_H1 + (size_t)64 * 36 * 1024;
constexpr size_t WS_H2 = WS_O + (size_t)64 * 256 * 1024;
constexpr size_t WS_H = WS_H2 + (size_t)64 * 36 * 1024;
constexpr size_t WS_WQ = WS_H + (size_t)64 * 288 * 1024;
constexpr size_t WS_CNT = WS_WQ + (size_t)32 * 36 * 4 * 1024;
constexpr size_t WS_X1R = WS_CNT + 128 * 256;
constexpr size_t WS_PB = WS_X1R + (size_t)256 * 576 * 32;
constexpr size_t WS_PD = WS_PB + (size_t)16 * 16 * 16 * 2304;
constexpr size_t WS_END = WS_PD + (size_t)16 * 16 * 16 * 2304;

template <int... Is, class F> DI void static_for_impl(std::integer_sequence<int, Is...>, F&& f) { (f(std::integral_constant<int, Is>{}), ...); }
template <int N, class F> DI void static_for(F&& f) { static_for_impl(std::make_integer_sequence<int, N>{}, f); }

DI float bflo(unsigned w) { return __uint_as_float(w << 16); }
DI float bfhi(unsigned w) { return __uint_as_float(w & 0xffff0000u); }
DI unsigned pk2(float lo, float hi) { f32x2 v = {lo, hi}; bf16x2 b = __builtin_convertvector(v, bf16x2); return __builtin_bit_cast(unsigned, b); }
DI unsigned rfl(unsigned x) { return __builtin_amdgcn_readfirstlane(x); }
DI int lane_id() { int l; asm volatile("v_mbcnt_lo_u32_b32 %0, -1, 0\n\tv_mbcnt_hi_u32_b32 %0, -1, %0" : "=v"(l)); return l; }
DI void SB() { __builtin_amdgcn_sched_barrier(0); }

template <int IMM> DI void dma16(unsigned m0v, unsigned voff, const void* sbase) {
  asm volatile("s_mov_b32 m0, %0\n\ts_nop 0\n\tglobal_load_lds_dwordx4 %1, %2 offset:%3" ::"s"(m0v), "v"(voff), "s"(sbase), "i"(IMM) : "memory");
}
DI void dma4(unsigned m0v, unsigned voff, const void* sbase) {
  asm volatile("s_mov_b32 m0, %0\n\ts_nop 0\n\tglobal_load_lds_dword %1, %2" ::"s"(m0v), "v"(voff), "s"(sbase) : "memory");
}
template <int IMM, class T> DI void gload16(T& dst, unsigned voff, const void* sbase) {
  asm volatile("global_load_dwordx4 %0, %1, %2 offset:%3" : "=v"(dst) : "v"(voff), "s"(sbase), "i"(IMM));
}
template <int IMM, class T> DI void gload16c(T& dst, unsigned voff, const void* sbase) {
  asm volatile("global_load_dwordx4 %0, %1, %2 offset:%3 sc1" : "=v"(dst) : "v"(voff), "s"(sbase), "i"(IMM));
}
DI void st16c(char* base, unsigned voff, u32x4 v) {
  asm volatile("global_store_dwordx4 %0, %1, %2 sc1\n\ts_nop 1" ::"v"(voff), "v"(v), "s"(base) : "memory");
}
template <int N> DI void wait_vm() { asm volatile("s_waitcnt vmcnt(%0)" ::"i"(N) : "memory"); }
template <int N> DI void vm_bar() { asm volatile("s_waitcnt vmcnt(%0)\n\ts_barrier" ::"i"(N) : "memory"); }
DI void bar() { asm volatile("s_barrier" ::: "memory"); }
template <class T> DI void pin(T& a) { asm volatile("" : "+v"(a)); }
DI bf16x4 tr4(LAS char* p) { return __builtin_amdgcn_ds_read_tr16_b64_v4bf16((LAS bf16x4*)p); }
DI bf16x8 cat(bf16x4 a, bf16x4 b) { return __builtin_shufflevector(a, b, 0, 1, 2, 3, 4, 5, 6, 7); }
DI f32x16 mfma32(bf16x8 a, bf16x8 b, f32x16 c) { return __builtin_amdgcn_mfma_f32_32x32x16_bf16(a, b, c, 0, 0, 0); }
DI f32x4 mfma16(bf16x8 a, bf16x8 b, f32x4 c) { return __builtin_amdgcn_mfma_f32_16x16x32_bf16(a, b, c, 0, 0, 0); }
DI float max3f(float a, float b, float c) { float r; asm("v_max3_f32 %0, %1, %2, %3" : "=v"(r) : "v"(a), "v"(b), "v"(c)); return r; }
DI float max2f(float a, float b) { float r; asm("v_max_f32_e32 %0, %1, %2" : "=v"(r) : "v"(a), "v"(b)); return r; }
template <int CTRL> DI float dppf(float v) { return __builtin_bit_cast(float, __builtin_amdgcn_update_dpp(0, __builtin_bit_cast(int, v), CTRL, 0xf, 0xf, true)); }
DI float sum8(float v) { v += dppf<0xB1>(v); v += dppf<0x4E>(v); v += dppf<0x141>(v); return v; }
DI f32x16 splat16(float v) { f32x16 r; for (int i = 0; i < 16; ++i) r[i] = v; return r; }

constexpr float QSCALE = 0.08838834764831845f * 1.4426950408889634f;
constexpr int PRE_RQ = 144, PRE_MOD = 108, PRE_CLR = 64;
constexpr int PRE_LDS = 32768 + 256;
DI float silu_f(float cv) { return cv * __builtin_amdgcn_rcpf(1.f + __builtin_amdgcn_exp2f(-1.4426950408889634f * cv)); }
DI float sum_x8_16_32(float v) {
  v += dppf<0x128>(v);
  { auto rr = __builtin_amdgcn_permlane16_swap(__float_as_uint(v), __float_as_uint(v), false, false); v = __uint_as_float(rr[0]) + __uint_as_float(rr[1]); }
  { auto rr = __builtin_amdgcn_permlane32_swap(__float_as_uint(v), __float_as_uint(v), false, false); v = __uint_as_float(rr[0]) + __uint_as_float(rr[1]); }
  return v;
}
__global__ __launch_bounds__(512) void k_pre(const unsigned short* __restrict__ c, const unsigned short* __restrict__ wmod, const unsigned short* __restrict__ xin, const char* __restrict__ wq, char* __restrict__ ws) {
  extern __shared__ __attribute__((aligned(16))) char lds_[];
  LAS char* lds = (LAS char*)lds_;
  const int tid = threadIdx.x, lane = tid & 63, wid = rfl(tid >> 6);
  int b = blockIdx.x;
  if (wid == 7 && b < 224) {
    unsigned long long pc; int d0; unsigned len;
    asm volatile("s_getpc_b64 %0\n.Lkp_here:\n\ts_mov_b32 %1, .Lkm_beg-.Lkp_here\n\ts_mov_b32 %2, .Lkm_fin-.Lkm_beg" : "=s"(pc), "=s"(d0), "=s"(len));
    const unsigned per = (len / 128 + 27) / 28, l0 = (b >> 3) * per + lane;
    unsigned off = (lane < (int)per && l0 * 128 < len) ? l0 * 128 : 0;
    dma4(32768, off, (const void*)(pc + (long long)d0 - 256));
  }
  if (b < PRE_MOD) {
    LAS float* sl = (LAS float*)lds; LAS float* red = (LAS float*)(lds + 2304);
    const int rg = tid >> 2, col0 = b * 32 + (tid & 3) * 8;
    u32x4 w[5]; unsigned cw;
    asm volatile("global_load_dword %0, %1, %2" : "=v"(cw) : "v"((unsigned)(tid < 288 ? tid : 0) * 4), "s"(c));
#pragma unroll
    for (int i = 0; i < 5; ++i) { const int row = rg + 128 * i < 576 ? rg + 128 * i : rg; gload16<0>(w[i], (unsigned)(row * 3456 + col0) * 2, wmod); }
    asm volatile("s_waitcnt vmcnt(5)" : "+v"(cw));
    if (tid < 288) *(LAS f32x2*)(sl + 2 * tid) = (f32x2){silu_f(bflo(cw)), silu_f(bfhi(cw))};
    asm volatile("s_waitcnt vmcnt(0) lgkmcnt(0)\n\ts_barrier" ::: "memory");
    float acc[8] = {};
#pragma unroll
    for (int i = 0; i < 5; ++i) { pin(w[i]); const float s = rg + 128 * i < 576 ? sl[rg + 128 * i] : 0.f;
#pragma unroll
      for (int j = 0; j < 4; ++j) { acc[2 * j] += s * bflo(w[i][j]); acc[2 * j + 1] += s * bfhi(w[i][j]); } }
#pragma unroll
    for (int j = 0; j < 8; ++j) { float v = acc[j]; v += dppf<0x124>(v); acc[j] = sum_x8_16_32(v); }
    if (lane < 4) { *(LAS f32x4*)(red + wid * 32 + lane * 8) = (f32x4){acc[0], acc[1], acc[2], acc[3]}; *(LAS f32x4*)(red + wid * 32 + lane * 8 + 4) = (f32x4){acc[4], acc[5], acc[6], acc[7]}; }
    asm volatile("s_waitcnt lgkmcnt(0)\n\ts_barrier" ::: "memory");
    if (tid < 32) { float t = 0.f;
#pragma unroll
      for (int w2 = 0; w2 < 8; ++w2) t += red[w2 * 32 + tid];
      ((float*)(ws + WS_MOD))[b * 32 + tid] = t; }
    return;
  }
  b -= PRE_MOD;
  if (b < PRE_CLR) {
    const int cc = lane & 15, row = (b * 8 + wid) * 4 + (lane >> 4);
    u32x4 xv[5];
#pragma unroll
    for (int i = 0; i < 5; ++i) xv[i] = *(const u32x4*)(xin + (size_t)row * D_ + (cc + 16 * i < 72 ? cc + 16 * i : cc) * 8);
    if (tid < 32) *(u32x4*)(ws + WS_CNT + (size_t)(b * 32 + tid) * 16) = (u32x4){0u, 0u, 0u, 0u};
#pragma unroll
    for (int i = 0; i < 5; ++i) { const unsigned o = (unsigned)(b * 36864 + i * 8192 + tid * 16); if (i < 4 || tid < 256) st16c(ws + WS_H1, o, (u32x4){~0u, ~0u, ~0u, ~0u}); }
    const bool last = cc < 8;
    float sm = 0.f;
#pragma unroll
    for (int i = 0; i < 5; ++i)
#pragma unroll
      for (int j = 0; j < 4; ++j) { const float t = bflo(xv[i][j]) + bfhi(xv[i][j]); sm += (i < 4 || last) ? t : 0.f; }
    sm = sum8(sm); sm += dppf<0x128>(sm);
    const float mean = sm * (1.f / 576.f);
    float q = 0.f;
#pragma unroll
    for (int i = 0; i < 5; ++i)
#pragma unroll
      for (int j = 0; j < 4; ++j) { const float d0 = bflo(xv[i][j]) - mean, d1 = bfhi(xv[i][j]) - mean; q += (i < 4 || last) ? d0 * d0 + d1 * d1 : 0.f; }
    q = sum8(q); q += dppf<0x128>(q);
    const float rstd = __builtin_amdgcn_rsqf(q * (1.f / 576.f) + 1e-5f);
    if (cc == 0) ((f32x2*)(ws + WS_STAT))[row] = (f32x2){mean, rstd};
    return;
  }
  b -= PRE_CLR;
  {
    const int u = b * 8 + wid, s32 = u >> 6, cg = u & 63;
    { const int rr = lane >> 3, pp = lane & 7;
      const unsigned v = rr * 8192 + ((((pp >> 1) ^ ((rr >> 1) & 3)) << 1) + (pp & 1)) * 16;
      const char* src = wq + (size_t)s32 * 32 * 8192 + cg * 128; const unsigned m = wid * 4096;
      dma16<0>(m, v, src); dma16<0>(m + 1024, v, src + 8 * 8192); dma16<0>(m + 2048, v, src + 16 * 8192); dma16<0>(m + 3072, v, src + 24 * 8192); }
    wait_vm<0>();
    const int g = lane >> 4, q = (lane & 15) >> 2, p = lane & 3, h = lane >> 5, blk = g & 1, pc = ((p & 1) << 1) | (p >> 1);
    LAS char* scr = lds + wid * 4096;
#pragma unroll
    for (int dbl = 0; dbl < 2; ++dbl)
#pragma unroll
      for (int ks = 0; ks < 2; ++ks) { const int r0 = 16 * ks + 8 * h + q, r1 = r0 + 4, cx = 2 * dbl + blk;
        const bf16x8 w = cat(tr4(scr + 128 * r0 + 32 * (cx ^ ((r0 >> 1) & 3)) + 8 * pc), tr4(scr + 128 * r1 + 32 * (cx ^ ((r1 >> 1) & 3)) + 8 * pc));
        st16c(ws + WS_WQ, (unsigned)((((cg >> 1) * 36 + 2 * s32 + ks) * 4 + 2 * (cg & 1) + dbl) * 1024 + lane * 16), __builtin_bit_cast(u32x4, w)); }
  }
}

constexpr float THR = 8.0f;
constexpr int A_TAB = 131072;
constexpr int A_LA = 4, Q_LA = 5;
struct MainArgs { const char* kc; const char* vc; const char* wg; const char* wu; const char* wo; const char* wd; const unsigned short* x; unsigned short* out; char* ws; };
constexpr int P_STG = 9216, P_NSTG = 4, P_T = 73728;
constexpr int C_BASE = 81920;
constexpr int MAIN_LDS = 163840;
constexpr int CNT_CLEAR = 0;
constexpr int CNT_O = 8, CNT_H = 40;
DI void signal_done(char* ws, int idx, int wid) {
  vm_bar<0>();
  if (wid == 0 && lane_id() == 0) __hip_atomic_fetch_add((unsigned*)(ws + WS_CNT) + idx * 64, 1u, __ATOMIC_RELAXED, __HIP_MEMORY_SCOPE_AGENT);
}
DI void wait_count(const char* ws, int idx, unsigned target, int wid) {
  if (wid == 0) {
    const unsigned* cnt = (const unsigned*)(ws + WS_CNT) + idx * 64;
    for (unsigned spin = 0; spin < (1u << 15); ++spin) {
      if (rfl(__hip_atomic_load(cnt, __ATOMIC_RELAXED, __HIP_MEMORY_SCOPE_AGENT)) >= target) break;
      __builtin_amdgcn_s_sleep(1);
    }
  }
  bar();
}
DI void st_empty(char* base, unsigned voff) { st16c(base, voff, (u32x4){~0u, ~0u, ~0u, ~0u}); }
DI bool has_empty(const bf16x8& a, const bf16x8& b) {
  const u32x4 x = __builtin_bit_cast(u32x4, a), y = __builtin_bit_cast(u32x4, b);
  unsigned m = x[0] > x[1] ? x[0] : x[1]; m = m > x[2] ? m : x[2]; m = m > x[3] ? m : x[3]; m = m > y[0] ? m : y[0]; m = m > y[1] ? m : y[1]; m = m > y[2] ? m : y[2]; m = m > y[3] ? m : y[3];
  return __any(m == ~0u);
}
template <int IMM1> DI void revalidate(bf16x8& a, bf16x8& b, unsigned oa, unsigned ob, const char* base) {
  if (__builtin_expect(has_empty(a, b), 0)) {
    for (unsigned spin = 0; spin < (1u << 15); ++spin) {
      __builtin_amdgcn_s_sleep(3);
      asm volatile("global_load_dwordx4 %0, %2, %4 sc1\n\tglobal_load_dwordx4 %1, %3, %4 offset:%5 sc1\n\ts_waitcnt vmcnt(0)" : "+v"(a), "+v"(b) : "v"(oa), "v"(ob), "s"(base), "i"(IMM1) : "memory");
      if (!has_empty(a, b)) break;
    }
  }
}
DI void proj_prefetch(const char* W, int nst, int wid) {
  const int lane = lane_id();
  const int x = blockIdx.x & 7, j = blockIdx.x >> 3, ks = x >> 1, ct = j & 3, rg = wid & 3, kh = wid >> 2;
  unsigned wsrc3[3];
#pragma unroll
  for (int e = 0; e < 3; ++e) { const int o = 64 * e + lane, row = o / 18, ch = o - row * 18; wsrc3[e] = row * 1152 + ch * 16; }
  const char* Ww = W + (size_t)((ks * 2 + kh) * nst * 32 + rg * 8) * 1152 + ct * 288;
  const unsigned wdst = kh * (P_NSTG * P_STG) + rg * 2304;
#pragma unroll
  for (int s = 0; s < 3; ++s) { const char* wsrc = Ww + (size_t)s * 32 * 1152; const unsigned m = s * P_STG + wdst;
    dma16<0>(m, wsrc3[0], wsrc); dma16<0>(m + 1024, wsrc3[1], wsrc); if (lane < 16) dma16<0>(m + 2048, wsrc3[2], wsrc); }
}
DI void phase_attn(const MainArgs& ar, LAS char* lds, const int wid) {
  char* const ws = ar.ws;
  const int lane = lane_id(), tid = wid * 64 + lane;
  const int x = blockIdx.x & 7, j = blockIdx.x >> 3, head = 4 * x + (j & 3), rt = j >> 2, kvh = head >> 1;
  const int rb = rt * 8 + wid;
  const int prb = 2 * (j >> 2) + (x & 1), pown = (j & 3) * 4 + (x >> 1);
  const int h = lane >> 5, r = lane & 31, blk = (lane >> 4) & 1, q = (lane & 15) >> 2, p = lane & 3;
  const int l4 = lane >> 4, pos = lane & 15;
  unsigned ksrc2[2];
#pragma unroll
  for (int e = 0; e < 2; ++e) { const int n = 2 * wid + e, row = 4 * n + l4, ch = pos ^ ((l4 << 2) | (n & 3)); ksrc2[e] = row * 256 + ch * 16 - e * 1024; }
  const char* wq_h = ws + WS_WQ + (size_t)head * 36 * 4096 + wid * 2048;
  const char* kb_ = ar.kc + ((size_t)kvh * SK + (SK - WIN)) * 256;
  const char* vb_ = ar.vc + ((size_t)kvh * SK + (SK - WIN)) * 256;
  const char* h1w = ws + WS_H1 + (size_t)rb * 36 * 1024;
  const unsigned lane16 = lane * 16;
  auto dmaW = [&](int qq, int e) { const unsigned m = 65536 + (qq & 3) * 16384 + wid * 2048; if (e == 0) dma16<0>(m, lane16, wq_h + qq * 16384); else dma16<1024>(m, lane16, wq_h + qq * 16384); };
  auto dmaK8 = [&](int t) { const unsigned m = (t & 3) * 16384 + wid * 2048; const char* s = kb_ + t * 16384; dma16<0>(m, ksrc2[0], s); dma16<1024>(m, ksrc2[1], s); };
  auto dmaV8 = [&](int t) { const unsigned m = 65536 + ((t + 1) & 3) * 16384 + wid * 2048; const char* s = vb_ + t * 16384; dma16<0>(m, ksrc2[0], s); dma16<1024>(m, ksrc2[1], s); };
  bf16x8 g[9][4];
  auto loadG1 = [&](auto QQ, auto E) { constexpr int qq = QQ, e = E; gload16c<e * 1024>(g[qq][e], lane16, h1w + qq * 4096); };
  auto checkG = [&](auto QQ) { constexpr int qq = QQ; static_for<4>([&](auto E) { pin(g[qq][E]); });
    revalidate<1024>(g[qq][0], g[qq][1], lane16, lane16, h1w + qq * 4096); revalidate<1024>(g[qq][2], g[qq][3], lane16, lane16, h1w + qq * 4096 + 2048); };
  auto loadG = [&](auto QQ) { static_for<4>([&](auto E) { loadG1(QQ, E); }); };
  auto mark = [&](auto MI) { constexpr int mi = MI;
    if constexpr (mi < 2) { const int rown = prb * 128 + pown * 8 + (tid & 7), kc = mi == 0 ? (tid >> 3) : 64 + (lane >> 3);
      st_empty(ws + WS_H2, (unsigned)(((rown >> 5) * 36 + (kc >> 1)) * 1024 + ((kc & 1) * 32 + (rown & 31)) * 16)); }
    else { const int n = tid + 512 * (mi - 2);
      st_empty(ws + WS_PB + (size_t)(prb * 16 + pown) * 36864, (unsigned)(n * 16 + (n < 2304 ? 0 : (unsigned)(WS_PD - WS_PB) - 36864u))); } };
  {
    const int row = rt * 256 + (8 * (j & 3) + x) * 8 + (tid & 7), u0 = tid >> 3, u1 = 64 + (lane >> 3);
    u32x4 xv[2]; f32x4 sa[2][2], ca[2][2]; f32x2 mr;
    asm volatile("global_load_dwordx2 %0, %1, %2" : "=v"(mr) : "v"((unsigned)row * 8), "s"(ws + WS_STAT));
    gload16<0>(xv[0], (unsigned)(row * D_ + u0 * 8) * 2, ar.x); gload16<0>(xv[1], (unsigned)(row * D_ + u1 * 8) * 2, ar.x);
#pragma unroll
    for (int i = 0; i < 2; ++i) { const unsigned o = (i ? u1 : u0) * 32; gload16<0>(sa[i][0], o, ws + WS_MOD); gload16<16>(sa[i][1], o, ws + WS_MOD); gload16<0>(ca[i][0], o, ws + WS_MOD + 2304); gload16<16>(ca[i][1], o, ws + WS_MOD + 2304); }
    dmaW(0, 0); dmaW(0, 1); dmaW(1, 0); dmaW(1, 1); dmaW(2, 0); dmaW(2, 1); dmaK8(0);
    wait_vm<8>(); pin(mr);
    const float rq = mr[1] * QSCALE;
#pragma unroll
    for (int i = 0; i < 2; ++i) { pin(xv[i]); pin(sa[i][0]); pin(sa[i][1]); pin(ca[i][0]); pin(ca[i][1]);
      const f32x4 v0 = {bflo(xv[i][0]), bfhi(xv[i][0]), bflo(xv[i][1]), bfhi(xv[i][1])}, v1 = {bflo(xv[i][2]), bfhi(xv[i][2]), bflo(xv[i][3]), bfhi(xv[i][3])};
      const f32x4 h0 = (v0 - mr[0]) * rq * (ca[i][0] + 1.f) + sa[i][0] * QSCALE, h1v = (v1 - mr[0]) * rq * (ca[i][1] + 1.f) + sa[i][1] * QSCALE; const int u = i ? u1 : u0;
      st16c(ws + WS_H1, (unsigned)(((row >> 5) * 36 + (u >> 1)) * 1024 + ((u & 1) * 32 + (row & 31)) * 16), (u32x4){pk2(h0[0], h0[1]), pk2(h0[2], h0[3]), pk2(h1v[0], h1v[1]), pk2(h1v[2], h1v[3])}); }
  }
  __builtin_amdgcn_s_sleep(16);
  for (unsigned spin = 0; spin < (1u << 15); ++spin) {
    loadG(integral_constant<int, 0>{}); wait_vm<0>();
    static_for<4>([&](auto E) { pin(g[0][E]); });
    if (!has_empty(g[0][0], g[0][1]) && !has_empty(g[0][2], g[0][3])) break;
    __builtin_amdgcn_s_sleep(2);
  }
  loadG(integral_constant<int, 1>{}); loadG(integral_constant<int, 2>{});
  unsigned wb0 = 65536 + lane16;
  pin(wb0);
  f32x16 acc[4] = {};
  {
    constexpr int LA = Q_LA, NIT = 9 * 16;
    bf16x8 wf[LA + 1];
    auto rd = [&](auto I) { constexpr int i = I; constexpr int qq = i / 16, ks = (i % 16) / 4, db = i % 4;
      wf[i % (LA + 1)] = *(LAS bf16x8*)(lds + wb0 + (qq & 3) * 16384 + ks * 4096 + db * 1024); };
    SB(); bar();
    SB();
    static_for<LA>([&](auto I) { rd(I); });
    SB();
    static_for<NIT>([&](auto I) { constexpr int i = I; constexpr int qq = i / 16, rr = i % 16, ks = rr / 4, db = rr % 4;
      if constexpr (rr == 0 && qq > 0) {
        constexpr int NW[9] = {8, 10, 10, 10, 10, 10, 10, 8, 8};
        vm_bar<NW[qq]>();
        checkG(integral_constant<int, qq>{});
        SB();
      }
      if constexpr (i + LA < NIT) rd(integral_constant<int, i + LA>{});
      acc[db] = mfma32(wf[i % (LA + 1)], g[qq][ks], acc[db]);
      if constexpr (qq + 3 < 9) {
        if constexpr (rr == 1) dmaW(qq + 3, 0);
        if constexpr (rr == 3) dmaW(qq + 3, 1);
        if constexpr (rr >= 5 && rr <= 11 && (rr & 1)) loadG1(integral_constant<int, qq + 3>{}, integral_constant<int, (rr - 5) / 2>{});
      } else {
        if constexpr (rr == 1) dmaV8(qq - 6);
        if constexpr (rr == 7) dmaK8(qq - 5);
      }
      SB();
    });
  }
  bf16x8 qf[8];
#pragma unroll
  for (int db = 0; db < 4; ++db)
#pragma unroll
    for (int s = 0; s < 2; ++s) { u32x4 w;
#pragma unroll
      for (int i = 0; i < 4; ++i) w[i] = pk2(acc[db][8 * s + 2 * i], acc[db][8 * s + 2 * i + 1]);
      qf[2 * db + s] = __builtin_bit_cast(bf16x8, w); }
  const unsigned kb0 = 256 * r + 16 * (h ^ (((r & 3) << 2) | ((r >> 2) & 3)));
  const unsigned vb0 = 65536 + 256 * (4 * h + q) + 16 * ((q << 2) | ((2 * blk + (p >> 1)) ^ h)) + 8 * (p & 1);
  unsigned ka[8], va[4][2];
#pragma unroll
  for (int s = 0; s < 8; ++s) ka[s] = kb0 ^ (s << 5);
#pragma unroll
  for (int db = 0; db < 4; ++db) { va[db][0] = vb0 ^ (db << 6); va[db][1] = vb0 ^ (db << 6) ^ 32; }
  f32x16 o[4] = {}; f32x16 S[2][2], negm; unsigned pw[16]; float m; f32x2 l2 = {0.f, 0.f};
  constexpr int LA = A_LA;
  bf16x8 fr[LA + 1];
  struct Map { int g[272]; int n; };
  constexpr Map MP = []() constexpr { Map mp{}; mp.n = 0; for (int G = 0; G < 9 * 32; ++G) { const int t = G / 32 - 1, i = G % 32; if (i < 16 ? (t + 1 <= 7) : (t >= 0 && t <= 7)) mp.g[mp.n++] = G; } return mp; }();
  constexpr int NIT = MP.n;
  auto rdA = [&](auto C) { constexpr int c = C; constexpr int G = MP.g[c], t = G / 32 - 1, i = G % 32;
    if constexpr (i < 16) { constexpr int s = i >> 1, kb = i & 1; fr[c % (LA + 1)] = *(LAS bf16x8*)(lds + ka[s] + ((t + 1) & 3) * 16384 + kb * 8192); }
    else { constexpr int ii = i - 16, kk = ii >> 2, db = ii & 3; constexpr int so = ((t + 1) & 3) * 16384 + kk * 4096;
      fr[c % (LA + 1)] = cat(tr4(lds + va[db][0] + so), tr4(lds + va[db][1] + so + 2048)); } };
  auto rowmax_finish = [&](float a) { auto rr = __builtin_amdgcn_permlane32_swap(__float_as_uint(a), __float_as_uint(a), false, false); return max2f(__uint_as_float(rr[0]), __uint_as_float(rr[1])); };
  float mxa, mxb;
  const unsigned gw = blockIdx.x * 8 + wid;
  auto touch = [&](auto TI) { constexpr int ti = TI; constexpr int m = ti >> 1, half = ti & 1;
    constexpr unsigned per = m == 0 ? 9 : 11, tot = m == 0 ? 36864 : 41472;
    unsigned line = (gw * 2 + half) * per + (lane < (int)per ? lane : 0); if (line >= tot) line = tot - 1;
    dma4(A_TAB + 2048 + wid * 256, line * 128, m == 0 ? ar.wo : ar.wd); };
  auto background = [&](auto BI) { constexpr int bi = BI;
    if constexpr ((bi & 3) == 0) touch(integral_constant<int, (bi >> 2) < 2 ? (bi >> 2) : (bi >> 2) + 4>{}); else mark(integral_constant<int, (bi >> 2) * 3 + (bi & 3) - 1>{}); };
  SB(); vm_bar<8>(); SB();
  static_for<LA>([&](auto C) { rdA(C); });
  SB();
  static_for<NIT>([&](auto C) { constexpr int c = C; constexpr int G = MP.g[c], t = G / 32 - 1, i = G % 32;
    constexpr bool firstX = (i == 0) || (t == 7 && i == 16);
    if constexpr (firstX && t >= 0) {
      constexpr int NW[8] = {4, 8, 12, 12, 11, 5, 0, 0};
      float a = rowmax_finish(max2f(mxa, mxb));
      if constexpr (t == 0) {
        m = a; negm = splat16(-a);
#pragma unroll
        for (int r2 = 0; r2 < 16; ++r2) { S[0][0][r2] -= a; S[0][1][r2] -= a; }
      } else if (__builtin_expect(__any(a > THR), 0)) {
        const float dl = fmaxf(a, 0.f), f = __builtin_amdgcn_exp2f(-dl);
        m += dl; negm = splat16(-m); l2 *= f;
#pragma unroll
        for (int r2 = 0; r2 < 16; ++r2) { S[t & 1][0][r2] -= dl; S[t & 1][1][r2] -= dl; }
#pragma unroll
        for (int d = 0; d < 4; ++d)
#pragma unroll
          for (int r2 = 0; r2 < 16; ++r2) o[d][r2] *= f;
      }
      SB(); vm_bar<NW[t]>(); SB();
      if constexpr (t == 7) { if (wid == 0 && lane == 0) __hip_atomic_fetch_add((unsigned*)(ws + WS_CNT) + (CNT_CLEAR + rt) * 64, 1u, __ATOMIC_RELAXED, __HIP_MEMORY_SCOPE_AGENT); }
      if constexpr (t == 7) proj_prefetch(ar.wo, 16, wid);
      if constexpr (t == 7) {
#pragma unroll
        for (int e = 0; e < 32; ++e) S[1][e >> 4][e & 15] = __builtin_amdgcn_exp2f(S[1][e >> 4][e & 15]);
#pragma unroll
        for (int e = 0; e < 16; ++e) pw[e] = pk2(S[1][e >> 3][(2 * e) & 15], S[1][e >> 3][((2 * e) & 15) + 1]);
        SB();
      }
    }
    if constexpr (c + LA < NIT) rdA(integral_constant<int, c + LA>{});
    if constexpr (i < 16) {
      constexpr int s = i >> 1, kb = i & 1, X = (t + 1) & 1;
      if constexpr (s == 0) { if constexpr (t < 0) S[X][kb] = mfma32(fr[c % (LA + 1)], qf[s], f32x16{}); else S[X][kb] = mfma32(fr[c % (LA + 1)], qf[s], negm); }
      else S[X][kb] = mfma32(fr[c % (LA + 1)], qf[s], S[X][kb]);
      if constexpr (t >= 0) {
        constexpr int e0 = 2 * i, e1 = 2 * i + 1, Y = t & 1;
        S[Y][e0 >> 4][e0 & 15] = __builtin_amdgcn_exp2f(S[Y][e0 >> 4][e0 & 15]);
        S[Y][e1 >> 4][e1 & 15] = __builtin_amdgcn_exp2f(S[Y][e1 >> 4][e1 & 15]);
        if constexpr (i >= 1) { constexpr int e = i - 1; pw[e] = pk2(S[Y][e >> 3][(2 * e) & 15], S[Y][e >> 3][((2 * e) & 15) + 1]); }
        if constexpr (i == 15) { SB(); pw[15] = pk2(S[Y][1][14], S[Y][1][15]); }
        if constexpr (t <= 3 && i == 6) dma16<0>((t & 3) * 16384 + wid * 2048, ksrc2[0], kb_ + (t + 4) * 16384);
        if constexpr (t <= 3 && i == 9) dma16<1024>((t & 3) * 16384 + wid * 2048, ksrc2[1], kb_ + (t + 4) * 16384);
        if constexpr (t <= 4 && i == 12) dma16<0>(65536 + (t & 3) * 16384 + wid * 2048, ksrc2[0], vb_ + (t + 3) * 16384);
        if constexpr (t <= 4 && i == 15) dma16<1024>(65536 + (t & 3) * 16384 + wid * 2048, ksrc2[1], vb_ + (t + 3) * 16384);
      }
      if constexpr (t < 0 && i == 15) {
        asm volatile("s_nop 11" : "+v"(S[0][0]), "+v"(S[0][1]));
        mxa = max3f(S[0][0][0], S[0][0][1], S[0][0][2]); mxb = max3f(S[0][1][0], S[0][1][1], S[0][1][2]);
#pragma unroll
        for (int r2 = 3; r2 < 15; r2 += 2) { mxa = max3f(mxa, S[0][0][r2], S[0][0][r2 + 1]); mxb = max3f(mxb, S[0][1][r2], S[0][1][r2 + 1]); }
        mxa = max3f(mxa, S[0][0][15], S[0][1][15]);
      }
    } else {
      constexpr int ii = i - 16, kk = ii >> 2, db = ii & 3, Y = t & 1, X = (t + 1) & 1;
      const u32x4 w = {pw[4 * kk], pw[4 * kk + 1], pw[4 * kk + 2], pw[4 * kk + 3]};
      o[db] = mfma32(fr[c % (LA + 1)], __builtin_bit_cast(bf16x8, w), o[db]);
      if constexpr (t <= 3 && (ii & 3) == 1 && 4 * t + (ii >> 2) < 15) background(integral_constant<int, 4 * t + (ii >> 2)>{});
      l2 += (f32x2){S[Y][ii >> 3][(2 * ii) & 15], S[Y][ii >> 3][((2 * ii) & 15) + 1]};
      if constexpr (t < 7) {
        if constexpr (ii == 4) { mxa = max3f(S[X][0][0], S[X][0][1], S[X][0][2]); mxb = max3f(S[X][0][3], S[X][0][4], S[X][0][5]); }
        if constexpr (ii == 5) { mxa = max3f(mxa, S[X][0][6], S[X][0][7]); mxb = max3f(mxb, S[X][0][8], S[X][0][9]); }
        if constexpr (ii == 6) { mxa = max3f(mxa, S[X][0][10], S[X][0][11]); mxb = max3f(mxb, S[X][0][12], S[X][0][13]); }
        if constexpr (ii == 7) { mxa = max3f(mxa, S[X][0][14], S[X][0][15]); mxb = max3f(mxb, S[X][1][0], S[X][1][1]); }
        if constexpr (ii == 8) { mxa = max3f(mxa, S[X][1][2], S[X][1][3]); mxb = max3f(mxb, S[X][1][4], S[X][1][5]); }
        if constexpr (ii == 9) { mxa = max3f(mxa, S[X][1][6], S[X][1][7]); mxb = max3f(mxb, S[X][1][8], S[X][1][9]); }
        if constexpr (ii == 10) { mxa = max3f(mxa, S[X][1][10], S[X][1][11]); mxb = max3f(mxb, S[X][1][12], S[X][1][13]); }
        if constexpr (ii == 11) { mxa = max3f(mxa, S[X][1][14], S[X][1][15]); }
      }
    }
    SB();
  });
  float l = l2[0] + l2[1];
  { auto rr = __builtin_amdgcn_permlane32_swap(__float_as_uint(l), __float_as_uint(l), false, false); l = __uint_as_float(rr[0]) + __uint_as_float(rr[1]); }
  const float inv = __builtin_amdgcn_rcpf(l);
  char* ob = ws + WS_O + ((size_t)rb * 256 + head * 8) * 1024;
#pragma unroll
  for (int db = 0; db < 4; ++db)
#pragma unroll
    for (int s = 0; s < 2; ++s) { u32x4 w;
#pragma unroll
      for (int i = 0; i < 4; ++i) w[i] = pk2(o[db][8 * s + 2 * i] * inv, o[db][8 * s + 2 * i + 1] * inv);
      st16c(ob, lane16 + (2 * db + s) * 1024, w); }
  signal_done(ws, CNT_O + rt * 4 + (head >> 3), wid);
}

constexpr int P_LA = 4;
DI void ffn1_prefetch(const MainArgs& ar, int wid);
template <bool FINAL>
DI void phase_proj(const MainArgs& ar, LAS char* lds, const int wid) {
  char* const ws = ar.ws; const char* W = FINAL ? ar.wd : ar.wo; const unsigned short* xin = ar.x; unsigned short* out = ar.out;
  constexpr int NS16 = FINAL ? 288 : 256, NST = NS16 / 16;
  const char* act = ws + (FINAL ? WS_H : WS_O);
  const float* mod = (const float*)(ws + WS_MOD);
  char* part = ws + (FINAL ? WS_PD : WS_PB);
  const int lane = lane_id(), tid = wid * 64 + lane, rg = wid & 3, kh = wid >> 2;
  const int x = blockIdx.x & 7, j = blockIdx.x >> 3, ks = x >> 1, rb = 2 * (j >> 2) + (x & 1), ct = j & 3;
  const int own = ct * 4 + ks;
  const int g = lane >> 4, m16 = lane & 15, q4 = (lane & 15) >> 2, p = lane & 3;
  const int kb32 = (ks * 2 + kh) * NST;
  unsigned aoff[2];
#pragma unroll
  for (int a = 0; a < 2; ++a) { const int row = rb * 128 + rg * 32 + a * 16 + m16; aoff[a] = (unsigned)(row >> 5) * (NS16 * 1024) + (row & 31) * 16 + (g >> 1) * 1024 + (g & 1) * 512; }
  const char* actw = act + (size_t)kb32 * 2048;
  unsigned wsrc3[3];
#pragma unroll
  for (int e = 0; e < 3; ++e) { const int o = 64 * e + lane, row = o / 18, ch = o - row * 18; wsrc3[e] = row * 1152 + ch * 16; }
  const char* Ww = W + (size_t)(kb32 * 32 + rg * 8) * 1152 + ct * 288;
  const unsigned wdst = kh * (P_NSTG * P_STG) + rg * 2304;
  bf16x8 af[NST][2];
  auto issue_piece = [&](auto S, auto E) { constexpr int s = S, e = E;
    const char* wsrc = Ww + (size_t)s * 32 * 1152; const unsigned m = (s % P_NSTG) * P_STG + wdst;
    if constexpr (e == 0) dma16<0>(m, wsrc3[0], wsrc);
    if constexpr (e == 1) dma16<0>(m + 1024, wsrc3[1], wsrc);
    if constexpr (e == 2) { if (lane < 16) dma16<0>(m + 2048, wsrc3[2], wsrc); }
    if constexpr (e == 3) gload16c<0>(af[s][0], aoff[0], actw + s * 2048);
    if constexpr (e == 4) gload16c<0>(af[s][1], aoff[1], actw + s * 2048); };
  if (!FINAL && wid != 0) { ffn1_prefetch(ar, wid); if (wid == 1) ffn1_prefetch(ar, 0); }
  wait_count(ws, (FINAL ? CNT_H : CNT_O) + (rb >> 1) * 4 + ks, 8u, wid);
  static_for<3>([&](auto S) { issue_piece(S, integral_constant<int, 3>{}); issue_piece(S, integral_constant<int, 4>{}); });
  unsigned tb[P_NSTG];
#pragma unroll
  for (int i = 0; i < P_NSTG; ++i) tb[i] = kh * (P_NSTG * P_STG) + i * P_STG + 288 * (16 * (g >> 1) + 4 * (g & 1) + q4) + 8 * p;
  f32x4 acc[2][9] = {};
  constexpr int LA = P_LA, NIT = NST * 9;
  bf16x8 wf[LA + 1];
  auto rd = [&](auto I) { constexpr int i = I; constexpr int s = i / 9, c = i % 9;
    wf[i % (LA + 1)] = cat(tr4(lds + tb[s % P_NSTG] + 32 * c), tr4(lds + tb[s % P_NSTG] + 288 * 8 + 32 * c)); };
  constexpr int NTB = FINAL ? 3 : 2;
  constexpr int TB = FINAL ? 81920 : 137600;
  SB(); vm_bar<4>(); pin(af[0][0]); pin(af[0][1]);
#pragma unroll
  for (int e = 0; e < NTB; ++e) { const int n = NTB * wid + e;
    if (!FINAL) dma16<0>(TB + n * 1024, lane * 16, n < 7 ? (const char*)(mod + 1152) + n * 1024 : (const char*)xin + (size_t)(rb * 128 + own * 8) * 1152 + (n - 7) * 1024);
    else { const int n2 = n < 21 ? n : 0; dma16<0>(TB + n2 * 1024, lane * 16, n2 < 3 ? (const char*)(mod + 2880) + n2 * 1024 : ws + WS_X1R + (size_t)blockIdx.x * 576 * 32 + (n2 - 3) * 1024); } }
  SB();
  static_for<LA>([&](auto I) { rd(I); });
  SB();
  static_for<NIT>([&](auto I) { constexpr int i = I; constexpr int s = i / 9, c = i % 9;
    if constexpr (c == 0 && s > 0) {
      constexpr int outst = s == 1 ? 2 + NTB + 5 : ((s + 2 < NST) ? 5 : 0);
      vm_bar<outst>(); pin(af[s][0]); pin(af[s][1]);
      SB();
    }
    if constexpr (i + LA < NIT) rd(integral_constant<int, i + LA>{});
    acc[0][c] = mfma16(wf[i % (LA + 1)], af[s][0], acc[0][c]);
    acc[1][c] = mfma16(wf[i % (LA + 1)], af[s][1], acc[1][c]);
    if constexpr (s + 3 < NST && c >= 1 && c <= 5) issue_piece(integral_constant<int, s + 3>{}, integral_constant<int, c - 1>{});
    SB();
  });
  const int r8 = tid & 7, rown = rb * 128 + own * 8 + r8;
  int colA[2], colB[2];
  colA[0] = 144 * (tid >> 7) + 32 * ((tid >> 5) & 3) + 4 * ((tid >> 3) & 3); colB[0] = colA[0] + 16;
  colA[1] = 288 * ((tid >> 5) & 1) + 128 + 4 * ((tid >> 3) & 3); colB[1] = colA[1] + 144;
  unsigned marked;
  if (!FINAL) asm volatile("global_load_dword %0, %1, %2 sc1" : "=v"(marked) : "v"(0u), "s"(ws + WS_CNT + (size_t)(CNT_CLEAR + (rb >> 1)) * 256));
  {
    LAS char* exw = lds + (rg * 2 + kh) * 9216 + lane * 16; LAS char* exr = lds + (rg * 2 + (kh ^ 1)) * 9216 + lane * 16;
    bar();
    f32x4 fin[9];
    if (kh) {
#pragma unroll
      for (int c = 0; c < 9; ++c) { *(LAS f32x4*)(exw + c * 1024) = acc[0][c]; fin[c] = acc[1][c]; }
    } else {
#pragma unroll
      for (int c = 0; c < 9; ++c) { *(LAS f32x4*)(exw + c * 1024) = acc[1][c]; fin[c] = acc[0][c]; }
    }
    asm volatile("s_waitcnt lgkmcnt(0)\n\ts_barrier" ::: "memory");
    char* pw = part + (size_t)(rb * 16 + own) * 36864;
#pragma unroll
    for (int c = 0; c < 9; ++c) fin[c] += *(LAS f32x4*)(exr + c * 1024);
    const unsigned jo = (unsigned)(rg * 4 + kh * 2 + (m16 >> 3)) * 2304, lo = g * 8 + (m16 & 7);
    u32x4 cw[4];
#pragma unroll
    for (int cp = 0; cp < 4; ++cp) cw[cp] = (u32x4){pk2(fin[2 * cp][0], fin[2 * cp][1]), pk2(fin[2 * cp][2], fin[2 * cp][3]), pk2(fin[2 * cp + 1][0], fin[2 * cp + 1][1]), pk2(fin[2 * cp + 1][2], fin[2 * cp + 1][3])};
    const u32x2 w8 = {pk2(fin[8][0], fin[8][1]), pk2(fin[8][2], fin[8][3])};
#pragma unroll
    for (int cp = 0; cp < 4; ++cp) st16c(pw, jo + (cp * 32 + lo) * 16, cw[cp]);
    asm volatile("global_store_dwordx2 %0, %1, %2 sc1\n\ts_nop 1" ::"v"(jo + 2048 + lo * 8), "v"(w8), "s"(pw) : "memory");
  }
  const char* pr = part + (size_t)rb * 16 * 36864 + own * 2304;
  const bool two = wid == 0;
  u32x4 pv[4]; u32x2 pq[4][2];
  if (!FINAL) {
    asm volatile("s_waitcnt vmcnt(5)" : "+v"(marked) :: "memory");
    if (!__all(marked >= 32u)) {
      const unsigned* cnt = (const unsigned*)(ws + WS_CNT) + (CNT_CLEAR + (rb >> 1)) * 64;
      for (unsigned spin = 0; spin < (1u << 15); ++spin) { if (__all(__hip_atomic_load(cnt, __ATOMIC_RELAXED, __HIP_MEMORY_SCOPE_AGENT) >= 32u)) break; __builtin_amdgcn_s_sleep(1); }
    }
  }
  if (wid == 7) {
    for (unsigned spin = 0; spin < (1u << 15); ++spin) {
      unsigned t; asm volatile("global_load_dword %0, %1, %2 sc1\n\ts_waitcnt vmcnt(0)" : "=&v"(t) : "v"((unsigned)((lane & 15) * 36864 + 2300)), "s"(pr) : "memory");
      if (__all(t != ~0u)) break;
      __builtin_amdgcn_s_sleep(1);
    } }
  bar();
  for (unsigned spin = 0; spin < (1u << 15); ++spin) {
    const unsigned o0 = (unsigned)((tid >> 7) * 4 * 36864 + (tid & 127) * 16), o1 = (unsigned)(((tid >> 5) & 1) * 8 * 36864 + 2048 + (tid & 31) * 8);
#pragma unroll
    for (int k = 0; k < 4; ++k) gload16c<0>(pv[k], o0 + k * 36864, pr);
    if (two) {
#pragma unroll
      for (int k = 0; k < 4; ++k) {
        asm volatile("global_load_dwordx2 %0, %1, %2 sc1" : "=v"(pq[k][0]) : "v"(o1 + k * 36864), "s"(pr));
        asm volatile("global_load_dwordx2 %0, %1, %2 sc1" : "=v"(pq[k][1]) : "v"(o1 + (4 + k) * 36864), "s"(pr)); } }
    wait_vm<0>();
    bool full = true;
#pragma unroll
    for (int k = 0; k < 4; ++k) { pin(pv[k]); full &= pv[k][0] != ~0u && pv[k][1] != ~0u && pv[k][2] != ~0u && pv[k][3] != ~0u; }
    if (two) {
#pragma unroll
      for (int k = 0; k < 4; ++k) { pin(pq[k][0]); pin(pq[k][1]); full &= pq[k][0][0] != ~0u && pq[k][0][1] != ~0u && pq[k][1][0] != ~0u && pq[k][1][1] != ~0u; } }
    if (__all(full)) break;
    __builtin_amdgcn_s_sleep(4);
  }
  f32x4 v[2][2], sc[2][2], sh[2][2];
#pragma unroll
  for (int i = 0; i < 2; ++i) if (i == 0 || two) {
#pragma unroll
    for (int hh = 0; hh < 2; ++hh) { const int col = hh ? colB[i] : colA[i]; f32x4 rs;
      const f32x4 gt = *(LAS f32x4*)(lds + TB + col * 4);
      if (FINAL) rs = *(LAS f32x4*)(lds + TB + 3072 + (i ? 512 + (tid & 63) : tid) * 32 + hh * 16);
      else { const u32x2 xr = *(LAS u32x2*)(lds + TB + 7168 + r8 * 1152 + col * 2); rs = (f32x4){bflo(xr[0]), bfhi(xr[0]), bflo(xr[1]), bfhi(xr[1])};
        sh[i][hh] = *(LAS f32x4*)(lds + TB + (576 + col) * 4); sc[i][hh] = *(LAS f32x4*)(lds + TB + (1152 + col) * 4); }
      f32x4 t = {0.f, 0.f, 0.f, 0.f};
#pragma unroll
      for (int k = 0; k < 4; ++k) { const unsigned w0 = i ? pq[k][hh][0] : pv[k][2 * hh], w1 = i ? pq[k][hh][1] : pv[k][2 * hh + 1]; t += (f32x4){bflo(w0), bfhi(w0), bflo(w1), bfhi(w1)}; }
      v[i][hh] = rs + gt * t; }
  }
  if (FINAL) {
#pragma unroll
    for (int i = 0; i < 2; ++i) if (i == 0 || two) {
      *(u32x2*)(out + (size_t)rown * D_ + colA[i]) = (u32x2){pk2(v[i][0][0], v[i][0][1]), pk2(v[i][0][2], v[i][0][3])};
      *(u32x2*)(out + (size_t)rown * D_ + colB[i]) = (u32x2){pk2(v[i][1][0], v[i][1][1]), pk2(v[i][1][2], v[i][1][3])}; }
  } else {
    LAS float* st = (LAS float*)(lds + P_T);
    auto s8 = [](const f32x4& a, const f32x4& b) { return ((a[0] + a[1]) + (a[2] + a[3])) + ((b[0] + b[1]) + (b[2] + b[3])); };
    float s = s8(v[0][0], v[0][1]) + (two ? s8(v[1][0], v[1][1]) : 0.f);
    s = sum_x8_16_32(s);
    if (lane < 8) st[wid * 8 + lane] = s;
    asm volatile("s_waitcnt lgkmcnt(0)\n\ts_barrier" ::: "memory");
    float tot = 0.f;
#pragma unroll
    for (int w = 0; w < 8; ++w) tot += st[w * 8 + r8];
    const float mean = tot * (1.f / 576.f);
    f32x4 d[2][2];
#pragma unroll
    for (int i = 0; i < 2; ++i) { d[i][0] = v[i][0] - mean; d[i][1] = v[i][1] - mean; }
    float qq = s8(d[0][0] * d[0][0], d[0][1] * d[0][1]) + (two ? s8(d[1][0] * d[1][0], d[1][1] * d[1][1]) : 0.f);
    qq = sum_x8_16_32(qq);
    if (lane < 8) st[64 + wid * 8 + lane] = qq;
    asm volatile("s_waitcnt lgkmcnt(0)\n\ts_barrier" ::: "memory");
    float tq = 0.f;
#pragma unroll
    for (int w = 0; w < 8; ++w) tq += st[64 + w * 8 + r8];
    const float rstd = __builtin_amdgcn_rsqf(tq * (1.f / 576.f) + 1e-5f);
#pragma unroll
    for (int i = 0; i < 2; ++i) if (i == 0 || two) {
      const f32x4 h0 = d[i][0] * rstd * (sc[i][0] + 1.f) + sh[i][0], h1 = d[i][1] * rstd * (sc[i][1] + 1.f) + sh[i][1];
      const int kc = i ? 64 + (tid >> 3) : (tid >> 3);
      st16c(ws + WS_H2, (unsigned)(((rown >> 5) * 36 + (kc >> 1)) * 1024 + ((kc & 1) * 32 + (rown & 31)) * 16), (u32x4){pk2(h0[0], h0[1]), pk2(h0[2], h0[3]), pk2(h1[0], h1[1]), pk2(h1[2], h1[3])});
      char* xb = ws + WS_X1R + (size_t)blockIdx.x * 576 * 32 + (i ? 512 + (tid & 63) : tid) * 32;
      *(f32x4*)xb = v[i][0]; *(f32x4*)(xb + 16) = v[i][1]; }
  }
}

constexpr int C_STG = 9216 + 128 + 9216, C_UOFF = 9216 + 128, C_NSTG = 4;
DI unsigned geglu2(f32x2 g, f32x2 u) {
  const float c0 = -2.f * 0.7978845608028654f * 1.4426950408889634f, c1 = c0 * 0.044715f;
  const f32x2 z = g * (g * g * c1 + c0);
  const f32x2 d = (f32x2){__builtin_amdgcn_exp2f(z[0]), __builtin_amdgcn_exp2f(z[1])} + 1.f;
  const f32x2 o = (g * u) * (f32x2){__builtin_amdgcn_rcpf(d[0]), __builtin_amdgcn_rcpf(d[1])};
  return pk2(o[0], o[1]);
}
constexpr int C_LA = 6;
DI void ffn1_prefetch(const MainArgs& ar, int wid) {
  const int lane = lane_id();
  const int x = blockIdx.x & 7, j = blockIdx.x >> 3, ct = 4 * x + (j & 3);
  unsigned wsrc3[3];
#pragma unroll
  for (int e = 0; e < 3; ++e) { const int o = 64 * e + lane, row = o / 18, ch = o - row * 18; wsrc3[e] = (16 * (row >> 2) + (row & 3)) * (FF * 2) + ch * 16; }
  const char* Ww = (wid < 4 ? ar.wg : ar.wu) + (size_t)(wid & 3) * 4 * (FF * 2) + ct * 288;
  const unsigned wdst = C_BASE + (wid < 4 ? 0 : C_UOFF) + (wid & 3) * 2304;
#pragma unroll
  for (int s = 0; s < 3; ++s) { const char* wsrc = Ww + (size_t)(32 * s) * (FF * 2); const unsigned m = s * C_STG + wdst;
    dma16<0>(m, wsrc3[0], wsrc); dma16<0>(m + 1024, wsrc3[1], wsrc); if (lane < 16) dma16<0>(m + 2048, wsrc3[2], wsrc); }
}
DI void phase_ffn1(const MainArgs& ar, LAS char* lds, const int wid) {
  char* const ws = ar.ws; const char* wg = ar.wg; const char* wu = ar.wu;
  const int lane = lane_id(), tid = wid * 64 + lane;
  const int x = blockIdx.x & 7, j = blockIdx.x >> 3, ct = 4 * x + (j & 3), rt = j >> 2;
  const int rb = rt * 8 + wid;
  const int h = lane >> 5, blk = (lane >> 4) & 1, q = (lane & 15) >> 2, p = lane & 3;
  unsigned wsrc3[3], wsrc3b[3];
#pragma unroll
  for (int e = 0; e < 3; ++e) { const int o = 64 * e + lane, row = o / 18, ch = o - row * 18;
    wsrc3[e] = (16 * (row >> 2) + (row & 3)) * (FF * 2) + ch * 16; wsrc3b[e] = (144 * (row >> 2) + (row & 3)) * (FF * 2) + ch * 16; }
  const char* Ww = (wid < 4 ? wg : wu) + (size_t)(wid & 3) * 4 * (FF * 2) + ct * 288;
  const unsigned wdst = C_BASE + (wid < 4 ? 0 : C_UOFF) + (wid & 3) * 2304;
  const char* hw = ws + WS_H2 + (size_t)rb * 36 * 1024;
  const unsigned lane16 = lane * 16;
  bf16x8 af[18][2];
  auto issue_piece = [&](auto S, auto E) { constexpr int s = S, e = E;
    constexpr int kbase = s < 16 ? 144 * (s >> 2) + 32 * (s & 3) : 288 * (s - 16) + 128;
    const char* wsrc = Ww + (size_t)kbase * (FF * 2); const unsigned m = (s % C_NSTG) * C_STG + wdst;
    if constexpr (e == 0) dma16<0>(m, s < 16 ? wsrc3[0] : wsrc3b[0], wsrc);
    if constexpr (e == 1) dma16<0>(m + 1024, s < 16 ? wsrc3[1] : wsrc3b[1], wsrc);
    if constexpr (e == 2) { if (lane < 16) dma16<0>(m + 2048, s < 16 ? wsrc3[2] : wsrc3b[2], wsrc); }
    if constexpr (e == 3) gload16c<0>(af[s][0], lane16, hw + s * 2048);
    if constexpr (e == 4) gload16c<1024>(af[s][1], lane16, hw + s * 2048); };
  static_for<3>([&](auto S) { issue_piece(S, integral_constant<int, 3>{}); issue_piece(S, integral_constant<int, 4>{}); });
  unsigned tb[C_NSTG];
#pragma unroll
  for (int i = 0; i < C_NSTG; ++i) { tb[i] = C_BASE + blk * C_UOFF + 288 * (8 * h + q) + 8 * p + i * C_STG; pin(tb[i]); }
  f32x16 acc[9] = {};
  constexpr int LA = C_LA, NIT = 18 * 18;
  bf16x8 wf[LA + 1];
  auto rd = [&](auto I) { constexpr int i = I; constexpr int s = i / 18, ks = (i % 18) / 9, f = i % 9;
    const bf16x4 lo = tr4(lds + tb[s % C_NSTG] + 288 * 16 * ks + 32 * f), hi = tr4(lds + tb[s % C_NSTG] + 288 * (16 * ks + 4) + 32 * f);
    wf[i % (LA + 1)] = cat(lo, hi); };
  SB(); vm_bar<4>(); pin(af[0][0]); pin(af[0][1]); revalidate<1024>(af[0][0], af[0][1], lane16, lane16, hw); SB();
  static_for<LA>([&](auto I) { rd(I); });
  SB();
  static_for<NIT>([&](auto I) { constexpr int i = I; constexpr int s = i / 18, r = i % 18, ks = r / 9, f = r % 9;
    if constexpr (r == 0 && s > 0) {
      constexpr int outst = (s + 2 < 18) ? 5 : 0;
      vm_bar<outst>(); pin(af[s][0]); pin(af[s][1]);
      revalidate<1024>(af[s][0], af[s][1], lane16, lane16, hw + s * 2048);
      if constexpr (s == 17) proj_prefetch(ar.wd, 18, wid);
      SB();
    }
    if constexpr (i + LA < NIT) rd(integral_constant<int, i + LA>{});
    acc[f] = mfma32(wf[i % (LA + 1)], af[s][ks], acc[f]);
    if constexpr (s + 3 < 18 && (r % 3) == 1 && r / 3 < 5) issue_piece(integral_constant<int, s + 3>{}, integral_constant<int, r / 3>{});
    SB();
  });
  char* ob = ws + WS_H + ((size_t)rb * 288 + ct * 9) * 1024;
#pragma unroll
  for (int f = 0; f < 9; ++f) { u32x4 w;
#pragma unroll
    for (int i = 0; i < 4; ++i) w[i] = geglu2((f32x2){acc[f][2 * i], acc[f][2 * i + 1]}, (f32x2){acc[f][8 + 2 * i], acc[f][8 + 2 * i + 1]});
    st16c(ob, lane16 + f * 1024, w); }
  signal_done(ws, CNT_H + rt * 4 + (ct >> 3), wid);
}

__global__ __launch_bounds__(512, 2) void k_main(MainArgs ar) {
  extern __shared__ __attribute__((aligned(16))) char lds_[];
  LAS char* lds = (LAS char*)lds_;
  const int wid = rfl(threadIdx.x >> 6);
  if (wid == 7) {
    unsigned long long pc; unsigned len;
    asm volatile("s_getpc_b64 %0\n.Lkm_beg:\n\ts_mov_b32 %1, .Lkm_fin-.Lkm_beg" : "=s"(pc), "=s"(len));
    const unsigned per = (len / 128 + 31) / 32, l0 = (blockIdx.x >> 3) * per + lane_id();
    unsigned off = (lane_id() < (int)per && l0 * 128 < len) ? l0 * 128 : 0;
    dma4(A_TAB + 2048 + 7 * 256, off, (const void*)pc);
  }
  phase_attn(ar, lds, wid);
  phase_proj<false>(ar, lds, wid);
  phase_ffn1(ar, lds, wid);
  phase_proj<true>(ar, lds, wid);
  asm volatile(".Lkm_fin:");
}

extern "C" void kernel_launch(void* const* d_in, const int* in_sizes, int n_in, void* d_out, int out_size, void* d_ws, size_t ws_size, hipStream_t stream) {
  const unsigned short* x = (const unsigned short*)d_in[0];
  const unsigned short* c = (const unsigned short*)d_in[9]; const unsigned short* wmod = (const unsigned short*)d_in[10];
  char* ws = (char*)d_ws;
  (void)hipFuncSetAttribute((const void*)k_pre, hipFuncAttributeMaxDynamicSharedMemorySize, PRE_LDS);
  (void)hipFuncSetAttribute((const void*)k_main, hipFuncAttributeMaxDynamicSharedMemorySize, MAIN_LDS);
  const char* kc = (const char*)d_in[1]; const char* vc = (const char*)d_in[2];
  const char* wq = (const char*)d_in[3]; const char* wo = (const char*)d_in[4];
  const char* wg = (const char*)d_in[6]; const char* wu = (const char*)d_in[7]; const char* wd = (const char*)d_in[8];
  k_pre<<<PRE_RQ + PRE_MOD + PRE_CLR, 512, PRE_LDS, stream>>>(c, wmod, x, wq, ws);
  MainArgs ar{kc, vc, wg, wu, wo, wd, x, (unsigned short*)d_out, ws};
  k_main<<<256, 512, MAIN_LDS, stream>>>(ar);
}
```
